```python
import torch

def _softmax(x, axis=-1):
    return torch.softmax(x, dim=axis)
def _gelu(x):
    return torch.nn.functional.gelu(x, approximate="tanh")
def _silu(x):
    return torch.nn.functional.silu(x)
def _sigmoid(x):
    return torch.sigmoid(x)
def _layernorm(x, axis=-1, eps=1e-5):
    m = x.mean(dim=axis, keepdim=True)
    v = x.var(dim=axis, keepdim=True, unbiased=False)
    return (x - m) / torch.sqrt(v + eps)
def _scatter_add(tgt, idx, src, axis=0):
    return tgt.clone().index_add_(axis, idx, src)
def _segment_sum(src, seg, n):
    out = torch.zeros((n,) + src.shape[1:], dtype=src.dtype)
    return out.index_add_(0, seg, src)
def _pad(x, pw):
    flat = tuple(v for pair in reversed(pw) for v in pair)
    return torch.nn.functional.pad(x, flat)

kernel_name = 'interleave_layers__sliding_block__glu_gated_geglu__prompt'

def setup_inputs():

    _w = (lambda a: (lambda t: t.to(torch.bfloat16) if t.is_floating_point() else t)(torch.as_tensor(a)))
    def _A_setup():
        B, S, D = 1, 2048, 640
        return {
            "x": _w((lambda *s: torch.randn(*s, dtype=torch.bfloat16) * 0.05)(B, S, D)),
            "wqkv": _w((lambda *s: torch.randn(*s, dtype=torch.bfloat16) * 0.05)(D, 3 * D)),
            "wo": _w((lambda *s: torch.randn(*s, dtype=torch.bfloat16) * 0.05)(D, D)),
        }
    _A_in = _A_setup()

    def _B_setup():
        B, S, Dm, Df = 1, 2048, 640, 1728
        return {
            "x": _w((lambda *s: torch.randn(*s, dtype=torch.bfloat16) * 0.05)(B, S, Dm)),
            "w_gate": _w((lambda *s: torch.randn(*s, dtype=torch.bfloat16) * 0.8)(Dm, Df) * (Dm ** -0.5)),
            "w_up": _w((lambda *s: torch.randn(*s, dtype=torch.bfloat16) * 0.8)(Dm, Df) * (Dm ** -0.5)),
            "w_down": _w((lambda *s: torch.randn(*s, dtype=torch.bfloat16) * 0.8)(Df, Dm) * (Df ** -0.5)),
        }
    _B_in = _B_setup()

    return {
        "A_x": _A_in["x"],
        "A_wqkv": _A_in["wqkv"],
        "A_wo": _A_in["wo"],
        "B_x": _B_in["x"],
        "B_w_gate": _B_in["w_gate"],
        "B_w_up": _B_in["w_up"],
        "B_w_down": _B_in["w_down"],
    }

def reference(A_x, A_wqkv, A_wo, B_x, B_w_gate, B_w_up, B_w_down):
    def _A_ref(x, wqkv, wo):
        B, S, D = x.shape
        H = 32; Dh = D // H; W = 256
        qkv = (lambda x, *d: x.permute(*d))((x @ wqkv).reshape(B, S, 3, H, Dh), 2, 0, 3, 1, 4)
        q, k, v = qkv[0], qkv[1], qkv[2]
        scores = torch.einsum("bhqd,bhkd->bhqk", q, k) * (Dh ** -0.5)
        idx = torch.arange(S)
        mask = (idx[:, None] - idx[None, :] >= 0) & (idx[:, None] - idx[None, :] < W)
        scores = torch.where(mask[None, None], scores, -1e9)
        p = _softmax(scores, axis=-1)
        o = torch.einsum("bhqk,bhkd->bhqd", p, v)
        return (lambda x, *d: x.permute(*d))(o, 0, 2, 1, 3).reshape(B, S, D) @ wo

    def _B_ref(x, w_gate, w_up, w_down):
        g = torch.einsum("bsd,df->bsf", x, w_gate)
        u = torch.einsum("bsd,df->bsf", x, w_up)
        h = _gelu(g) * u
        return torch.einsum("bsf,fd->bsd", h, w_down)

    x = A_x
    x = x + _A_ref(_layernorm(x, axis=-1), A_wqkv, A_wo)
    x = x + _B_ref(_layernorm(x, axis=-1), B_w_gate, B_w_up, B_w_down)
    x = x + _A_ref(_layernorm(x, axis=-1), A_wqkv, A_wo)
    x = x + _B_ref(_layernorm(x, axis=-1), B_w_gate, B_w_up, B_w_down)
    return x
```

```cpp
#include <hip/hip_runtime.h>
#include <hip/hip_bf16.h>
#include <stdint.h>

typedef unsigned short u16;
__device__ __forceinline__ float bf2f(u16 b) { return __uint_as_float(((unsigned)b) << 16); }
__device__ __forceinline__ u16 f2bf(float f) { unsigned u = __float_as_uint(f); u += 0x7FFFu + ((u >> 16) & 1u); return (u16)(u >> 16); }

constexpr int S = 2048, D = 640, H = 32, DH = 20, W = 256, DF = 1728;

__global__ void k_cvt(const u16* x, float* y, int n) { int i = blockIdx.x * 256 + threadIdx.x; if (i < n) y[i] = bf2f(x[i]); }
__global__ void k_out(const float* x, u16* y, int n) { int i = blockIdx.x * 256 + threadIdx.x; if (i < n) y[i] = f2bf(x[i]); }

__global__ void k_ln(const float* x, u16* y) {
  int row = blockIdx.x; int t = threadIdx.x;
  float v[10]; float s = 0;
  for (int i = 0; i < 10; ++i) { v[i] = x[row * D + t + 64 * i]; s += v[i]; }
  for (int m = 32; m >= 1; m >>= 1) s += __shfl_xor(s, m);
  float mean = s / D; float q = 0;
  for (int i = 0; i < 10; ++i) { float d = v[i] - mean; q += d * d; }
  for (int m = 32; m >= 1; m >>= 1) q += __shfl_xor(q, m);
  float rstd = 1.0f / sqrtf(q / D + 1e-5f);
  for (int i = 0; i < 10; ++i) y[row * D + t + 64 * i] = f2bf((v[i] - mean) * rstd);
}
template <int MODE>
__global__ void k_gemm(const u16* A, const u16* B, u16* Cb, float* Cf, int M, int N, int K) {
  __shared__ float As[16][17], Bs[16][17];
  int tx = threadIdx.x & 15, ty = threadIdx.x >> 4;
  int row = blockIdx.y * 16 + ty, col = blockIdx.x * 16 + tx;
  float acc = 0;
  for (int k0 = 0; k0 < K; k0 += 16) {
    As[ty][tx] = bf2f(A[row * K + k0 + tx]);
    Bs[ty][tx] = bf2f(B[(k0 + ty) * N + col]);
    __syncthreads();
    for (int k = 0; k < 16; ++k) acc += As[ty][k] * Bs[k][tx];
    __syncthreads();
  }
  if (MODE == 0) Cb[row * N + col] = f2bf(acc);
  else Cf[row * N + col] += acc;
}
__global__ void k_attn(const u16* qkv, u16* o) {
  int q = blockIdx.x, h = blockIdx.y, t = threadIdx.x;
  __shared__ float p[256]; __shared__ float red[4];
  int key = q - t;
  float s = -1e30f;
  if (key >= 0) { s = 0; for (int d = 0; d < DH; ++d) s += bf2f(qkv[q * 3 * D + h * DH + d]) * bf2f(qkv[key * 3 * D + D + h * DH + d]); s *= 0.22360679775f; }
  float m = s; for (int k = 32; k >= 1; k >>= 1) m = fmaxf(m, __shfl_xor(m, k));
  if ((t & 63) == 0) red[t >> 6] = m; __syncthreads();
  m = fmaxf(fmaxf(red[0], red[1]), fmaxf(red[2], red[3])); __syncthreads();
  float e = key >= 0 ? __expf(s - m) : 0.f; p[t] = e;
  float l = e; for (int k = 32; k >= 1; k >>= 1) l += __shfl_xor(l, k);
  if ((t & 63) == 0) red[t >> 6] = l; __syncthreads();
  l = red[0] + red[1] + red[2] + red[3];
  if (t < DH) { float a = 0; for (int j = 0; j < 256; ++j) { int kk = q - j; if (kk >= 0) a += p[j] * bf2f(qkv[kk * 3 * D + 2 * D + h * DH + t]); } o[q * D + h * DH + t] = f2bf(a / l); }
}
__global__ void k_act(const u16* g, const u16* u, u16* hh, int n) {
  int i = blockIdx.x * 256 + threadIdx.x; if (i >= n) return;
  float x = bf2f(g[i]); float t = tanhf(0.7978845608f * (x + 0.044715f * x * x * x));
  hh[i] = f2bf(0.5f * x * (1.f + t) * bf2f(u[i]));
}

extern "C" void kernel_launch(void* const* d_in, const int* in_sizes, int n_in, void* d_out, int out_size, void* d_ws, size_t ws_size, hipStream_t stream) {
  const u16* x = (const u16*)d_in[0]; const u16* wqkv = (const u16*)d_in[1]; const u16* wo = (const u16*)d_in[2];
  const u16* wg = (const u16*)d_in[4]; const u16* wu = (const u16*)d_in[5]; const u16* wd = (const u16*)d_in[6];
  char* ws = (char*)d_ws;
  float* xf = (float*)ws; ws += S * D * 4;
  u16* xn = (u16*)ws; ws += S * D * 2;
  u16* qkv = (u16*)ws; ws += S * 3 * D * 2;
  u16* o = (u16*)ws; ws += S * D * 2;
  u16* g = (u16*)ws; ws += S * DF * 2;
  u16* u = (u16*)ws; ws += S * DF * 2;
  u16* hh = (u16*)ws; ws += S * DF * 2;
  k_cvt<<<S * D / 256, 256, 0, stream>>>(x, xf, S * D);
  for (int l = 0; l < 2; ++l) {
    k_ln<<<S, 64, 0, stream>>>(xf, xn);
    k_gemm<0><<<dim3(3 * D / 16, S / 16), 256, 0, stream>>>(xn, wqkv, qkv, nullptr, S, 3 * D, D);
    k_attn<<<dim3(S, H), 256, 0, stream>>>(qkv, o);
    k_gemm<1><<<dim3(D / 16, S / 16), 256, 0, stream>>>(o, wo, nullptr, xf, S, D, D);
    k_ln<<<S, 64, 0, stream>>>(xf, xn);
    k_gemm<0><<<dim3(DF / 16, S / 16), 256, 0, stream>>>(xn, wg, g, nullptr, S, DF, D);
    k_gemm<0><<<dim3(DF / 16, S / 16), 256, 0, stream>>>(xn, wu, u, nullptr, S, DF, D);
    k_act<<<S * DF / 256, 256, 0, stream>>>(g, u, hh, S * DF);
    k_gemm<1><<<dim3(D / 16, S / 16), 256, 0, stream>>>(hh, wd, nullptr, xf, S, D, DF);
  }
  k_out<<<S * D / 256, 256, 0, stream>>>(xf, (u16*)d_out, S * D);
}
```

```cpp
#include <hip/hip_runtime.h>
#include <stdint.h>
#include <utility>

typedef unsigned short u16;
typedef unsigned u32;
typedef unsigned v4u __attribute__((ext_vector_type(4)));
typedef unsigned v2u __attribute__((ext_vector_type(2)));
typedef __bf16 bh8 __attribute__((ext_vector_type(8)));
typedef __bf16 bh2 __attribute__((ext_vector_type(2)));
typedef float f2v __attribute__((ext_vector_type(2)));
typedef float f4v __attribute__((ext_vector_type(4)));
typedef float f16v __attribute__((ext_vector_type(16)));
#define LAS __attribute__((address_space(3)))
#define DI __device__ __forceinline__
typedef __amdgpu_buffer_rsrc_t rsrc_t;

constexpr u32 UNIT = 10240;
constexpr u32 O_W1 = 0;
constexpr u32 O_W3 = O_W1 + 16 * 15 * UNIT;
constexpr u32 O_W4 = O_W3 + 8 * 10 * UNIT;
constexpr u32 O_W5 = O_W4 + 108 * 4 * UNIT;
constexpr u32 O_CS = O_W5 + 8 * 27 * UNIT;
constexpr u32 X_SZ = 128 * 80 * 256;
constexpr u32 O_X = O_CS + 4 * 5376 * 4;
constexpr u32 ST_SZ = 128 * 8 * 128;
constexpr u32 O_ST = O_X + 4 * X_SZ;
constexpr u32 KV_SZ = 64 * 16 * 5120;
constexpr u32 O_KV = O_ST + 4 * ST_SZ;
constexpr u32 OM_SZ = 128 * 64 * 256;
constexpr u32 O_OM = O_KV + 2 * KV_SZ;
constexpr u32 OE_SZ = 128 * 32 * 128;
constexpr u32 O_OE = O_OM + 2 * OM_SZ;
constexpr u32 H_SZ = 128 * 216 * 256;
constexpr u32 O_H = O_OE + 2 * OE_SZ;
constexpr u32 O_FL = O_H + 2 * H_SZ;
constexpr u32 FL_LINES = 5 * 2 * 64;
constexpr u32 FL_SZ = FL_LINES * 128;
constexpr u32 O_DBG = O_FL + FL_SZ;
constexpr u32 WS_USED = O_DBG + 256 * 8 * 64 * 8;
DI u32 fl_off(int kind, int layer, int grb) { return O_FL + ((kind * 2 + layer) * 64 + grb) * 128; }

constexpr u32 L_MISC = 15 * UNIT;
constexpr u32 L_QX = L_MISC;
constexpr u32 L_STAT = L_QX + 5120;
constexpr u32 L_CS1 = L_STAT + 1024;
constexpr u32 L_CS4 = L_CS1 + 512;
constexpr u32 LDS_BYTES = 163840;
static_assert(L_CS4 + 896 <= LDS_BYTES, "lds");
constexpr u32 KVB = 5696;

template <int B, class F, int... I> DI void sfor_(F&& f, std::integer_sequence<int, I...>) { (f(std::integral_constant<int, B + I>{}), ...); }
template <int B, int E, class F> DI void sfor(F&& f) { sfor_<B>(f, std::make_integer_sequence<int, E - B>{}); }
#define LAM(i) [&](auto i##_) __attribute__((always_inline))
#define CI(i) constexpr int i = decltype(i##_)::value
DI float bf_lo(u32 x) { return __uint_as_float(x << 16); }
DI float bf_hi(u32 x) { return __uint_as_float(x & 0xffff0000u); }
DI u32 pk2(float a, float b) { f2v v = {a, b}; bh2 r = __builtin_convertvector(v, bh2); return __builtin_bit_cast(u32, r); }
DI v4u lds_r128(u32 a) { return *(const LAS v4u*)(uintptr_t)a; }
DI v2u lds_r64(u32 a) { return *(const LAS v2u*)(uintptr_t)a; }
DI f4v lds_rf4(u32 a) { return *(const LAS f4v*)(uintptr_t)a; }
DI float lds_rf(u32 a) { return *(const LAS float*)(uintptr_t)a; }
DI void lds_w128(u32 a, v4u v) { *(LAS v4u*)(uintptr_t)a = v; }
DI void lds_wf4(u32 a, f4v v) { *(LAS f4v*)(uintptr_t)a = v; }
DI void lds_w64(u32 a, v2u v) { *(LAS v2u*)(uintptr_t)a = v; }
DI void lds_wf(u32 a, float v) { *(LAS float*)(uintptr_t)a = v; }
DI void lds_w16(u32 a, u32 v) { *(LAS u16*)(uintptr_t)a = (u16)v; }
DI f16v mfma32(v4u a, v4u b, f16v c) { return __builtin_amdgcn_mfma_f32_32x32x16_bf16(__builtin_bit_cast(bh8, a), __builtin_bit_cast(bh8, b), c, 0, 0, 0); }
DI f4v mfma16(v4u a, v4u b, f4v c) { return __builtin_amdgcn_mfma_f32_16x16x32_bf16(__builtin_bit_cast(bh8, a), __builtin_bit_cast(bh8, b), c, 0, 0, 0); }
#define SB() __builtin_amdgcn_sched_barrier(0)
#define PIN(x) asm volatile("" : "+v"(x))
DI void mm32(f16v& c, v4u a, v4u b) { c = mfma32(a, b, c); PIN(c); }
DI void mm16(f4v& c, v4u a, v4u b) { c = mfma16(a, b, c); PIN(c); }
#define BAR() do { SB(); asm volatile("s_barrier" ::: "memory"); SB(); } while (0)
#define BAR_LGKM() do { SB(); asm volatile("s_waitcnt lgkmcnt(0)\n\ts_barrier" ::: "memory"); SB(); } while (0)
#define WAIT_VM(n) asm volatile("s_waitcnt vmcnt(" #n ")" ::: "memory")
#define LBAR(n) asm volatile("s_waitcnt vmcnt(" #n ")\n\ts_barrier" ::: "memory")
DI v4u ld128(rsrc_t r, u32 vo, u32 so) { return __builtin_amdgcn_raw_buffer_load_b128(r, vo, so, 16); }
DI v2u ld64(rsrc_t r, u32 vo, u32 so) { return __builtin_amdgcn_raw_buffer_load_b64(r, vo, so, 16); }
DI void st128(rsrc_t r, v4u v, u32 vo, u32 so) { __builtin_amdgcn_raw_buffer_store_b128(v, r, vo, so, 16); }
DI void st64(rsrc_t r, v2u v, u32 vo, u32 so) { __builtin_amdgcn_raw_buffer_store_b64(v, r, vo, so, 16); }
DI float sum32(float v) { auto r = __builtin_amdgcn_permlane32_swap(__float_as_uint(v), __float_as_uint(v), false, false); return __uint_as_float(r[0]) + __uint_as_float(r[1]); }
DI float sum16(float v) { auto r = __builtin_amdgcn_permlane16_swap(__float_as_uint(v), __float_as_uint(v), false, false); return __uint_as_float(r[0]) + __uint_as_float(r[1]); }
DI float max32(float v) { auto r = __builtin_amdgcn_permlane32_swap(__float_as_uint(v), __float_as_uint(v), false, false); return fmaxf(__uint_as_float(r[0]), __uint_as_float(r[1])); }
DI u32 opaque(u32 v) { asm volatile("" : "+v"(v)); return v; }

#define STAMP(i) do {} while (0)

struct Cx {
  rsrc_t rs;
  rsrc_t ro;
  int g, j, c, rh, w, layer;
  u32 lane;
  u32 dead;
};

DI void opq(Cx& cx) { asm volatile("" : "+s"(cx.g), "+s"(cx.j), "+s"(cx.c), "+s"(cx.rh), "+s"(cx.w), "+s"(cx.layer), "+v"(cx.lane)); }
DI void poll(Cx& cx, u32 fo, u32 nf, u32 val) {
  const u32 vo = (cx.lane < nf ? cx.lane : 0) * 4;
  u32 it = 0;
  while (true) {
    u32 f = __builtin_amdgcn_raw_buffer_load_b32(cx.rs, vo, fo, 16);
    asm volatile("" ::: "memory");
    if (__builtin_amdgcn_ballot_w64(f < val) == 0ull || cx.dead) break;
    if (++it > 200000u) { cx.dead = 1; break; }
    __builtin_amdgcn_s_sleep(1);
  }
  asm volatile("" ::: "memory");
}
DI void set_flag(Cx& cx, u32 fo, u32 val) {
  __builtin_amdgcn_raw_buffer_store_b32(opaque(val), cx.rs, cx.lane << 25, fo, 16);
}

DI void ld_unit(Cx& cx, u32 lvo, u32 src, u32 ldsw, int slot) {
  u32 keep;
  asm volatile(
      "s_mov_b32 %0, m0\n\ts_mov_b32 m0, %2\n\ts_nop 0\n\t"
      "buffer_load_dwordx4 %1, %3, %4 offen lds\n\t"
      "buffer_load_dwordx4 %1, %3, %4 offen offset:1024 lds\n\t"
      "s_mov_b32 exec_hi, 0\n\t"
      "buffer_load_dwordx4 %1, %3, %4 offen offset:2048 lds\n\t"
      "s_mov_b32 exec_hi, -1\n\t"
      "s_mov_b32 m0, %0"
      : "=&s"(keep) : "v"(lvo), "s"(ldsw + (u32)slot * UNIT), "s"(cx.rs), "s"(src) : "memory");
}

DI constexpr int slotA(int q) { return q == 0 ? 0 : q == 1 ? 7 : q == 2 ? 2 : 9; }
DI constexpr int slotV(int q) { return q == 0 ? 1 : q == 1 ? 8 : q == 2 ? 6 : 12; }
DI constexpr int slotQ(int q) { return q == 0 ? 13 : q == 1 ? 14 : q == 2 ? 10 : 11; }
DI constexpr int slot3(int u) { return u == 0 ? 7 : u == 1 ? 8 : u == 2 ? 9 : u == 3 ? 12 : u == 4 ? 11 : u == 5 ? 13 : u == 6 ? 14 : u == 7 ? 10 : u == 8 ? 0 : 1; }
DI constexpr int cyc(int v) { int p = v % 15; return p < 5 ? p + 2 : p == 5 ? 9 : p < 10 ? p + 5 : p == 10 ? 10 : p == 11 ? 0 : p == 12 ? 1 : p == 13 ? 7 : 8; }
DI constexpr int unit4(int t, int q) { return t < 2 ? 2 * q + t : 4 * t + q; }

struct LB { u32 b[3]; };
DI LB mk_lb(u32 base) { LB l; l.b[0] = base; l.b[1] = base + 6 * UNIT; l.b[2] = base + 12 * UNIT;
  asm volatile("" : "+v"(l.b[0]), "+v"(l.b[1]), "+v"(l.b[2])); return l; }
DI v4u rd_slot(const LB& l, int slot, int off) { return lds_r128(l.b[slot / 6] + (u32)((slot % 6) * UNIT + off)); }

DI int tile_of(int j, int t) {
  int kh = j >> 3, jj = j & 7;
  if (t < 6) return 2 * (27 * kh + (t >> 1) * 8 + jj) + (t & 1);
  if (jj >= 6) jj = 0;
  return 2 * (27 * kh + 24 + (jj >> 1)) + (jj & 1);
}

DI void stage_kv(u32 lane, int blk, v4u k0, v4u k1, v4u kx, v4u v0, v4u v1, u32 bias) {
  const u32 base = (u32)blk * KVB, n = lane & 31, h = lane >> 5;
  lds_w128(base + lane * 16, k0);
  lds_w128(base + 1024 + lane * 16, k1);
  lds_w128(base + 2048 + lane * 16, (v4u){kx.x, kx.y, bias, 0u});
  const u32 vb = base + 3072;
  lds_w128(vb + h * 656 + (4 + n) * 16, v0);
  lds_w128(vb + (2 + h) * 656 + (4 + n) * 16, v1);
  const u32 ea = vb + ((n >> 4) * 2 + ((n >> 2) & 1)) * 656 + (h ? 0 : 36 * 16) + ((n & 3) + 4 * ((n >> 3) & 1)) * 2;
  lds_w16(ea, kx.z); lds_w16(ea + 16, kx.z >> 16); lds_w16(ea + 32, kx.w); lds_w16(ea + 48, kx.w >> 16);
  if (lane < 16) lds_wf(vb + (lane >> 2) * 656 + 640 + (lane & 3) * 4, __uint_as_float(opaque(0x3f803f80u)));
}

DI void load_x(Cx& cx, int xi, v4u (&xf)[40], float& mean, float& var) {
  const u32 n = cx.lane & 31, h = cx.lane >> 5;
  const u32 tb0 = 8 * cx.g + 2 * cx.w;
  v2u sp[4];
  {
    const u32 vo = (n >> 4) * 1024 + h * 512 + (n & 15) * 8, so = O_ST + xi * ST_SZ + tb0 * 1024;
#pragma unroll
    for (int i = 0; i < 4; ++i) sp[i] = ld64(cx.rs, vo + i * 128, so);
  }
  const u32 vo = (n >> 4) * 20480 + h * 256 + (n & 15) * 16, so = O_X + xi * X_SZ + tb0 * 20480;
#pragma unroll
  for (int s = 0; s < 40; ++s) xf[s] = ld128(cx.rs, vo + (s & 7) * 512, so + (s >> 3) * 4096);
  SB();
  float s1 = 0, s2 = 0;
#pragma unroll
  for (int i = 0; i < 4; ++i) { s1 += __uint_as_float(sp[i].x); s2 += __uint_as_float(sp[i].y); }
  s1 = sum32(s1); s2 = sum32(s2);
  mean = s1 * (1.0f / 640.0f);
  var = fmaxf(s2 * (1.0f / 640.0f) - mean * mean, 0.f) + 1e-5f;
}

DI void p1_c(Cx& cx, v4u& q0, v4u& q1) {
  const u32 lane = cx.lane, n = lane & 31, h = lane >> 5;
  if (cx.layer) poll(cx, fl_off(4, 0, cx.g * 4 + cx.w), 16, 1);
  STAMP(1);
  v4u xf[40]; float mean, var;
  load_x(cx, cx.layer * 2, xf, mean, var);
  const float rstd = __builtin_amdgcn_rsqf(var);
  const u32 stb = L_STAT + cx.w * 256;
  if (h == 0) { lds_wf(stb + n * 4, mean); lds_wf(stb + 128 + n * 4, rstd); }
  const LB lb = mk_lb(lane * 16);
  u32 xb = 3 * UNIT + (h * 24 + (n < 24 ? n : n < 28 ? n - 4 : n - 12)) * 16;
  asm volatile("" : "+v"(xb));
  BAR_LGKM();
  STAMP(2);
  constexpr int D = 4;
  f16v aA = {0}, aV = {0}, aX = {0};
  {
    v4u wf[120];
    auto rd = [&](int i) __attribute__((always_inline)) { int s = i / 3, t = i % 3;
      return t == 0 ? rd_slot(lb, slotA(s / 10), (s % 10) * 1024) : t == 1 ? rd_slot(lb, slotV(s / 10), (s % 10) * 1024) : lds_r128(xb + s * 768); };
#pragma unroll
    for (int i = 0; i < D; ++i) wf[i] = rd(i);
    sfor<0, 120>(LAM(i) { CI(i);
      if constexpr (i + D < 120) wf[i + D] = rd(i + D);
      constexpr int s = i / 3, t = i % 3;
      if constexpr (t == 0) mm32(aA, wf[i], xf[s]);
      else if constexpr (t == 1) mm32(aV, xf[s], wf[i]);
      else mm32(aX, wf[i], xf[s]);
      SB();
      if constexpr (i == 59) BAR();
    });
  }
  BAR();
  STAMP(3);
  v4u wq[40];
#pragma unroll
  for (int i = 0; i < D; ++i) wq[i] = rd_slot(lb, slotQ(0), i * 1024);
  const u32 csb = opaque(L_CS1 + h * 16);
  v4u k0, k1, kx, v0, v1; v2u qx;
  const float qs = rstd * (0.22360679774997896f * 1.4426950408889634f);
  {
    float t[16];
#pragma unroll
    for (int r4 = 0; r4 < 4; ++r4) { f4v cs = lds_rf4(csb + r4 * 32);
#pragma unroll
      for (int i = 0; i < 4; ++i) t[r4 * 4 + i] = (aA[r4 * 4 + i] - mean * cs[i]) * rstd; }
    k0 = (v4u){pk2(t[0], t[1]), pk2(t[2], t[3]), pk2(t[4], t[5]), pk2(t[6], t[7])};
    k1 = (v4u){pk2(t[8], t[9]), pk2(t[10], t[11]), pk2(t[12], t[13]), pk2(t[14], t[15])};
#pragma unroll
    for (int r4 = 0; r4 < 3; ++r4) { f4v cs = lds_rf4(csb + 256 + r4 * 32);
#pragma unroll
      for (int i = 0; i < 4; ++i) t[r4 * 4 + i] = (aX[r4 * 4 + i] - mean * cs[i]) * (r4 == 2 ? qs : rstd); }
    kx = (v4u){pk2(t[0], t[1]), pk2(t[2], t[3]), pk2(t[4], t[5]), pk2(t[6], t[7])};
    qx = (v2u){pk2(t[8], t[9]), pk2(t[10], t[11])};
    const float csv = lds_rf(L_CS1 + 128 + n * 4);
#pragma unroll
    for (int r4 = 0; r4 < 4; ++r4) { f4v mm = lds_rf4(stb + h * 16 + r4 * 32), rr = lds_rf4(stb + 128 + h * 16 + r4 * 32);
#pragma unroll
      for (int i = 0; i < 4; ++i) t[r4 * 4 + i] = (aV[r4 * 4 + i] - mm[i] * csv) * rr[i]; }
    v0 = (v4u){pk2(t[0], t[1]), pk2(t[2], t[3]), pk2(t[4], t[5]), pk2(t[6], t[7])};
    v1 = (v4u){pk2(t[8], t[9]), pk2(t[10], t[11]), pk2(t[12], t[13]), pk2(t[14], t[15])};
  }
  {
    const u32 so = O_KV + cx.layer * KV_SZ + ((4 * cx.g + cx.w) * 16 + cx.j) * 5120;
    st128(cx.rs, k0, lane * 16, so); st128(cx.rs, k1, lane * 16 + 1024, so); st128(cx.rs, kx, lane * 16 + 2048, so);
    st128(cx.rs, v0, lane * 16 + 3072, so); st128(cx.rs, v1, lane * 16, so + 4096);
  }
  SB();
  stage_kv(lane, 8 + cx.w, k0, k1, kx, v0, v1, 0u);
  SB();
  f16v aQ = {0};
#pragma unroll
  for (int i = 0; i < 40; ++i) {
    if (i + D < 40) wq[i + D] = rd_slot(lb, slotQ((i + D) / 10), ((i + D) % 10) * 1024);
    mm32(aQ, wq[i], xf[i]);
    SB();
    if (i == 31) { WAIT_VM(0); set_flag(cx, fl_off(0, cx.layer, cx.g * 4 + cx.w) + cx.j * 4, 1); SB(); }
  }
  {
    float t[16];
#pragma unroll
    for (int r4 = 0; r4 < 4; ++r4) { f4v cs = lds_rf4(csb + 384 + r4 * 32);
#pragma unroll
      for (int i = 0; i < 4; ++i) t[r4 * 4 + i] = (aQ[r4 * 4 + i] - mean * cs[i]) * qs; }
    q0 = (v4u){pk2(t[0], t[1]), pk2(t[2], t[3]), pk2(t[4], t[5]), pk2(t[6], t[7])};
    lds_w128(L_QX + cx.w * 1280 + lane * 16, (v4u){pk2(t[8], t[9]), pk2(t[10], t[11]), pk2(t[12], t[13]), pk2(t[14], t[15])});
    if (h) lds_w64(L_QX + cx.w * 1280 + 1024 + n * 8, qx);
    q1 = h ? (v4u){0u, 0u, 0u, 0u} : (v4u){qx.x, qx.y, 0x3f80u, 0u};
  }
  STAMP(4);
  BAR_LGKM();
}

DI void p1_l(Cx& cx, v4u& q0, v4u& q1) {
  const u32 ldsw = (cx.w & 3) * 2560, lvo = ldsw + cx.lane * 16;
  const u32 lane = cx.lane, n = lane & 31, h = lane >> 5;
  const int w = cx.w & 3;
  const u32 w1 = O_W1 + cx.j * 15 * UNIT, w3 = O_W3 + cx.c * 10 * UNIT;
  asm volatile("s_waitcnt vmcnt(12) lgkmcnt(0)\n\ts_barrier" ::: "memory");
  ld_unit(cx, lvo, w1 + 13 * UNIT, ldsw, slotQ(2)); ld_unit(cx, lvo, w1 + 14 * UNIT, ldsw, slotQ(3));
  LBAR(12);
  LBAR(0);
#pragma unroll
  for (int u = 0; u < 4; ++u) ld_unit(cx, lvo, w3 + u * UNIT, ldsw, slot3(u));
  {
    v4u d[2][5];
#pragma unroll
    for (int e = 0; e < 2; ++e) {
      const int gg = cx.g - 2 + e;
      if (gg >= 0) {
        poll(cx, fl_off(0, cx.layer, gg * 4 + w) + cx.j * 4, 1, 1);
        const u32 so = O_KV + cx.layer * KV_SZ + ((4 * gg + w) * 16 + cx.j) * 5120;
#pragma unroll
        for (int p = 0; p < 4; ++p) d[e][p] = ld128(cx.rs, lane * 16 + p * 1024, so);
        d[e][4] = ld128(cx.rs, lane * 16, so + 4096);
      } else {
#pragma unroll
        for (int p = 0; p < 5; ++p) d[e][p] = (v4u){0u, 0u, 0u, 0u};
      }
    }
#pragma unroll
    for (int e = 0; e < 2; ++e) stage_kv(lane, 4 * e + w, d[e][0], d[e][1], d[e][2], d[e][3], d[e][4], cx.g - 2 + e >= 0 ? 0u : 0xf149u);
  }
  STAMP(4);
  BAR_LGKM();
#pragma unroll
  for (int u = 4; u < 8; ++u) ld_unit(cx, lvo, w3 + u * UNIT, ldsw, slot3(u));
  q0 = lds_r128(L_QX + w * 1280 + lane * 16);
  v2u qx = lds_r64(L_QX + w * 1280 + 1024 + n * 8);
  q1 = h ? (v4u){qx.x, qx.y, 0x3f80u, 0u} : (v4u){0u, 0u, 0u, 0u};
}

DI void attn(Cx& cx, v4u q0, v4u q1) {
  const u32 lane = cx.lane, n = lane & 31, h = lane >> 5;
  const int rb = cx.w & 3, hd = cx.w >> 2;
  STAMP(5);
  const u32 kb = rb * KVB + lane * 16;
  const u32 vr = n < 16 ? 4 + 16 * hd + n : n < 20 ? (hd ? 0 : 36) + (n - 16) : n < 28 ? 40 : (hd ? 0 : 36) + (n - 28);
  const u32 vb = rb * KVB + 3072 + h * 656 + vr * 16;
  f16v S[9];
#pragma unroll
  for (int i = 0; i < 9; ++i) {
    const int ii = i == 0 ? 0 : i == 1 ? 8 : i - 1;
    v4u a0 = lds_r128(kb + hd * 1024 + ii * KVB), a1 = lds_r128(kb + 2048 + ii * KVB);
    f16v z = {0};
    S[ii] = mfma32(a1, q1, mfma32(a0, q0, z));
  }
  const int z = (int)n - 4 * (int)h;
#pragma unroll
  for (int r = 0; r < 16; ++r) S[0][r] = ((r & 3) + 8 * (r >> 2) <= z) ? S[8][r] : S[0][r];
  float m = S[0][0];
#pragma unroll
  for (int i = 0; i < 8; ++i)
#pragma unroll
    for (int r = 0; r < 16; ++r) m = fmaxf(m, S[i][r]);
  m = max32(m);
  f16v O = {0};
#pragma unroll
  for (int i = 1; i < 9; ++i) {
    const int ii = i & 7;
    float p[16];
#pragma unroll
    for (int r = 0; r < 16; ++r) p[r] = __builtin_amdgcn_exp2f(S[ii][r] - m);
    if (ii) {
      v4u b0 = {pk2(p[0], p[1]), pk2(p[2], p[3]), pk2(p[4], p[5]), pk2(p[6], p[7])};
      v4u b1 = {pk2(p[8], p[9]), pk2(p[10], p[11]), pk2(p[12], p[13]), pk2(p[14], p[15])};
      O = mfma32(lds_r128(vb + ii * KVB), b0, O);
      O = mfma32(lds_r128(vb + ii * KVB + 1312), b1, O);
    } else {
      float pd[16], pf[16];
#pragma unroll
      for (int r = 0; r < 16; ++r) { bool cd = (r & 3) + 8 * (r >> 2) <= z; pd[r] = cd ? p[r] : 0.f; pf[r] = cd ? 0.f : p[r]; }
      v4u b0 = {pk2(pf[0], pf[1]), pk2(pf[2], pf[3]), pk2(pf[4], pf[5]), pk2(pf[6], pf[7])};
      v4u b1 = {pk2(pf[8], pf[9]), pk2(pf[10], pf[11]), pk2(pf[12], pf[13]), pk2(pf[14], pf[15])};
      O = mfma32(lds_r128(vb), b0, O);
      O = mfma32(lds_r128(vb + 1312), b1, O);
      v4u c0 = {pk2(pd[0], pd[1]), pk2(pd[2], pd[3]), pk2(pd[4], pd[5]), pk2(pd[6], pd[7])};
      v4u c1 = {pk2(pd[8], pd[9]), pk2(pd[10], pd[11]), pk2(pd[12], pd[13]), pk2(pd[14], pd[15])};
      O = mfma32(lds_r128(vb + 8 * KVB), c0, O);
      O = mfma32(lds_r128(vb + 8 * KVB + 1312), c1, O);
    }
  }
  const float inv = __builtin_amdgcn_rcpf(h ? O[8] : O[12]);
  const u32 tb0 = 8 * cx.g + 2 * rb, head = 2 * cx.j + hd;
  v4u om = {pk2(O[0] * inv, O[1] * inv), pk2(O[2] * inv, O[3] * inv), pk2(O[4] * inv, O[5] * inv), pk2(O[6] * inv, O[7] * inv)};
  st128(cx.rs, om, (n >> 4) * 16384 + h * 256 + (n & 15) * 16, O_OM + cx.layer * OM_SZ + tb0 * 16384 + head * 512);
  if (h == 0) st64(cx.rs, (v2u){pk2(O[8] * inv, O[9] * inv), pk2(O[10] * inv, O[11] * inv)}, (n >> 4) * 4096 + (n & 15) * 8,
                   O_OE + cx.layer * OE_SZ + tb0 * 4096 + head * 128);
  STAMP(6);
  WAIT_VM(0);
  set_flag(cx, fl_off(1, cx.layer, cx.g * 4 + rb) + head * 4, 1);
  BAR();
  STAMP(7);
}

DI void tail35(Cx& cx, f4v (&acc)[2][5], v4u r0, v4u r1, v2u r2, int s0, int s1, int xo, int kind, bool final_) {
  const u32 lane = cx.lane, n = lane & 15, q = lane >> 4;
  const int tb = cx.w >> 1, kh = cx.w & 1;
  const u32 sb = (tb ? s1 : s0) * UNIT + lane * 16;
#pragma unroll
  for (int t = 0; t < 5; ++t) lds_wf4(sb + kh * 5120 + t * 1024, acc[1][t]);
  BAR_LGKM();
  float v[20];
#pragma unroll
  for (int t = 0; t < 5; ++t) { f4v o = lds_rf4(sb + (kh ^ 1) * 5120 + t * 1024);
#pragma unroll
    for (int i = 0; i < 4; ++i) v[t * 4 + i] = acc[0][t][i] + o[i]; }
  const u32 rr[10] = {r0.x, r0.y, r0.z, r0.w, r1.x, r1.y, r1.z, r1.w, r2.x, r2.y};
  u32 o[10];
#pragma unroll
  for (int i = 0; i < 10; ++i) o[i] = pk2(v[2 * i] + bf_lo(rr[i]), v[2 * i + 1] + bf_hi(rr[i]));
  const u32 tbf = 8 * cx.g + 4 * cx.rh + 2 * tb + kh;
  if (final_) {
    const u32 so = tbf * 16 * 1280 + cx.c * 160, vo = n * 1280 + q * 16;
    __builtin_amdgcn_raw_buffer_store_b128((v4u){o[0], o[1], o[2], o[3]}, cx.ro, vo, so, 0);
    __builtin_amdgcn_raw_buffer_store_b128((v4u){o[4], o[5], o[6], o[7]}, cx.ro, vo + 64, so, 0);
    __builtin_amdgcn_raw_buffer_store_b64((v2u){o[8], o[9]}, cx.ro, n * 1280 + q * 8 + 128, so, 0);
  } else {
    const u32 so = O_X + xo * X_SZ + tbf * 20480 + cx.c * 2560;
    st128(cx.rs, (v4u){o[0], o[1], o[2], o[3]}, lane * 16, so);
    st128(cx.rs, (v4u){o[4], o[5], o[6], o[7]}, lane * 16 + 1024, so);
    st64(cx.rs, (v2u){o[8], o[9]}, 2048 + (q >> 1) * 256 + n * 16 + (q & 1) * 8, so);
    float s1v = 0, s2v = 0;
#pragma unroll
    for (int i = 0; i < 10; ++i) { float a = bf_lo(o[i]), b = bf_hi(o[i]); s1v += a + b; s2v += a * a + b * b; }
    s1v = sum32(sum16(s1v)); s2v = sum32(sum16(s2v));
    if (q == 0) st64(cx.rs, (v2u){__float_as_uint(s1v), __float_as_uint(s2v)}, n * 8, O_ST + xo * ST_SZ + tbf * 1024 + cx.c * 128);
    WAIT_VM(0);
    set_flag(cx, fl_off(kind, cx.layer, cx.g * 4 + 2 * cx.rh + tb) + (cx.c * 2 + kh) * 4, 1);
  }
}
DI void load_res(Cx& cx, int xi, v4u& r0, v4u& r1, v2u& r2) {
  const u32 lane = cx.lane, n = lane & 15, q = lane >> 4;
  const u32 tbf = 8 * cx.g + 4 * cx.rh + 2 * (cx.w >> 1) + (cx.w & 1);
  const u32 so = O_X + xi * X_SZ + tbf * 20480 + cx.c * 2560;
  r0 = ld128(cx.rs, lane * 16, so); r1 = ld128(cx.rs, lane * 16 + 1024, so);
  r2 = ld64(cx.rs, 2048 + (q >> 1) * 256 + n * 16 + (q & 1) * 8, so);
}

DI void p3_c(Cx& cx) {
  const u32 lane = cx.lane, n = lane & 15, q = lane >> 4;
  const int tb = cx.w >> 1, kh = cx.w & 1;
  v4u r0, r1; v2u r2;
  load_res(cx, cx.layer * 2, r0, r1, r2);
  poll(cx, fl_off(1, cx.layer, cx.g * 4 + 2 * cx.rh + tb) + kh * 64, 16, 1);
  STAMP(8);
  v4u af[10][2];
  {
    const u32 t0 = 8 * cx.g + 4 * cx.rh + 2 * tb;
#pragma unroll
    for (int u = 0; u < 8; ++u)
#pragma unroll
      for (int e = 0; e < 2; ++e)
        af[u][e] = ld128(cx.rs, lane * 16 + (u & 3) * 1024, O_OM + cx.layer * OM_SZ + (t0 + (e ^ kh)) * 16384 + kh * 8192 + (u >> 2) * 4096);
#pragma unroll
    for (int u = 8; u < 10; ++u)
#pragma unroll
      for (int e = 0; e < 2; ++e) {
        const u32 so = O_OE + cx.layer * OE_SZ + (t0 + (e ^ kh)) * 4096 + kh * 2048, vo = q * 256 + n * 8 + (u - 8) * 1024;
        v2u a = ld64(cx.rs, vo, so), b = ld64(cx.rs, vo + 128, so);
        af[u][e] = (v4u){a.x, a.y, b.x, b.y};
      }
  }
  SB();
  const LB lb = mk_lb(lane * 16 + kh * 5120);
  f4v acc[2][5];
#pragma unroll
  for (int t = 0; t < 5; ++t) { acc[0][t] = (f4v){0, 0, 0, 0}; acc[1][t] = (f4v){0, 0, 0, 0}; }
  constexpr int D = 4;
  v4u wf[50];
#pragma unroll
  for (int i = 0; i < D; ++i) wf[i] = rd_slot(lb, slot3(0), i * 1024);
#pragma unroll
  for (int i = 0; i < 50; ++i) {
    if (i + D < 50) wf[i + D] = rd_slot(lb, slot3((i + D) / 5), ((i + D) % 5) * 1024);
    mm16(acc[0][i % 5], wf[i], af[i / 5][0]);
    mm16(acc[1][i % 5], wf[i], af[i / 5][1]);
    SB();
    if (i == 34) BAR();
  }
  STAMP(9);
  tail35(cx, acc, r0, r1, r2, 7, 8, cx.layer * 2 + 1, 2, false);
  STAMP(10);
}

struct Epi { float t[4], g[4]; f4v cs; u32 o[4]; };
#define E4(stmt) do { _Pragma("unroll") for (int i = 0; i < 4; ++i) { stmt; } } while (0)
#define PIN4(x) asm volatile("" : "+v"(x[0]), "+v"(x[1]), "+v"(x[2]), "+v"(x[3]))
template <int k, int t, bool pin>
DI void epi4(Cx& cx, const f16v& a, Epi& e, u32 csb, float nmean, float c1, float c2, float kk, u32 hvo) {
  constexpr int hf = k / 14, s = k % 14, b = hf * 4;
  if constexpr (s == 0) e.cs = lds_rf4(csb + t * 128 + hf * 32);
  else if constexpr (s == 2) E4(e.g[i] = __builtin_fmaf(nmean, e.cs[i], a[b + i]));
  else if constexpr (s == 3) { e.cs = lds_rf4(csb + t * 128 + 64 + hf * 32); E4(e.t[i] = e.g[i] * e.g[i]); }
  else if constexpr (s == 4) E4(e.t[i] = __builtin_fmaf(e.t[i], c2, c1));
  else if constexpr (s == 5) E4(e.t[i] = e.t[i] * e.g[i]);
  else if constexpr (s == 6 || s == 7) { e.t[2 * s - 12] = __builtin_amdgcn_exp2f(e.t[2 * s - 12]); e.t[2 * s - 11] = __builtin_amdgcn_exp2f(e.t[2 * s - 11]); }
  else if constexpr (s == 8) E4(e.t[i] = __builtin_fmaf(e.t[i], kk, kk));
  else if constexpr (s == 9 || s == 10) { e.t[2 * s - 18] = __builtin_amdgcn_rcpf(e.t[2 * s - 18]); e.t[2 * s - 17] = __builtin_amdgcn_rcpf(e.t[2 * s - 17]); }
  else if constexpr (s == 11) E4(e.g[i] = e.g[i] * e.t[i]);
  else if constexpr (s == 12) E4(e.t[i] = __builtin_fmaf(nmean, e.cs[i], a[8 + b + i]));
  else if constexpr (s == 13) E4(e.g[i] = e.g[i] * e.t[i]);
  if constexpr (pin) {
    if constexpr (s == 2 || s == 11 || s == 13) PIN4(e.g);
    else if constexpr (s >= 3) PIN4(e.t);
  }
  if constexpr (k == 15) { e.o[0] = pk2(e.g[0], e.g[1]); e.o[1] = pk2(e.g[2], e.g[3]); if constexpr (pin) { PIN(e.o[0]); PIN(e.o[1]); } }
  if constexpr (k == 27) {
    e.o[2] = pk2(e.g[0], e.g[1]); e.o[3] = pk2(e.g[2], e.g[3]);
    if constexpr (t > 0) { WAIT_VM(0); set_flag(cx, fl_off(3, cx.layer, cx.g * 4 + cx.w) + cx.j * 4, t); }
    if (t < 6 || (cx.j & 7) < 6)
      st128(cx.rs, (v4u){e.o[0], e.o[1], e.o[2], e.o[3]}, hvo, O_H + cx.layer * H_SZ + (8 * cx.g + 2 * cx.w) * 55296 + tile_of(cx.j, t) * 512);
  }
}
DI void p4_c(Cx& cx) {
  const u32 lane = cx.lane, n = lane & 31, h = lane >> 5;
  poll(cx, fl_off(2, cx.layer, cx.g * 4 + cx.w), 16, 1);
  STAMP(11);
  v4u xf[40]; float mean, var;
  load_x(cx, cx.layer * 2 + 1, xf, mean, var);
  const float rstd = __builtin_amdgcn_rsqf(var);
  const float nmean = -mean, kk = var;
  const float c1 = rstd * (-2.0f * 0.7978845608028654f * 1.4426950408889634f);
  const float c2 = c1 * rstd * rstd * 0.044715f;
  const LB lb = mk_lb(lane * 16);
  const u32 csb = opaque(L_CS4 + h * 16), hvo = (n >> 4) * 55296 + h * 256 + (n & 15) * 16;
  BAR_LGKM();
  STAMP(12);
  constexpr int D = 3;
  f16v acc[7];
#pragma unroll
  for (int t = 0; t < 7; ++t) acc[t] = (f16v){0};
  v4u wf[280];
  Epi e;
  auto rd = [&](int i) __attribute__((always_inline)) { int t = i < 80 ? (i & 1) : i / 40, s = i < 80 ? (i >> 1) : i % 40; return rd_slot(lb, cyc(unit4(t, s / 10)), (s % 10) * 1024); };
#pragma unroll
  for (int i = 0; i < D; ++i) wf[i] = rd(i);
  sfor<0, 280>(LAM(i) { CI(i);
    if constexpr (i + D < 280) wf[i + D] = rd(i + D);
    constexpr int t = i < 80 ? (i & 1) : i / 40, s = i < 80 ? (i >> 1) : i % 40;
    mm32(acc[t], wf[i], xf[s]);
    constexpr int gm = i - 80;
    if constexpr (gm >= 1 && gm < 113) epi4<(gm - 1) % 28, (gm - 1) / 28, true>(cx, acc[(gm - 1) / 28], e, csb, nmean, c1, c2, kk, hvo);
    else if constexpr (gm >= 121 && gm < 149) epi4<gm - 121, 4, true>(cx, acc[4], e, csb, nmean, c1, c2, kk, hvo);
    else if constexpr (gm >= 161 && gm < 189) epi4<gm - 161, 5, true>(cx, acc[5], e, csb, nmean, c1, c2, kk, hvo);
    SB();
    if constexpr (i % 40 == 39) BAR();
  });
  STAMP(13);
  sfor<0, 28>(LAM(k) { CI(k); epi4<k, 6, false>(cx, acc[6], e, csb, nmean, c1, c2, kk, hvo); });
}

DI void p5_c(Cx& cx) {
  const u32 lane = cx.lane;
  const int tb = cx.w >> 1, kh = cx.w & 1;
  const u32 fo = fl_off(3, cx.layer, cx.g * 4 + 2 * cx.rh + tb) + kh * 32;
  const u32 t0 = 8 * cx.g + 4 * cx.rh + 2 * tb;
  const u32 so0 = O_H + cx.layer * H_SZ + (t0 + kh) * 55296 + kh * 27648, so1 = O_H + cx.layer * H_SZ + (t0 + (kh ^ 1)) * 55296 + kh * 27648;
  v4u af[27][2];
  auto lda = [&](int u) __attribute__((always_inline)) { af[u][0] = ld128(cx.rs, lane * 16 + (u & 3) * 1024, so0 + (u >> 2) * 4096);
                          af[u][1] = ld128(cx.rs, lane * 16 + (u & 3) * 1024, so1 + (u >> 2) * 4096); };
  poll(cx, fo, 8, 4);
  set_flag(cx, fl_off(3, cx.layer, cx.g * 4 + cx.w) + cx.j * 4, 7);
  STAMP(14);
#pragma unroll
  for (int u = 0; u < 16; ++u) lda(u);
  v4u r0, r1; v2u r2;
  SB();
  const LB lb = mk_lb(lane * 16 + kh * 5120);
  f4v acc[2][5];
#pragma unroll
  for (int t = 0; t < 5; ++t) { acc[0][t] = (f4v){0, 0, 0, 0}; acc[1][t] = (f4v){0, 0, 0, 0}; }
  constexpr int D = 4;
  v4u wf[135];
#pragma unroll
  for (int i = 0; i < D; ++i) wf[i] = rd_slot(lb, cyc(28), i * 1024);
  sfor<0, 135>(LAM(i) { CI(i);
    if constexpr (i + D < 135) wf[i + D] = rd_slot(lb, cyc(28 + (i + D) / 5), ((i + D) % 5) * 1024);
    mm16(acc[0][i % 5], wf[i], af[i / 5][0]);
    mm16(acc[1][i % 5], wf[i], af[i / 5][1]);
    SB();
    if constexpr (i % 15 == 14 && i < 134) BAR();
    if constexpr (i == 29) {
      poll(cx, fo, 8, 7);
#pragma unroll
      for (int u = 16; u < 27; ++u) lda(u);
      load_res(cx, cx.layer * 2 + 1, r0, r1, r2);
      SB();
    }
  });
  STAMP(15);
  tail35(cx, acc, r0, r1, r2, 10, 11, 2, 4, cx.layer == 1);
  STAMP(16);
}

DI void p345_l(Cx& cx) {
  const u32 ldsw = (cx.w & 3) * 2560, lvo = ldsw + cx.lane * 16;
  const u32 w1 = O_W1 + cx.j * 15 * UNIT, w3 = O_W3 + cx.c * 10 * UNIT, w5 = O_W5 + cx.c * 27 * UNIT;
  auto lv = [&](int v) __attribute__((always_inline)) {
    if (v < 28) { const int t = v < 8 ? (v & 1) : v >> 2, q = v < 8 ? (v >> 1) : (v & 3);
      ld_unit(cx, lvo, O_W4 + (tile_of(cx.j, t) * 4 + q) * UNIT, ldsw, cyc(v)); }
    else ld_unit(cx, lvo, w5 + (v - 28) * UNIT, ldsw, cyc(v)); };
  ld_unit(cx, lvo, w3 + 8 * UNIT, ldsw, 0); ld_unit(cx, lvo, w3 + 9 * UNIT, ldsw, 1);
#pragma unroll
  for (int v = 0; v < 5; ++v) lv(v);
  LBAR(15);
#pragma unroll
  for (int v = 5; v < 10; ++v) lv(v);
  LBAR(63);
#pragma unroll
  for (int v = 10; v < 13; ++v) lv(v);
  LBAR(12);
  lv(13); lv(14);
  LBAR(63);
#pragma unroll
  for (int v = 15; v < 19; ++v) lv(v);
#pragma unroll
  for (int b = 0; b < 5; ++b) {
    LBAR(18);
#pragma unroll
    for (int v = 19 + 4 * b; v < 23 + 4 * b; ++v) lv(v);
  }
  LBAR(21);
#pragma unroll
  for (int v = 39; v < 43; ++v) lv(v);
#pragma unroll
  for (int b = 0; b < 4; ++b) {
    LBAR(24);
#pragma unroll
    for (int v = 43 + 3 * b; v < 46 + 3 * b; ++v) lv(v);
  }
  LBAR(24);
  if (cx.layer == 0) {
    ld_unit(cx, lvo, w1 + 0 * UNIT, ldsw, slotA(0)); ld_unit(cx, lvo, w1 + 4 * UNIT, ldsw, slotV(0));
    LBAR(21);
    ld_unit(cx, lvo, w1 + 1 * UNIT, ldsw, slotA(1)); ld_unit(cx, lvo, w1 + 5 * UNIT, ldsw, slotV(1)); ld_unit(cx, lvo, w1 + 2 * UNIT, ldsw, slotA(2));
    LBAR(21);
    ld_unit(cx, lvo, w1 + 8 * UNIT, ldsw, 3); ld_unit(cx, lvo, w1 + 9 * UNIT, ldsw, 4); ld_unit(cx, lvo, w1 + 10 * UNIT, ldsw, 5);
    LBAR(24);
    ld_unit(cx, lvo, w1 + 6 * UNIT, ldsw, slotV(2)); ld_unit(cx, lvo, w1 + 3 * UNIT, ldsw, slotA(3));
    LBAR(63);
    ld_unit(cx, lvo, w1 + 7 * UNIT, ldsw, slotV(3)); ld_unit(cx, lvo, w1 + 11 * UNIT, ldsw, slotQ(0)); ld_unit(cx, lvo, w1 + 12 * UNIT, ldsw, slotQ(1));
  } else {
    LBAR(15); LBAR(6); LBAR(0); LBAR(0);
  }
}

__global__ void __launch_bounds__(512, 2) k_main(char* ws, char* out) {
  Cx cx;
  cx.rs = __builtin_amdgcn_make_buffer_rsrc(ws, 0, WS_USED, 0x00020000);
  cx.ro = __builtin_amdgcn_make_buffer_rsrc(out, 0, 2048 * 640 * 2, 0x00020000);
  const int b = blockIdx.x, xcd = b & 7, bi = b >> 3;
  cx.g = (xcd >> 1) * 4 + (bi >> 3); cx.j = (xcd & 1) * 8 + (bi & 7);
  cx.c = (cx.j >> 3) * 4 + (cx.j & 3); cx.rh = (cx.j >> 2) & 1;
  cx.w = __builtin_amdgcn_readfirstlane(threadIdx.x >> 6);
  cx.lane = threadIdx.x & 63; cx.dead = 0; cx.layer = 0;
  STAMP(0);
  const bool isC = cx.w < 4;
  const u32 ldsw = (cx.w & 3) * 2560, lvo = ldsw + cx.lane * 16;
  if (!isC) {
    const u32 w1 = O_W1 + cx.j * 15 * UNIT;
    ld_unit(cx, lvo, w1 + 0 * UNIT, ldsw, slotA(0)); ld_unit(cx, lvo, w1 + 4 * UNIT, ldsw, slotV(0));
    ld_unit(cx, lvo, w1 + 1 * UNIT, ldsw, slotA(1)); ld_unit(cx, lvo, w1 + 5 * UNIT, ldsw, slotV(1)); ld_unit(cx, lvo, w1 + 2 * UNIT, ldsw, slotA(2));
    ld_unit(cx, lvo, w1 + 8 * UNIT, ldsw, 3); ld_unit(cx, lvo, w1 + 9 * UNIT, ldsw, 4); ld_unit(cx, lvo, w1 + 10 * UNIT, ldsw, 5);
    ld_unit(cx, lvo, w1 + 6 * UNIT, ldsw, slotV(2)); ld_unit(cx, lvo, w1 + 3 * UNIT, ldsw, slotA(3));
    ld_unit(cx, lvo, w1 + 7 * UNIT, ldsw, slotV(3)); ld_unit(cx, lvo, w1 + 11 * UNIT, ldsw, slotQ(0)); ld_unit(cx, lvo, w1 + 12 * UNIT, ldsw, slotQ(1));
  } else {
#pragma unroll
    for (int rep = 0; rep < 2; ++rep) {
      const int e = threadIdx.x + rep * 256;
      if (e < 352) {
        const int t = e >> 5, m = e & 31;
        int col;
        if (t == 2) { const int mm = m < 24 ? m : 0, sec = mm < 8 ? 1 : mm < 16 ? 2 : 0; col = sec * 640 + (2 * cx.j + ((mm >> 2) & 1)) * 20 + 16 + (mm & 3); }
        else if (t < 4) col = (t == 0 ? 640 : t == 1 ? 1280 : 0) + (2 * cx.j + (m >> 4)) * 20 + (m & 15);
        else col = 1920 + (m >> 4) * 1728 + tile_of(cx.j, t - 4) * 16 + (m & 15);
        float s = 0;
#pragma unroll
        for (int k = 0; k < 4; ++k) s += __uint_as_float(__builtin_amdgcn_raw_buffer_load_b32(cx.rs, col * 4, O_CS + k * 5376 * 4, 0));
        lds_wf(L_CS1 + e * 4, s);
      }
    }
  }
#pragma unroll 1
  for (int layer = 0; layer < 2; ++layer) {
    cx.layer = layer;
    v4u q0, q1;
    opq(cx);
    if (isC) p1_c(cx, q0, q1); else p1_l(cx, q0, q1);
    opq(cx);
    attn(cx, q0, q1);
    opq(cx);
    if (isC) { p3_c(cx); opq(cx); p4_c(cx); opq(cx); p5_c(cx); } else p345_l(cx);
  }
}

__global__ void __launch_bounds__(256) k_prep(const u16* x, const u16* wqkv, const u16* wo, const u16* wg, const u16* wu, const u16* wd, char* ws) {
  __shared__ __attribute__((aligned(16))) u16 tile[192 * 64];
  __shared__ float red[256];
  const int tid = threadIdx.x;
  int b = blockIdx.x;
  if (b < 128) {
    const int row = tid & 15, gs = tid >> 4;
    const v4u* src = (const v4u*)(x + (size_t)(b * 16 + row) * 640) + gs * 5;
    v4u v[5];
#pragma unroll
    for (int i = 0; i < 5; ++i) v[i] = src[i];
    float s1 = 0, s2 = 0;
#pragma unroll
    for (int i = 0; i < 5; ++i) {
      *(v4u*)(ws + O_X + b * 20480 + (gs * 5 + i) * 256 + row * 16) = v[i];
#pragma unroll
      for (int e = 0; e < 4; ++e) { float a = bf_lo(v[i][e]), c = bf_hi(v[i][e]); s1 += a + c; s2 += a * a + c * c; }
    }
    s1 += __shfl_xor(s1, 16); s2 += __shfl_xor(s2, 16);
    if (!(gs & 1)) *(f2v*)(ws + O_ST + b * 1024 + (gs >> 1) * 128 + row * 8) = (f2v){s1, s2};
    if (tid < 160) *(u32*)(ws + O_FL + b * 640 + tid * 4) = 0u;
    return;
  }
  b -= 128;
  int kind, strip, slab, rows, ld; const u16* src;
  if (b < 120) { kind = 0; strip = b >> 2; slab = b & 3; rows = 160; ld = 1920; src = wqkv; }
  else if (b < 160) { b -= 120; kind = 1; strip = b >> 2; slab = b & 3; rows = 160; ld = 640; src = wo; }
  else if (b < 268) { b -= 160; kind = 2; strip = b >> 2; slab = b & 3; rows = 160; ld = 1728; src = wg; }
  else if (b < 376) { b -= 268; kind = 3; strip = b >> 2; slab = b & 3; rows = 160; ld = 1728; src = wu; }
  else { b -= 376; kind = 4; strip = b / 9; slab = b % 9; rows = 192; ld = 640; src = wd; }
  {
    v4u v[6];
#pragma unroll
    for (int i = 0; i < 6; ++i) { const int r = (tid >> 3) + 32 * i;
      if (r < rows) v[i] = *(const v4u*)(src + (size_t)(slab * rows + r) * ld + strip * 64 + (tid & 7) * 8); }
#pragma unroll
    for (int i = 0; i < 6; ++i) { const int r = (tid >> 3) + 32 * i;
      if (r < rows) *(v4u*)(tile + r * 64 + (tid & 7) * 8) = v[i]; }
  }
  __syncthreads();
  const int cl = tid & 63, n = strip * 64 + cl;
  u32 colpart;
  if (kind == 0) { const int sec = n / 640, hh = (n % 640) / 20, d = n % 20, hd = hh & 1;
    colpart = O_W1 + (hh >> 1) * 15 * UNIT + (d < 16 ? (sec == 1 ? 0 : sec == 2 ? 4 * UNIT : 11 * UNIT) + (hd * 16 + d) * 16
                                                     : 8 * UNIT + ((sec == 1 ? 0 : sec == 2 ? 8 : 16) + hd * 4 + (d - 16)) * 16);
  } else if (kind == 2 || kind == 3) colpart = O_W4 + (n >> 4) * 4 * UNIT + ((n & 15) + (kind == 3 ? 16 : 0)) * 16;
  else { const int c = n / 80, f = n % 80;
    const int tl = f < 64 ? 2 * (f >> 5) + ((f >> 2) & 1) : 4, m = f < 64 ? 4 * ((f >> 3) & 3) + (f & 3) : f - 64;
    colpart = (kind == 1 ? O_W3 + c * 10 * UNIT : O_W5 + c * 27 * UNIT) + tl * 1024 + m * 16; }
  const bool xt = kind == 0 && (n % 20) >= 16;
  float cs = 0;
  for (int gi = tid >> 6; gi < rows / 8; gi += 4) {
    int ra, rb2; u32 rowpart;
    if (kind == 0 || kind == 2 || kind == 3) { ra = gi * 8; rb2 = ra + 4; const int r = slab * 20 + gi;
      rowpart = xt ? (r >> 1) * 768 + (r & 1) * 384 : (r >> 1) * 1024 + (r & 1) * 512; }
    else if (kind == 1) { const int jl = gi / 5, e5 = gi % 5, jp = slab * 4 + jl;
      if (e5 < 4) { ra = jl * 40 + (e5 >> 1) * 20 + (e5 & 1) * 4; rb2 = ra + 8; rowpart = (jp & 7) * UNIT + (jp >> 3) * 5120 + e5 * 256; }
      else { ra = jl * 40 + 16; rb2 = ra + 20; const int e = jp >> 2; rowpart = (8 + (e & 1)) * UNIT + (e >> 1) * 5120 + (jp & 3) * 256; } }
    else { ra = (gi >> 1) * 16 + (gi & 1) * 4; rb2 = ra + 8; const int T = slab * 12 + (gi >> 1), sg = T >> 1;
      rowpart = (sg % 27) * UNIT + (sg / 27) * 5120 + ((T & 1) * 2 + (gi & 1)) * 256; }
    u32 e[8];
#pragma unroll
    for (int i = 0; i < 4; ++i) { e[i] = tile[(ra + i) * 64 + cl]; e[4 + i] = tile[(rb2 + i) * 64 + cl]; }
#pragma unroll
    for (int i = 0; i < 8; ++i) cs += bf_lo(e[i]);
    *(v4u*)(ws + colpart + rowpart) = (v4u){e[0] | (e[1] << 16), e[2] | (e[3] << 16), e[4] | (e[5] << 16), e[6] | (e[7] << 16)};
  }
  if (kind == 0 || kind == 2 || kind == 3) {
    red[tid] = cs;
    __syncthreads();
    if (tid < 64) *(float*)(ws + O_CS + (slab * 5376 + (kind == 0 ? 0 : kind == 2 ? 1920 : 3648) + n) * 4) = red[tid] + red[tid + 64] + red[tid + 128] + red[tid + 192];
  }
}

extern "C" void kernel_launch(void* const* d_in, const int* in_sizes, int n_in, void* d_out, int out_size, void* d_ws, size_t ws_size, hipStream_t stream) {
  (void)in_sizes; (void)n_in; (void)out_size; (void)ws_size;
  (void)hipFuncSetAttribute((const void*)k_main, hipFuncAttributeMaxDynamicSharedMemorySize, LDS_BYTES);
  k_prep<<<128 + 120 + 40 + 108 + 108 + 90, 256, 0, stream>>>((const u16*)d_in[0], (const u16*)d_in[1], (const u16*)d_in[2], (const u16*)d_in[4],
                                                              (const u16*)d_in[5], (const u16*)d_in[6], (char*)d_ws);
  k_main<<<256, 512, LDS_BYTES, stream>>>((char*)d_ws, (char*)d_out);
}
```

```cpp
#include <hip/hip_runtime.h>
#include <stdint.h>
#include <utility>

typedef unsigned short u16;
typedef unsigned u32;
typedef unsigned v4u __attribute__((ext_vector_type(4)));
typedef unsigned v2u __attribute__((ext_vector_type(2)));
typedef __bf16 bh8 __attribute__((ext_vector_type(8)));
typedef __bf16 bh2 __attribute__((ext_vector_type(2)));
typedef float f2v __attribute__((ext_vector_type(2)));
typedef float f4v __attribute__((ext_vector_type(4)));
typedef float f16v __attribute__((ext_vector_type(16)));
#define LAS __attribute__((address_space(3)))
#define DI __device__ __forceinline__
typedef __amdgpu_buffer_rsrc_t rsrc_t;

constexpr u32 UNIT = 10240;
constexpr u32 O_W1 = 0;
constexpr u32 O_W3 = O_W1 + 16 * 15 * UNIT;
constexpr u32 O_W4 = O_W3 + 8 * 10 * UNIT;
constexpr u32 O_W5 = O_W4 + 112 * 4 * UNIT;
constexpr u32 O_X = O_W5 + 8 * 27 * UNIT;
constexpr u32 O_ST = O_X + 128 * 80 * 256;
constexpr u32 O_KV = O_ST + 128 * 8 * 128;
constexpr u32 O_OM = O_KV + 64 * 16 * 5120;
constexpr u32 O_OE = O_OM + 128 * 64 * 256;
constexpr u32 O_H = O_OE + 128 * 16 * 256;
constexpr u32 O_FL = O_H + 128 * 216 * 256;
constexpr u32 F_EP = O_FL;
constexpr u32 F_3 = O_FL + 4096;
constexpr u32 F_1 = O_FL + 8192;
constexpr u32 F_2 = F_1 + 32768;
constexpr u32 F_H = F_2 + 32768;
constexpr u32 O_C1 = F_H + 131072;
constexpr u32 O_C4 = O_C1 + 131072;
constexpr u32 O_DBG = O_C4 + 262144;
constexpr u32 O_DBG2 = O_DBG + 256 * 8 * 64 * 8;
constexpr u32 WS_USED = O_DBG2 + 256 * 8 * 768;

constexpr u32 L_MISC = 15 * UNIT;
constexpr u32 LDS_BYTES = 163840;

#define RAWNT "nt"
#define ARMS 0
#define XM 0
#define RAW 0
#define LXR 0
#define A2 0
#define PFL 0
#define AL 1
#define E5 1
#define LW 5
#define NT1 "nt"
#define XR_ASM ""
#define XR_CLOB "v96", "v97", "v98", "v99", "v100", "v101", "v102", "v103", "v104", "v105", "v106", "v107", "v108", "v109", "v110", "v111", "v112", "v113", "v114", "v115", "v116", "v117", "v118", "v119", "v120", "v121", "v122", "v123", "v124", "v125", "v126", "v127", "v128", "v129", "v130", "v131", "v132", "v133", "v134", "v135", "v136", "v137", "v138", "v139", "v140", "v141", "v142", "v143", "v144", "v145", "v146", "v147", "v148", "v149", "v150", "v151", "v152", "v153", "v154", "v155", "v156", "v157", "v158", "v159", "v160", "v161", "v162", "v163", "v164", "v165", "v166", "v167", "v168", "v169", "v170", "v171", "v172", "v173", "v174", "v175", "v176", "v177", "v178", "v179", "v180", "v181", "v182", "v183", "v184", "v185", "v186", "v187", "v188", "v189", "v190", "v191", "v192", "v193", "v194", "v195", "v196", "v197", "v198", "v199", "v200", "v201", "v202", "v203", "v204", "v205", "v206", "v207", "v208", "v209", "v210", "v211", "v212", "v213", "v214", "v215", "v216", "v217", "v218", "v219", "v220", "v221", "v222", "v223", "v224", "v225", "v226", "v227", "v228", "v229", "v230", "v231", "v232", "v233", "v234", "v235", "v236", "v237", "v238", "v239", "v240", "v241", "v242", "v243", "v244", "v245", "v246", "v247", "v248", "v249", "v250", "v251", "v252", "v253", "v254", "v255", "memory"
#define RAW_PRE {{1280, 102400, 2560, 30720}, {0, 81920, 492800, 112640}, {494080, 40960, 491520, 92160}, {984320, 122880, 985600, 51200}, {983040, 0, 1474560, 10240}}
#define MAIN_ASM \
"s_memrealtime s[20:21]\n" \
"s_waitcnt lgkmcnt(0)\n" \
"s_barrier\n" \
"v_mbcnt_lo_u32_b32 v0, -1, 0\n" \
"v_mbcnt_hi_u32_b32 v0, -1, v0\n" \
"v_lshlrev_b32 v0, 4, v0\n" \
"s_mov_b32 s24, 0\n" \
"s_mov_b32 s30, -1\n" \
"s_mov_b32 s31, 0\n" \
"s_mov_b32 s22, 0x07060302\n" \
"s_and_b32 s27, %4, 3\n" \
"s_mov_b32 s68, 4294967295\n" \
"s_mov_b32 s69, 4294967280\n" \
"s_mov_b32 s70, 4294967294\n" \
"s_mov_b32 s71, 4294967264\n" \
"s_mov_b32 s72, 4294967292\n" \
"s_mov_b32 s73, 4294967232\n" \
"s_mov_b32 s74, 4294967288\n" \
"s_mov_b32 s75, 4294967168\n" \
"s_mov_b32 s76, 4294967040\n" \
"s_mov_b32 s77, 4294963200\n" \
"s_mov_b32 s78, 4294966784\n" \
"s_mov_b32 s79, 4294959104\n" \
"s_mov_b32 s80, 4294966272\n" \
"s_mov_b32 s81, 4294950912\n" \
"s_mov_b32 s82, 4294965248\n" \
"s_mov_b32 s83, 4294934528\n" \
"s_mov_b32 s84, 4294901760\n" \
"s_mov_b32 s85, 4293918720\n" \
"s_mov_b32 s86, 4294836224\n" \
"s_mov_b32 s87, 4292870144\n" \
"s_mov_b32 s88, 4294705152\n" \
"s_mov_b32 s89, 4290772992\n" \
"s_mov_b32 s90, 4294443008\n" \
"s_mov_b32 s91, 4286578688\n" \
"s_mov_b32 s92, 4278190080\n" \
"s_mov_b32 s93, 4026531840\n" \
"s_mov_b32 s94, 4261412864\n" \
"s_mov_b32 s95, 3758096384\n" \
"s_mov_b32 s96, 4227858432\n" \
"s_mov_b32 s97, 3221225472\n" \
"s_mov_b32 s98, 4160749568\n" \
"s_mov_b32 s99, 2147483648\n" \
"s_mul_i32 s23, s27, 2624\n" \
"s_add_u32 s23, s23, 122880\n" \
"s_mul_i32 s67, s27, 2560\n" \
"s_add_u32 s67, s67, 92160\n" \
"s_mul_i32 s66, s27, 1280\n" \
"s_add_u32 s66, s66, 154368\n" \
"s_lshr_b32 s26, %3, 3\n" \
"s_and_b32 s28, %3, 7\n" \
"s_lshr_b32 s29, %4, 2\n" \
"s_mul_i32 s42, %2, 131072\n" \
"s_mul_i32 s25, s27, 32768\n" \
"s_add_u32 s42, s42, s25\n" \
"s_mul_i32 s25, s26, 8192\n" \
"s_add_u32 s42, s42, s25\n" \
"s_mul_i32 s25, s28, 512\n" \
"s_add_u32 s42, s42, s25\n" \
"s_mul_i32 s25, s29, -4096\n" \
"s_add_u32 s42, s42, s25\n" \
"s_add_u32 s42, s42, 18075648\n" \
"s_lshr_b32 s28, s28, 1\n" \
"s_mul_i32 s43, %2, 32768\n" \
"s_mul_i32 s25, s27, 8192\n" \
"s_add_u32 s43, s43, s25\n" \
"s_mul_i32 s25, s26, 2048\n" \
"s_add_u32 s43, s43, s25\n" \
"s_mul_i32 s25, s28, 256\n" \
"s_add_u32 s43, s43, s25\n" \
"s_mul_i32 s25, s29, -1024\n" \
"s_add_u32 s43, s43, s25\n" \
"s_add_u32 s43, s43, 20169728\n" \
"s_and_b32 s28, %3, 1\n" \
"s_lshl_b32 s28, s28, 3\n" \
"s_add_u32 s43, s43, s28\n" \
"s_cmp_ge_u32 %4, 4\n" \
"s_cbranch_scc1 .Lloader_%=\n" \
"v_add_u32 v1, 61440, v0\n" \
"v_add_u32 v2, 122880, v0\n" \
"v_and_b32 v3, 0xf0, v0\n" \
"v_lshrrev_b32 v12, 9, v0\n" \
"v_lshl_add_u32 v3, v12, 8, v3\n" \
"v_lshlrev_b32 v7, 4, v12\n" \
"v_add_u32 v7, 160512, v7\n" \
"v_bfe_u32 v12, v0, 8, 1\n" \
"v_mul_u32_u24 v12, 20480, v12\n" \
"v_add_u32 v3, v3, v12\n" \
"s_lshr_b32 s28, %3, 3\n" \
"s_and_b32 s29, %3, 7\n" \
"s_mul_i32 s52, %2, 442368\n" \
"s_mul_i32 s25, %4, 110592\n" \
"s_add_u32 s52, s52, s25\n" \
"s_mul_i32 s25, s28, 27648\n" \
"s_add_u32 s52, s52, s25\n" \
"s_mul_i32 s25, s29, 512\n" \
"s_add_u32 s52, s52, s25\n" \
"s_add_u32 s52, s52, 20692992\n" \
"s_cmp_ge_u32 s29, 6\n" \
"s_cselect_b32 s64, 2147483648, 0\n" \
"s_lshl_b32 s28, s28, 2\n" \
"s_and_b32 s29, %3, 3\n" \
"s_add_u32 s28, s28, s29\n" \
"s_bfe_u32 s29, %3, 0x10002\n" \
"s_and_b32 s27, %4, 1\n" \
"s_mul_i32 s26, %2, 8\n" \
"s_mul_i32 s25, s29, 4\n" \
"s_add_u32 s26, s26, s25\n" \
"s_mul_i32 s25, %4, 1\n" \
"s_add_u32 s26, s26, s25\n" \
"s_add_u32 s26, s26, 0\n" \
"s_mul_i32 s34, %2, 163840\n" \
"s_mul_i32 s25, %4, 40960\n" \
"s_add_u32 s34, s34, s25\n" \
"s_add_u32 s34, s34, 10076160\n" \
"s_add_u32 s35, s34, 4096\n" \
"s_add_u32 s36, s34, 8192\n" \
"s_add_u32 s37, s34, 12288\n" \
"s_add_u32 s38, s34, 16384\n" \
"s_mul_i32 s39, %2, 8192\n" \
"s_mul_i32 s25, %4, 2048\n" \
"s_add_u32 s39, s39, s25\n" \
"s_add_u32 s39, s39, 12697600\n" \
"s_mul_i32 s40, %2, 327680\n" \
"s_mul_i32 s25, %4, 81920\n" \
"s_add_u32 s40, s40, s25\n" \
"s_mul_i32 s25, %3, 5120\n" \
"s_add_u32 s40, s40, s25\n" \
"s_add_u32 s40, s40, 12828672\n" \
"s_add_u32 s41, s40, 4096\n" \
"s_mul_i32 s44, s26, 16384\n" \
"s_add_u32 s44, s44, 18071552\n" \
"s_add_u32 s45, s44, 8192\n" \
"s_add_u32 s46, s44, 4096\n" \
"s_add_u32 s47, s44, 12288\n" \
"s_mul_i32 s48, s26, 4096\n" \
"s_add_u32 s48, s48, 20168704\n" \
"s_mul_i32 s49, s26, 20480\n" \
"s_mul_i32 s25, s28, 2560\n" \
"s_add_u32 s49, s49, s25\n" \
"s_add_u32 s49, s49, 10076160\n" \
"s_mul_i32 s51, s26, 1024\n" \
"s_mul_i32 s25, s28, 128\n" \
"s_add_u32 s51, s51, s25\n" \
"s_add_u32 s51, s51, 12697600\n" \
"s_mul_i32 s58, s26, 20480\n" \
"s_mul_i32 s25, s28, 160\n" \
"s_add_u32 s58, s58, s25\n" \
"s_add_u32 s58, s58, 0\n" \
"s_mul_i32 s53, s26, 55296\n" \
"s_mul_i32 s25, s27, 27648\n" \
"s_add_u32 s53, s53, s25\n" \
"s_add_u32 s53, s53, 20705280\n" \
"s_mul_i32 s55, s26, 55296\n" \
"s_mul_i32 s25, s27, -27648\n" \
"s_add_u32 s55, s55, s25\n" \
"s_add_u32 s55, s55, 20744192\n" \
"s_mul_i32 s54, s26, 55296\n" \
"s_mul_i32 s25, s27, -82944\n" \
"s_add_u32 s54, s54, s25\n" \
"s_add_u32 s54, s54, 20760576\n" \
"s_mul_i32 s56, %2, 442368\n" \
"s_mul_i32 s25, %3, 27648\n" \
"s_add_u32 s56, s56, s25\n" \
"s_mul_i32 s25, %4, 6144\n" \
"s_add_u32 s56, s56, s25\n" \
"s_add_u32 s56, s56, 20692992\n" \
"s_add_u32 s57, s56, 4096\n" \
"s_mul_i32 s59, s28, 102400\n" \
"s_mul_i32 s25, %4, 10240\n" \
"s_add_u32 s59, s59, s25\n" \
"s_add_u32 s59, s59, 2508800\n" \
"s_mul_i32 s60, %4, 10240\n" \
"s_add_u32 s60, s60, 40960\n" \
"s_mul_i32 s61, %4, 2560\n" \
"s_add_u32 s61, s61, 112640\n" \
"s_mul_i32 s62, %4, 2560\n" \
"s_mul_i32 s25, s27, -5120\n" \
"s_add_u32 s62, s62, s25\n" \
"s_add_u32 s62, s62, 115200\n" \
"s_mul_i32 s63, s27, 5120\n" \
"s_add_u32 s63, s63, 0\n" \
"s_mul_i32 s65, %4, 256\n" \
"s_add_u32 s65, s65, 159488\n" \
"s_mul_i32 s26, %2, 2048\n" \
"s_mul_i32 s25, %4, 512\n" \
"s_add_u32 s26, s26, s25\n" \
"s_add_u32 s26, s26, 27779072\n" \
"v_bfe_u32 v12, v0, 4, 2\n" \
"v_lshlrev_b32 v12, 7, v12\n" \
"s_mov_b32 s28, s26\n" \
"v_mov_b32 v32, -1\n" \
"v_mov_b32 v33, -1\n" \
"v_mov_b32 v34, -1\n" \
"v_mov_b32 v35, -1\n" \
"s_mul_i32 s26, %2, 327680\n" \
"s_mul_i32 s25, %4, 81920\n" \
"s_add_u32 s26, s26, s25\n" \
"s_mul_i32 s25, %3, 5120\n" \
"s_add_u32 s26, s26, s25\n" \
"s_add_u32 s26, s26, 12828672\n" \
"buffer_store_dwordx4 v[32:35], v0, %0, s26 offen offset:0 sc1\n" \
"buffer_store_dwordx4 v[32:35], v0, %0, s26 offen offset:1024 sc1\n" \
"buffer_store_dwordx4 v[32:35], v0, %0, s26 offen offset:2048 sc1\n" \
"buffer_store_dwordx4 v[32:35], v0, %0, s26 offen offset:3072 sc1\n" \
"s_add_u32 s26, s26, 4096\n" \
"buffer_store_dwordx4 v[32:35], v0, %0, s26 offen offset:0 sc1\n" \
"s_mul_i32 s26, %2, 131072\n" \
"s_mul_i32 s25, %3, 8192\n" \
"s_add_u32 s26, s26, s25\n" \
"s_mul_i32 s25, %4, 2048\n" \
"s_add_u32 s26, s26, s25\n" \
"s_add_u32 s26, s26, 18071552\n" \
"buffer_store_dwordx4 v[32:35], v0, %0, s26 offen offset:0 sc1\n" \
"buffer_store_dwordx4 v[32:35], v0, %0, s26 offen offset:1024 sc1\n" \
"s_mul_i32 s26, %2, 32768\n" \
"s_mul_i32 s25, %3, 2048\n" \
"s_add_u32 s26, s26, s25\n" \
"s_mul_i32 s25, %4, 512\n" \
"s_add_u32 s26, s26, s25\n" \
"s_add_u32 s26, s26, 20168704\n" \
"s_mov_b32 exec_hi, 0\n" \
"buffer_store_dwordx4 v[32:35], v0, %0, s26 offen offset:0 sc1\n" \
"s_mov_b32 exec_hi, -1\n" \
"s_mul_i32 s26, %2, 442368\n" \
"s_mul_i32 s25, %3, 27648\n" \
"s_add_u32 s26, s26, s25\n" \
"s_mul_i32 s25, %4, 768\n" \
"s_add_u32 s26, s26, s25\n" \
"s_add_u32 s26, s26, 20717568\n" \
"s_mov_b32 exec_hi, 65535\n" \
"buffer_store_dwordx4 v[32:35], v0, %0, s26 offen offset:0 sc1\n" \
"s_mov_b32 exec_hi, -1\n" \
"s_mov_b32 s26, s28\n" \
"buffer_load_dword v13, v12, %0, s26 offen sc1\n" \
"s_waitcnt vmcnt(0)\n" \
"v_cmp_ne_u32 vcc, %5, v13\n" \
"s_cbranch_vccnz .Ld1_%=\n" \
".Ld2_%=:\n" \
".p2align 6\n" \
".Lcloop_%=:\n" \
"s_nop 1\n" \
"v_add_u32 v4, s67, v0\n" \
"v_lshrrev_b32 v5, 1, v0\n" \
"v_add_u32 v5, s67, v5\n" \
"v_bfe_u32 v12, v0, 4, 5\n" \
"v_subrev_u32 v6, 4, v12\n" \
"v_subrev_u32 v13, 12, v12\n" \
"v_cmp_gt_u32 vcc, 28, v12\n" \
"s_nop 1\n" \
"v_cndmask_b32 v6, v13, v6, vcc\n" \
"v_cmp_gt_u32 vcc, 24, v12\n" \
"s_nop 1\n" \
"v_cndmask_b32 v6, v6, v12, vcc\n" \
"v_lshrrev_b32 v12, 9, v0\n" \
"v_mad_u32_u24 v6, v12, 24, v6\n" \
"v_lshlrev_b32 v6, 4, v6\n" \
"v_add_u32 v6, 92160, v6\n" \
"v_bfe_u32 v13, v0, 8, 1\n" \
"v_lshlrev_b32 v13, 10, v13\n" \
"v_and_b32 v12, 0x200, v0\n" \
"v_add_u32 v13, v13, v12\n" \
"v_bfe_u32 v12, v0, 1, 7\n" \
"v_add_u32 v13, v13, v12\n" \
"s_cmp_lg_u32 s24, 1\n" \
"s_cbranch_scc1 .Lc1_%=\n" \
"s_sleep 8\n" \
".Lc1_%=:\n" \
"buffer_load_dwordx4 v[96:99], v3, %0, s34 offen offset:2048 sc1\n" \
"buffer_load_dwordx4 v[100:103], v3, %0, s35 offen offset:512 sc1\n" \
"buffer_load_dwordx4 v[104:107], v3, %0, s35 offen offset:3072 sc1\n" \
"buffer_load_dwordx4 v[108:111], v3, %0, s36 offen offset:1536 sc1\n" \
"buffer_load_dwordx4 v[112:115], v3, %0, s37 offen offset:0 sc1\n" \
"buffer_load_dwordx4 v[116:119], v3, %0, s37 offen offset:2560 sc1\n" \
"buffer_load_dwordx4 v[120:123], v3, %0, s38 offen offset:1024 sc1\n" \
"buffer_load_dwordx4 v[124:127], v3, %0, s38 offen offset:3584 sc1\n" \
"buffer_load_dwordx4 v[128:131], v3, %0, s34 offen offset:1536 sc1\n" \
"buffer_load_dwordx4 v[132:135], v3, %0, s35 offen offset:0 sc1\n" \
"s_cmp_lg_u32 s24, 0\n" \
"s_cbranch_scc1 .Lc2_%=\n" \
"s_barrier\n" \
".Lc2_%=:\n" \
"ds_read_b128 v[80:83], v2 offset:20480\n" \
"ds_read_b128 v[84:87], v6 offset:0\n" \
"ds_read_b128 v[88:91], v0 offset:0\n" \
"ds_read_b128 v[92:95], v2 offset:21504\n" \
"s_waitcnt vmcnt(8)\n" \
"v_bitop3_b32 v12, v96, v100, 1 bitop3:168\n" \
"v_cmp_ne_u32 vcc, 0, v12\n" \
"s_cbranch_vccnz .Lc3_%=\n" \
".Lc4_%=:\n" \
"s_waitcnt lgkmcnt(2)\n" \
"v_mfma_f32_32x32x16_bf16 v[32:47], v[80:83], v[96:99], 0\n" \
"ds_read_b128 v[80:83], v6 offset:768\n" \
"v_mfma_f32_32x32x16_bf16 v[48:63], v[84:87], v[96:99], 0\n" \
"ds_read_b128 v[84:87], v0 offset:1024\n" \
"buffer_load_dwordx4 v[136:139], v3, %0, s35 offen offset:2560 sc1\n" \
"s_waitcnt lgkmcnt(2)\n" \
"v_mfma_f32_32x32x16_bf16 v[64:79], v[88:91], v[96:99], 0\n" \
"ds_read_b128 v[88:91], v2 offset:22528\n" \
"v_mfma_f32_32x32x16_bf16 v[32:47], v[92:95], v[100:103], v[32:47]\n" \
"ds_read_b128 v[92:95], v6 offset:1536\n" \
"buffer_load_dwordx4 v[140:143], v3, %0, s36 offen offset:1024 sc1\n" \
"s_waitcnt lgkmcnt(2)\n" \
"v_mfma_f32_32x32x16_bf16 v[48:63], v[80:83], v[100:103], v[48:63]\n" \
"ds_read_b128 v[80:83], v0 offset:2048\n" \
"s_waitcnt vmcnt(8)\n" \
"v_bitop3_b32 v12, v104, v108, 1 bitop3:168\n" \
"v_cmp_ne_u32 vcc, 0, v12\n" \
"v_mfma_f32_32x32x16_bf16 v[64:79], v[84:87], v[100:103], v[64:79]\n" \
"ds_read_b128 v[84:87], v2 offset:23552\n" \
"buffer_load_dwordx4 v[144:147], v3, %0, s36 offen offset:3584 sc1\n" \
"s_cbranch_vccnz .Lc5_%=\n" \
".Lc6_%=:\n" \
"s_waitcnt lgkmcnt(2)\n" \
"v_mfma_f32_32x32x16_bf16 v[32:47], v[88:91], v[104:107], v[32:47]\n" \
"ds_read_b128 v[88:91], v6 offset:2304\n" \
"v_mfma_f32_32x32x16_bf16 v[48:63], v[92:95], v[104:107], v[48:63]\n" \
"ds_read_b128 v[92:95], v0 offset:3072\n" \
"buffer_load_dwordx4 v[148:151], v3, %0, s37 offen offset:2048 sc1\n" \
"s_waitcnt lgkmcnt(2)\n" \
"v_mfma_f32_32x32x16_bf16 v[64:79], v[80:83], v[104:107], v[64:79]\n" \
"ds_read_b128 v[80:83], v2 offset:24576\n" \
"v_mfma_f32_32x32x16_bf16 v[32:47], v[84:87], v[108:111], v[32:47]\n" \
"ds_read_b128 v[84:87], v6 offset:3072\n" \
"buffer_load_dwordx4 v[152:155], v3, %0, s38 offen offset:512 sc1\n" \
"s_waitcnt lgkmcnt(2)\n" \
"v_mfma_f32_32x32x16_bf16 v[48:63], v[88:91], v[108:111], v[48:63]\n" \
"ds_read_b128 v[88:91], v0 offset:4096\n" \
"s_waitcnt vmcnt(9)\n" \
"v_bitop3_b32 v12, v112, v116, 1 bitop3:168\n" \
"v_cmp_ne_u32 vcc, 0, v12\n" \
"v_mfma_f32_32x32x16_bf16 v[64:79], v[92:95], v[108:111], v[64:79]\n" \
"ds_read_b128 v[92:95], v2 offset:25600\n" \
"buffer_load_dwordx4 v[156:159], v3, %0, s38 offen offset:3072 sc1\n" \
"s_cbranch_vccnz .Lc7_%=\n" \
".Lc8_%=:\n" \
"s_waitcnt lgkmcnt(2)\n" \
"v_mfma_f32_32x32x16_bf16 v[32:47], v[80:83], v[112:115], v[32:47]\n" \
"ds_read_b128 v[80:83], v6 offset:3840\n" \
"v_mfma_f32_32x32x16_bf16 v[48:63], v[84:87], v[112:115], v[48:63]\n" \
"ds_read_b128 v[84:87], v0 offset:5120\n" \
"buffer_load_dwordx4 v[160:163], v3, %0, s34 offen offset:1024 sc1\n" \
"s_waitcnt lgkmcnt(2)\n" \
"v_mfma_f32_32x32x16_bf16 v[64:79], v[88:91], v[112:115], v[64:79]\n" \
"ds_read_b128 v[88:91], v2 offset:26624\n" \
"buffer_load_dwordx2 v[16:17], v13, %0, s39 offen offset:0 sc1\n" \
"buffer_load_dwordx2 v[18:19], v13, %0, s39 offen offset:128 sc1\n" \
"v_mfma_f32_32x32x16_bf16 v[32:47], v[92:95], v[116:119], v[32:47]\n" \
"ds_read_b128 v[92:95], v6 offset:4608\n" \
"buffer_load_dwordx2 v[20:21], v13, %0, s39 offen offset:256 sc1\n" \
"buffer_load_dwordx2 v[22:23], v13, %0, s39 offen offset:384 sc1\n" \
"s_waitcnt lgkmcnt(2)\n" \
"v_mfma_f32_32x32x16_bf16 v[48:63], v[80:83], v[116:119], v[48:63]\n" \
"ds_read_b128 v[80:83], v0 offset:6144\n" \
"buffer_load_dwordx4 v[164:167], v3, %0, s34 offen offset:3584 sc1\n" \
"s_waitcnt vmcnt(14)\n" \
"v_bitop3_b32 v12, v120, v124, 1 bitop3:168\n" \
"v_cmp_ne_u32 vcc, 0, v12\n" \
"v_mfma_f32_32x32x16_bf16 v[64:79], v[84:87], v[116:119], v[64:79]\n" \
"ds_read_b128 v[84:87], v2 offset:27648\n" \
"s_cbranch_vccnz .Lc9_%=\n" \
".Lc10_%=:\n" \
"s_waitcnt lgkmcnt(2)\n" \
"v_mfma_f32_32x32x16_bf16 v[32:47], v[88:91], v[120:123], v[32:47]\n" \
"ds_read_b128 v[88:91], v6 offset:5376\n" \
"buffer_load_dwordx4 v[168:171], v3, %0, s35 offen offset:2048 sc1\n" \
"v_mfma_f32_32x32x16_bf16 v[48:63], v[92:95], v[120:123], v[48:63]\n" \
"ds_read_b128 v[92:95], v0 offset:7168\n" \
"s_waitcnt lgkmcnt(2)\n" \
"v_mfma_f32_32x32x16_bf16 v[64:79], v[80:83], v[120:123], v[64:79]\n" \
"ds_read_b128 v[80:83], v2 offset:28672\n" \
"buffer_load_dwordx4 v[172:175], v3, %0, s36 offen offset:512 sc1\n" \
"v_mfma_f32_32x32x16_bf16 v[32:47], v[84:87], v[124:127], v[32:47]\n" \
"ds_read_b128 v[84:87], v6 offset:6144\n" \
"s_waitcnt lgkmcnt(2)\n" \
"v_mfma_f32_32x32x16_bf16 v[48:63], v[88:91], v[124:127], v[48:63]\n" \
"ds_read_b128 v[88:91], v0 offset:8192\n" \
"buffer_load_dwordx4 v[176:179], v3, %0, s36 offen offset:3072 sc1\n" \
"s_waitcnt vmcnt(15)\n" \
"v_bitop3_b32 v12, v128, v132, 1 bitop3:168\n" \
"v_cmp_ne_u32 vcc, 0, v12\n" \
"v_mfma_f32_32x32x16_bf16 v[64:79], v[92:95], v[124:127], v[64:79]\n" \
"ds_read_b128 v[92:95], v2 offset:29696\n" \
"s_cbranch_vccnz .Lc11_%=\n" \
".Lc12_%=:\n" \
"s_waitcnt lgkmcnt(2)\n" \
"v_mfma_f32_32x32x16_bf16 v[32:47], v[80:83], v[128:131], v[32:47]\n" \
"ds_read_b128 v[80:83], v6 offset:6912\n" \
"buffer_load_dwordx4 v[180:183], v3, %0, s37 offen offset:1536 sc1\n" \
"s_barrier\n" \
"v_mfma_f32_32x32x16_bf16 v[48:63], v[84:87], v[128:131], v[48:63]\n" \
"ds_read_b128 v[84:87], v0 offset:9216\n" \
"s_waitcnt lgkmcnt(2)\n" \
"v_mfma_f32_32x32x16_bf16 v[64:79], v[88:91], v[128:131], v[64:79]\n" \
"ds_read_b128 v[88:91], v1 offset:20480\n" \
"buffer_load_dwordx4 v[184:187], v3, %0, s38 offen offset:0 sc1\n" \
"v_mfma_f32_32x32x16_bf16 v[32:47], v[92:95], v[132:135], v[32:47]\n" \
"ds_read_b128 v[92:95], v6 offset:7680\n" \
"s_waitcnt lgkmcnt(2)\n" \
"v_mfma_f32_32x32x16_bf16 v[48:63], v[80:83], v[132:135], v[48:63]\n" \
"ds_read_b128 v[80:83], v0 offset:10240\n" \
"buffer_load_dwordx4 v[188:191], v3, %0, s38 offen offset:2560 sc1\n" \
"s_waitcnt vmcnt(16)\n" \
"v_bitop3_b32 v12, v136, v140, 1 bitop3:168\n" \
"v_cmp_ne_u32 vcc, 0, v12\n" \
"v_mfma_f32_32x32x16_bf16 v[64:79], v[84:87], v[132:135], v[64:79]\n" \
"ds_read_b128 v[84:87], v1 offset:21504\n" \
"s_cbranch_vccnz .Lc13_%=\n" \
".Lc14_%=:\n" \
"s_waitcnt lgkmcnt(2)\n" \
"v_mfma_f32_32x32x16_bf16 v[32:47], v[88:91], v[136:139], v[32:47]\n" \
"ds_read_b128 v[88:91], v6 offset:8448\n" \
"buffer_load_dwordx4 v[192:195], v3, %0, s34 offen offset:512 sc1\n" \
"v_mfma_f32_32x32x16_bf16 v[48:63], v[92:95], v[136:139], v[48:63]\n" \
"ds_read_b128 v[92:95], v0 offset:11264\n" \
"s_waitcnt lgkmcnt(2)\n" \
"v_mfma_f32_32x32x16_bf16 v[64:79], v[80:83], v[136:139], v[64:79]\n" \
"ds_read_b128 v[80:83], v1 offset:22528\n" \
"buffer_load_dwordx4 v[196:199], v3, %0, s34 offen offset:3072 sc1\n" \
"v_mfma_f32_32x32x16_bf16 v[32:47], v[84:87], v[140:143], v[32:47]\n" \
"ds_read_b128 v[84:87], v6 offset:9216\n" \
"s_waitcnt lgkmcnt(2)\n" \
"v_mfma_f32_32x32x16_bf16 v[48:63], v[88:91], v[140:143], v[48:63]\n" \
"ds_read_b128 v[88:91], v0 offset:12288\n" \
"buffer_load_dwordx4 v[200:203], v3, %0, s35 offen offset:1536 sc1\n" \
"s_waitcnt vmcnt(17)\n" \
"v_bitop3_b32 v12, v144, v148, 1 bitop3:168\n" \
"v_cmp_ne_u32 vcc, 0, v12\n" \
"v_mfma_f32_32x32x16_bf16 v[64:79], v[92:95], v[140:143], v[64:79]\n" \
"ds_read_b128 v[92:95], v1 offset:23552\n" \
"s_cbranch_vccnz .Lc15_%=\n" \
".Lc16_%=:\n" \
"s_waitcnt lgkmcnt(2)\n" \
"v_mfma_f32_32x32x16_bf16 v[32:47], v[80:83], v[144:147], v[32:47]\n" \
"ds_read_b128 v[80:83], v6 offset:9984\n" \
"buffer_load_dwordx4 v[204:207], v3, %0, s36 offen offset:0 sc1\n" \
"v_mfma_f32_32x32x16_bf16 v[48:63], v[84:87], v[144:147], v[48:63]\n" \
"ds_read_b128 v[84:87], v0 offset:13312\n" \
"s_waitcnt lgkmcnt(2)\n" \
"v_mfma_f32_32x32x16_bf16 v[64:79], v[88:91], v[144:147], v[64:79]\n" \
"ds_read_b128 v[88:91], v1 offset:24576\n" \
"buffer_load_dwordx4 v[208:211], v3, %0, s36 offen offset:2560 sc1\n" \
"v_mfma_f32_32x32x16_bf16 v[32:47], v[92:95], v[148:151], v[32:47]\n" \
"ds_read_b128 v[92:95], v6 offset:10752\n" \
"s_waitcnt lgkmcnt(2)\n" \
"v_mfma_f32_32x32x16_bf16 v[48:63], v[80:83], v[148:151], v[48:63]\n" \
"ds_read_b128 v[80:83], v1 offset:25600\n" \
"buffer_load_dwordx4 v[212:215], v3, %0, s37 offen offset:1024 sc1\n" \
"s_waitcnt vmcnt(18)\n" \
"v_bitop3_b32 v12, v152, v156, 1 bitop3:168\n" \
"v_cmp_ne_u32 vcc, 0, v12\n" \
"v_mfma_f32_32x32x16_bf16 v[64:79], v[84:87], v[148:151], v[64:79]\n" \
"ds_read_b128 v[84:87], v6 offset:11520\n" \
"s_cbranch_vccnz .Lc17_%=\n" \
".Lc18_%=:\n" \
"s_waitcnt lgkmcnt(2)\n" \
"v_mfma_f32_32x32x16_bf16 v[32:47], v[88:91], v[152:155], v[32:47]\n" \
"ds_read_b128 v[88:91], v1 offset:26624\n" \
"buffer_load_dwordx4 v[216:219], v3, %0, s37 offen offset:3584 sc1\n" \
"v_mfma_f32_32x32x16_bf16 v[48:63], v[92:95], v[152:155], v[48:63]\n" \
"ds_read_b128 v[92:95], v6 offset:12288\n" \
"s_waitcnt lgkmcnt(2)\n" \
"v_mfma_f32_32x32x16_bf16 v[32:47], v[80:83], v[156:159], v[32:47]\n" \
"ds_read_b128 v[80:83], v1 offset:27648\n" \
"buffer_load_dwordx4 v[220:223], v3, %0, s38 offen offset:2048 sc1\n" \
"s_waitcnt vmcnt(14)\n" \
"v_bitop3_b32 v12, v160, v164, 1 bitop3:168\n" \
"v_cmp_ne_u32 vcc, 0, v12\n" \
"v_mfma_f32_32x32x16_bf16 v[48:63], v[84:87], v[156:159], v[48:63]\n" \
"ds_read_b128 v[84:87], v6 offset:13056\n" \
"s_cbranch_vccnz .Lc19_%=\n" \
".Lc20_%=:\n" \
"s_waitcnt lgkmcnt(2)\n" \
"v_mfma_f32_32x32x16_bf16 v[32:47], v[88:91], v[160:163], v[32:47]\n" \
"ds_read_b128 v[88:91], v1 offset:28672\n" \
"buffer_load_dwordx4 v[224:227], v3, %0, s34 offen offset:0 sc1\n" \
"v_mfma_f32_32x32x16_bf16 v[48:63], v[92:95], v[160:163], v[48:63]\n" \
"ds_read_b128 v[92:95], v6 offset:13824\n" \
"s_waitcnt lgkmcnt(2)\n" \
"v_mfma_f32_32x32x16_bf16 v[32:47], v[80:83], v[164:167], v[32:47]\n" \
"ds_read_b128 v[80:83], v1 offset:29696\n" \
"buffer_load_dwordx4 v[228:231], v3, %0, s34 offen offset:2560 sc1\n" \
"s_waitcnt vmcnt(14)\n" \
"v_bitop3_b32 v12, v168, v172, 1 bitop3:168\n" \
"v_cmp_ne_u32 vcc, 0, v12\n" \
"v_mfma_f32_32x32x16_bf16 v[48:63], v[84:87], v[164:167], v[48:63]\n" \
"ds_read_b128 v[84:87], v6 offset:14592\n" \
"s_cbranch_vccnz .Lc21_%=\n" \
".Lc22_%=:\n" \
"s_barrier\n" \
"s_waitcnt lgkmcnt(2)\n" \
"v_mfma_f32_32x32x16_bf16 v[32:47], v[88:91], v[168:171], v[32:47]\n" \
"ds_read_b128 v[88:91], v2 offset:0\n" \
"buffer_load_dwordx4 v[232:235], v3, %0, s35 offen offset:1024 sc1\n" \
"v_mfma_f32_32x32x16_bf16 v[48:63], v[92:95], v[168:171], v[48:63]\n" \
"ds_read_b128 v[92:95], v6 offset:15360\n" \
"s_waitcnt lgkmcnt(2)\n" \
"v_mfma_f32_32x32x16_bf16 v[32:47], v[80:83], v[172:175], v[32:47]\n" \
"ds_read_b128 v[80:83], v2 offset:1024\n" \
"buffer_load_dwordx4 v[236:239], v3, %0, s35 offen offset:3584 sc1\n" \
"s_waitcnt vmcnt(14)\n" \
"v_bitop3_b32 v12, v176, v180, 1 bitop3:168\n" \
"v_cmp_ne_u32 vcc, 0, v12\n" \
"v_mfma_f32_32x32x16_bf16 v[48:63], v[84:87], v[172:175], v[48:63]\n" \
"ds_read_b128 v[84:87], v6 offset:16128\n" \
"s_cbranch_vccnz .Lc23_%=\n" \
".Lc24_%=:\n" \
"s_waitcnt lgkmcnt(2)\n" \
"v_mfma_f32_32x32x16_bf16 v[32:47], v[88:91], v[176:179], v[32:47]\n" \
"ds_read_b128 v[88:91], v2 offset:2048\n" \
"buffer_load_dwordx4 v[240:243], v3, %0, s36 offen offset:2048 sc1\n" \
"v_mfma_f32_32x32x16_bf16 v[48:63], v[92:95], v[176:179], v[48:63]\n" \
"ds_read_b128 v[92:95], v6 offset:16896\n" \
"s_waitcnt lgkmcnt(2)\n" \
"v_mfma_f32_32x32x16_bf16 v[32:47], v[80:83], v[180:183], v[32:47]\n" \
"ds_read_b128 v[80:83], v2 offset:3072\n" \
"buffer_load_dwordx4 v[244:247], v3, %0, s37 offen offset:512 sc1\n" \
"s_waitcnt vmcnt(14)\n" \
"v_bitop3_b32 v12, v184, v188, 1 bitop3:168\n" \
"v_cmp_ne_u32 vcc, 0, v12\n" \
"v_mfma_f32_32x32x16_bf16 v[48:63], v[84:87], v[180:183], v[48:63]\n" \
"ds_read_b128 v[84:87], v6 offset:17664\n" \
"s_cbranch_vccnz .Lc25_%=\n" \
".Lc26_%=:\n" \
"v_bitop3_b32 v14, v16, v17, v18 bitop3:254\n" \
"v_bitop3_b32 v14, v14, v19, v20 bitop3:254\n" \
"s_waitcnt lgkmcnt(2)\n" \
"v_mfma_f32_32x32x16_bf16 v[32:47], v[88:91], v[184:187], v[32:47]\n" \
"ds_read_b128 v[88:91], v2 offset:4096\n" \
"buffer_load_dwordx4 v[248:251], v3, %0, s37 offen offset:3072 sc1\n" \
"v_bitop3_b32 v14, v14, v21, v22 bitop3:254\n" \
"v_mfma_f32_32x32x16_bf16 v[48:63], v[92:95], v[184:187], v[48:63]\n" \
"ds_read_b128 v[92:95], v6 offset:18432\n" \
"v_bitop3_b32 v14, v14, v23, 1 bitop3:168\n" \
"v_cmp_ne_u32 s[28:29], 0, v14\n" \
"s_nop 0\n" \
"s_cmp_lg_u64 s[28:29], 0\n" \
"s_cbranch_scc1 .Lc27_%=\n" \
".Lc28_%=:\n" \
"v_add_f32 v16, v16, v18\n" \
"s_waitcnt lgkmcnt(2)\n" \
"v_mfma_f32_32x32x16_bf16 v[32:47], v[80:83], v[188:191], v[32:47]\n" \
"ds_read_b128 v[80:83], v2 offset:5120\n" \
"buffer_load_dwordx4 v[252:255], v3, %0, s38 offen offset:1536 sc1\n" \
"s_waitcnt vmcnt(14)\n" \
"v_bitop3_b32 v12, v192, v196, 1 bitop3:168\n" \
"v_cmp_ne_u32 vcc, 0, v12\n" \
"v_mfma_f32_32x32x16_bf16 v[48:63], v[84:87], v[188:191], v[48:63]\n" \
"ds_read_b128 v[84:87], v6 offset:19200\n" \
"s_cbranch_vccnz .Lc29_%=\n" \
".Lc30_%=:\n" \
"v_add_f32 v17, v17, v19\n" \
"v_add_f32 v20, v20, v22\n" \
"s_waitcnt lgkmcnt(2)\n" \
"v_mfma_f32_32x32x16_bf16 v[32:47], v[88:91], v[192:195], v[32:47]\n" \
"ds_read_b128 v[88:91], v2 offset:6144\n" \
"v_add_f32 v21, v21, v23\n" \
"v_add_f32 v16, v16, v20\n" \
"v_add_f32 v17, v17, v21\n" \
"v_mfma_f32_32x32x16_bf16 v[48:63], v[92:95], v[192:195], v[48:63]\n" \
"ds_read_b128 v[92:95], v6 offset:19968\n" \
"s_waitcnt lgkmcnt(2)\n" \
"v_mfma_f32_32x32x16_bf16 v[32:47], v[80:83], v[196:199], v[32:47]\n" \
"ds_read_b128 v[80:83], v2 offset:7168\n" \
"s_waitcnt vmcnt(12)\n" \
"v_bitop3_b32 v12, v200, v204, 1 bitop3:168\n" \
"v_cmp_ne_u32 vcc, 0, v12\n" \
"v_permlane32_swap_b32 v16, v17\n" \
"v_mfma_f32_32x32x16_bf16 v[48:63], v[84:87], v[196:199], v[48:63]\n" \
"ds_read_b128 v[84:87], v6 offset:20736\n" \
"s_cbranch_vccnz .Lc31_%=\n" \
".Lc32_%=:\n" \
"s_waitcnt lgkmcnt(2)\n" \
"v_mfma_f32_32x32x16_bf16 v[32:47], v[88:91], v[200:203], v[32:47]\n" \
"ds_read_b128 v[88:91], v2 offset:8192\n" \
"v_add_f32 v16, v16, v17\n" \
"v_mov_b32 v17, v16\n" \
"v_mfma_f32_32x32x16_bf16 v[48:63], v[92:95], v[200:203], v[48:63]\n" \
"ds_read_b128 v[92:95], v6 offset:21504\n" \
"v_permlane32_swap_b32 v16, v17\n" \
"s_waitcnt lgkmcnt(2)\n" \
"v_mfma_f32_32x32x16_bf16 v[32:47], v[80:83], v[204:207], v[32:47]\n" \
"ds_read_b128 v[80:83], v2 offset:9216\n" \
"s_waitcnt vmcnt(10)\n" \
"v_bitop3_b32 v12, v208, v212, 1 bitop3:168\n" \
"v_cmp_ne_u32 vcc, 0, v12\n" \
"v_mul_f32 v16, 0x3acccccd, v16\n" \
"v_mfma_f32_32x32x16_bf16 v[48:63], v[84:87], v[204:207], v[48:63]\n" \
"ds_read_b128 v[84:87], v6 offset:22272\n" \
"s_cbranch_vccnz .Lc33_%=\n" \
".Lc34_%=:\n" \
"v_mul_f32 v17, 0x3acccccd, v17\n" \
"v_fma_f32 v17, -v16, v16, v17\n" \
"s_barrier\n" \
"s_waitcnt lgkmcnt(2)\n" \
"v_mfma_f32_32x32x16_bf16 v[32:47], v[88:91], v[208:211], v[32:47]\n" \
"ds_read_b128 v[88:91], v2 offset:10240\n" \
"v_max_f32 v17, 0, v17\n" \
"v_add_f32 v17, 0x3727c5ac, v17\n" \
"v_mfma_f32_32x32x16_bf16 v[48:63], v[92:95], v[208:211], v[48:63]\n" \
"ds_read_b128 v[92:95], v6 offset:23040\n" \
"v_rsq_f32 v8, v17\n" \
"v_lshrrev_b32 v13, 2, v0\n" \
"s_waitcnt lgkmcnt(2)\n" \
"v_mfma_f32_32x32x16_bf16 v[32:47], v[80:83], v[212:215], v[32:47]\n" \
"ds_read_b128 v[80:83], v2 offset:11264\n" \
"s_waitcnt vmcnt(8)\n" \
"v_bitop3_b32 v12, v216, v220, 1 bitop3:168\n" \
"v_cmp_ne_u32 vcc, 0, v12\n" \
"v_add_u32 v13, s65, v13\n" \
"v_mfma_f32_32x32x16_bf16 v[48:63], v[84:87], v[212:215], v[48:63]\n" \
"ds_read_b128 v[84:87], v6 offset:23808\n" \
"s_cbranch_vccnz .Lc35_%=\n" \
".Lc36_%=:\n" \
"v_mul_f32 v9, -v16, v8\n" \
"v_mul_f32 v10, 0x3ea52b5c, v8\n" \
"s_waitcnt lgkmcnt(2)\n" \
"v_mfma_f32_32x32x16_bf16 v[32:47], v[88:91], v[216:219], v[32:47]\n" \
"ds_read_b128 v[88:91], v2 offset:12288\n" \
"v_mul_f32 v11, 0x3ea52b5c, v9\n" \
"v_cndmask_b32 v14, v8, v9, s[30:31]\n" \
"ds_write_b32 v13, v14\n" \
"v_mfma_f32_32x32x16_bf16 v[48:63], v[92:95], v[216:219], v[48:63]\n" \
"ds_read_b128 v[92:95], v6 offset:24576\n" \
"ds_read_b128 v[16:19], v7 offset:0\n" \
"ds_read_b128 v[20:23], v7 offset:32\n" \
"ds_read_b128 v[24:27], v7 offset:64\n" \
"s_waitcnt lgkmcnt(6)\n" \
"v_mfma_f32_32x32x16_bf16 v[32:47], v[80:83], v[220:223], v[32:47]\n" \
"ds_read_b128 v[80:83], v2 offset:13312\n" \
"s_waitcnt vmcnt(6)\n" \
"v_bitop3_b32 v12, v224, v228, 1 bitop3:168\n" \
"v_cmp_ne_u32 vcc, 0, v12\n" \
"ds_read_b128 v[28:31], v7 offset:96\n" \
"v_mfma_f32_32x32x16_bf16 v[48:63], v[84:87], v[220:223], v[48:63]\n" \
"ds_read_b128 v[84:87], v6 offset:25344\n" \
"s_cbranch_vccnz .Lc37_%=\n" \
".Lc38_%=:\n" \
"s_waitcnt lgkmcnt(6)\n" \
"v_mfma_f32_32x32x16_bf16 v[32:47], v[88:91], v[224:227], v[32:47]\n" \
"ds_read_b128 v[88:91], v2 offset:14336\n" \
"v_mfma_f32_32x32x16_bf16 v[48:63], v[92:95], v[224:227], v[48:63]\n" \
"ds_read_b128 v[92:95], v6 offset:26112\n" \
"s_waitcnt lgkmcnt(2)\n" \
"v_mfma_f32_32x32x16_bf16 v[32:47], v[80:83], v[228:231], v[32:47]\n" \
"ds_read_b128 v[80:83], v2 offset:15360\n" \
"s_waitcnt vmcnt(4)\n" \
"v_bitop3_b32 v12, v232, v236, 1 bitop3:168\n" \
"v_cmp_ne_u32 vcc, 0, v12\n" \
"v_mul_f32 v16, v9, v16\n" \
"v_mfma_f32_32x32x16_bf16 v[48:63], v[84:87], v[228:231], v[48:63]\n" \
"ds_read_b128 v[84:87], v6 offset:26880\n" \
"s_cbranch_vccnz .Lc39_%=\n" \
".Lc40_%=:\n" \
"v_mul_f32 v17, v9, v17\n" \
"v_mul_f32 v18, v9, v18\n" \
"s_waitcnt lgkmcnt(2)\n" \
"v_mfma_f32_32x32x16_bf16 v[32:47], v[88:91], v[232:235], v[32:47]\n" \
"ds_read_b128 v[88:91], v2 offset:16384\n" \
"v_mul_f32 v19, v9, v19\n" \
"v_mul_f32 v20, v9, v20\n" \
"v_mul_f32 v21, v9, v21\n" \
"v_mfma_f32_32x32x16_bf16 v[48:63], v[92:95], v[232:235], v[48:63]\n" \
"ds_read_b128 v[92:95], v6 offset:27648\n" \
"v_mul_f32 v22, v9, v22\n" \
"v_mul_f32 v23, v9, v23\n" \
"v_mul_f32 v24, v9, v24\n" \
"s_waitcnt lgkmcnt(2)\n" \
"v_mfma_f32_32x32x16_bf16 v[32:47], v[80:83], v[236:239], v[32:47]\n" \
"ds_read_b128 v[80:83], v2 offset:17408\n" \
"s_waitcnt vmcnt(2)\n" \
"v_bitop3_b32 v12, v240, v244, 1 bitop3:168\n" \
"v_cmp_ne_u32 vcc, 0, v12\n" \
"v_mul_f32 v25, v9, v25\n" \
"v_mfma_f32_32x32x16_bf16 v[48:63], v[84:87], v[236:239], v[48:63]\n" \
"ds_read_b128 v[84:87], v6 offset:28416\n" \
"s_cbranch_vccnz .Lc41_%=\n" \
".Lc42_%=:\n" \
"v_mul_f32 v26, v9, v26\n" \
"v_mul_f32 v27, v9, v27\n" \
"s_waitcnt lgkmcnt(2)\n" \
"v_mfma_f32_32x32x16_bf16 v[32:47], v[88:91], v[240:243], v[32:47]\n" \
"ds_read_b128 v[88:91], v2 offset:18432\n" \
"v_mul_f32 v28, v9, v28\n" \
"v_mul_f32 v29, v9, v29\n" \
"v_mul_f32 v30, v9, v30\n" \
"v_mfma_f32_32x32x16_bf16 v[48:63], v[92:95], v[240:243], v[48:63]\n" \
"ds_read_b128 v[92:95], v6 offset:29184\n" \
"v_mul_f32 v31, v9, v31\n" \
"s_waitcnt lgkmcnt(2)\n" \
"v_mfma_f32_32x32x16_bf16 v[32:47], v[80:83], v[244:247], v[32:47]\n" \
"ds_read_b128 v[80:83], v2 offset:19456\n" \
"s_waitcnt vmcnt(0)\n" \
"v_bitop3_b32 v12, v248, v252, 1 bitop3:168\n" \
"v_cmp_ne_u32 vcc, 0, v12\n" \
"v_mfma_f32_32x32x16_bf16 v[48:63], v[84:87], v[244:247], v[48:63]\n" \
"ds_read_b128 v[84:87], v6 offset:29952\n" \
"s_cbranch_vccnz .Lc43_%=\n" \
".Lc44_%=:\n" \
"s_waitcnt lgkmcnt(2)\n" \
"v_mfma_f32_32x32x16_bf16 v[32:47], v[88:91], v[248:251], v[32:47]\n" \
"ds_read_b128 v[88:91], v0 offset:14336\n" \
"v_mfma_f32_32x32x16_bf16 v[48:63], v[92:95], v[248:251], v[48:63]\n" \
"ds_read_b128 v[92:95], v0 offset:15360\n" \
"s_waitcnt lgkmcnt(2)\n" \
"v_mfma_f32_32x32x16_bf16 v[32:47], v[80:83], v[252:255], v[32:47]\n" \
"ds_read_b128 v[80:83], v0 offset:16384\n" \
"v_mfma_f32_32x32x16_bf16 v[48:63], v[84:87], v[252:255], v[48:63]\n" \
"ds_read_b128 v[84:87], v0 offset:17408\n" \
"s_waitcnt lgkmcnt(2)\n" \
"v_mfma_f32_32x32x16_bf16 v[64:79], v[88:91], v[152:155], v[64:79]\n" \
"ds_read_b128 v[88:91], v0 offset:18432\n" \
"s_barrier\n" \
"v_mfma_f32_32x32x16_bf16 v[64:79], v[92:95], v[156:159], v[64:79]\n" \
"ds_read_b128 v[92:95], v0 offset:19456\n" \
"s_nop 2\n" \
"v_fma_f32 v32, v32, v8, v16\n" \
"v_fma_f32 v33, v33, v8, v17\n" \
"v_fma_f32 v34, v34, v8, v18\n" \
"v_fma_f32 v35, v35, v8, v19\n" \
"v_fma_f32 v36, v36, v8, v20\n" \
"s_waitcnt lgkmcnt(2)\n" \
"v_mfma_f32_32x32x16_bf16 v[64:79], v[80:83], v[160:163], v[64:79]\n" \
"ds_read_b128 v[80:83], v0 offset:20480\n" \
"v_fma_f32 v37, v37, v8, v21\n" \
"v_fma_f32 v38, v38, v8, v22\n" \
"v_fma_f32 v39, v39, v8, v23\n" \
"v_fma_f32 v40, v40, v8, v24\n" \
"v_fma_f32 v41, v41, v8, v25\n" \
"v_mfma_f32_32x32x16_bf16 v[64:79], v[84:87], v[164:167], v[64:79]\n" \
"ds_read_b128 v[84:87], v0 offset:21504\n" \
"v_fma_f32 v42, v42, v8, v26\n" \
"v_fma_f32 v43, v43, v8, v27\n" \
"v_fma_f32 v44, v44, v8, v28\n" \
"v_fma_f32 v45, v45, v8, v29\n" \
"v_fma_f32 v46, v46, v8, v30\n" \
"s_waitcnt lgkmcnt(2)\n" \
"v_mfma_f32_32x32x16_bf16 v[64:79], v[88:91], v[168:171], v[64:79]\n" \
"ds_read_b128 v[88:91], v0 offset:22528\n" \
"v_fma_f32 v47, v47, v8, v31\n" \
"ds_read_b128 v[16:19], v7 offset:256\n" \
"ds_read_b128 v[20:23], v7 offset:288\n" \
"ds_read_b128 v[24:27], v7 offset:320\n" \
"v_cvt_pk_bf16_f32 v32, v32, v33\n" \
"v_mfma_f32_32x32x16_bf16 v[64:79], v[92:95], v[172:175], v[64:79]\n" \
"ds_read_b128 v[92:95], v0 offset:23552\n" \
"v_cvt_pk_bf16_f32 v33, v34, v35\n" \
"v_cvt_pk_bf16_f32 v34, v36, v37\n" \
"v_cvt_pk_bf16_f32 v35, v38, v39\n" \
"v_bfi_b32 v32, 1, s24, v32\n" \
"s_waitcnt lgkmcnt(5)\n" \
"v_mfma_f32_32x32x16_bf16 v[64:79], v[80:83], v[176:179], v[64:79]\n" \
"ds_read_b128 v[80:83], v0 offset:24576\n" \
"buffer_store_dwordx4 v[32:35], v0, %0, s40 offen offset:0 sc1\n" \
"v_cvt_pk_bf16_f32 v36, v40, v41\n" \
"v_cvt_pk_bf16_f32 v37, v42, v43\n" \
"v_cvt_pk_bf16_f32 v38, v44, v45\n" \
"v_mfma_f32_32x32x16_bf16 v[64:79], v[84:87], v[180:183], v[64:79]\n" \
"ds_read_b128 v[84:87], v0 offset:25600\n" \
"v_cvt_pk_bf16_f32 v39, v46, v47\n" \
"v_bfi_b32 v36, 1, s24, v36\n" \
"buffer_store_dwordx4 v[36:39], v0, %0, s40 offen offset:1024 sc1\n" \
"s_waitcnt lgkmcnt(5)\n" \
"v_mul_f32 v16, v9, v16\n" \
"s_waitcnt lgkmcnt(2)\n" \
"v_mfma_f32_32x32x16_bf16 v[64:79], v[88:91], v[184:187], v[64:79]\n" \
"ds_read_b128 v[88:91], v0 offset:26624\n" \
"v_mul_f32 v17, v9, v17\n" \
"v_mul_f32 v18, v9, v18\n" \
"v_mul_f32 v19, v9, v19\n" \
"v_mul_f32 v20, v9, v20\n" \
"v_mul_f32 v21, v9, v21\n" \
"v_mfma_f32_32x32x16_bf16 v[64:79], v[92:95], v[188:191], v[64:79]\n" \
"ds_read_b128 v[92:95], v0 offset:27648\n" \
"v_mul_f32 v22, v9, v22\n" \
"v_mul_f32 v23, v9, v23\n" \
"v_fma_f32 v48, v48, v8, v16\n" \
"v_fma_f32 v49, v49, v8, v17\n" \
"v_fma_f32 v50, v50, v8, v18\n" \
"s_waitcnt lgkmcnt(2)\n" \
"v_mfma_f32_32x32x16_bf16 v[64:79], v[80:83], v[192:195], v[64:79]\n" \
"ds_read_b128 v[80:83], v0 offset:28672\n" \
"v_fma_f32 v51, v51, v8, v19\n" \
"v_fma_f32 v52, v52, v8, v20\n" \
"v_fma_f32 v53, v53, v8, v21\n" \
"v_fma_f32 v54, v54, v8, v22\n" \
"v_fma_f32 v55, v55, v8, v23\n" \
"v_mfma_f32_32x32x16_bf16 v[64:79], v[84:87], v[196:199], v[64:79]\n" \
"ds_read_b128 v[84:87], v0 offset:29696\n" \
"v_cvt_pk_bf16_f32 v48, v48, v49\n" \
"v_cvt_pk_bf16_f32 v49, v50, v51\n" \
"v_cvt_pk_bf16_f32 v50, v52, v53\n" \
"v_cvt_pk_bf16_f32 v51, v54, v55\n" \
"v_bfi_b32 v48, 1, s24, v48\n" \
"s_waitcnt lgkmcnt(2)\n" \
"v_mfma_f32_32x32x16_bf16 v[64:79], v[88:91], v[200:203], v[64:79]\n" \
"ds_read_b128 v[88:91], v0 offset:30720\n" \
"buffer_store_dwordx4 v[48:51], v0, %0, s40 offen offset:2048 sc1\n" \
"ds_write_b128 v4, v[32:35] offset:20480\n" \
"ds_write_b128 v4, v[36:39] offset:21504\n" \
"v_mfma_f32_32x32x16_bf16 v[64:79], v[92:95], v[204:207], v[64:79]\n" \
"ds_read_b128 v[92:95], v0 offset:31744\n" \
"ds_write_b64 v5, v[48:49] offset:22528\n" \
"v_bfe_u32 v13, v0, 8, 1\n" \
"v_bfe_u32 v14, v0, 6, 1\n" \
"v_lshl_add_u32 v13, v13, 1, v14\n" \
"s_waitcnt lgkmcnt(5)\n" \
"v_mfma_f32_32x32x16_bf16 v[64:79], v[80:83], v[208:211], v[64:79]\n" \
"ds_read_b128 v[80:83], v0 offset:32768\n" \
"v_mul_u32_u24 v13, 656, v13\n" \
"v_bfe_u32 v14, v0, 3, 3\n" \
"v_bfe_u32 v12, v0, 7, 1\n" \
"v_lshl_add_u32 v14, v12, 3, v14\n" \
"v_mfma_f32_32x32x16_bf16 v[64:79], v[84:87], v[212:215], v[64:79]\n" \
"ds_read_b128 v[84:87], v0 offset:33792\n" \
"v_add3_u32 v13, v13, v14, s23\n" \
"v_add_u32 v14, 576, v13\n" \
"v_cndmask_b32 v13, v13, v14, s[30:31]\n" \
"ds_write_b16 v13, v50 offset:20992\n" \
"s_waitcnt lgkmcnt(4)\n" \
"v_mfma_f32_32x32x16_bf16 v[64:79], v[88:91], v[216:219], v[64:79]\n" \
"ds_read_b128 v[88:91], v0 offset:34816\n" \
"ds_write_b16_d16_hi v13, v50 offset:21008\n" \
"ds_write_b16 v13, v51 offset:21024\n" \
"ds_write_b16_d16_hi v13, v51 offset:21040\n" \
"v_mul_f32 v24, v11, v24\n" \
"v_mfma_f32_32x32x16_bf16 v[64:79], v[92:95], v[220:223], v[64:79]\n" \
"ds_read_b128 v[92:95], v0 offset:35840\n" \
"v_mul_f32 v25, v11, v25\n" \
"v_mul_f32 v26, v11, v26\n" \
"v_mul_f32 v27, v11, v27\n" \
"v_fma_f32 v56, v56, v10, v24\n" \
"s_waitcnt lgkmcnt(6)\n" \
"v_mfma_f32_32x32x16_bf16 v[64:79], v[80:83], v[224:227], v[64:79]\n" \
"ds_read_b128 v[80:83], v0 offset:36864\n" \
"v_fma_f32 v57, v57, v10, v25\n" \
"v_fma_f32 v58, v58, v10, v26\n" \
"v_fma_f32 v59, v59, v10, v27\n" \
"v_cvt_pk_bf16_f32 v52, v56, v57\n" \
"v_mfma_f32_32x32x16_bf16 v[64:79], v[84:87], v[228:231], v[64:79]\n" \
"ds_read_b128 v[84:87], v0 offset:37888\n" \
"v_cvt_pk_bf16_f32 v53, v58, v59\n" \
"v_bfe_u32 v13, v0, 1, 8\n" \
"v_and_b32 v14, 0x200, v0\n" \
"v_lshl_add_u32 v13, v14, 1, v13\n" \
"s_waitcnt lgkmcnt(2)\n" \
"v_mfma_f32_32x32x16_bf16 v[64:79], v[88:91], v[232:235], v[64:79]\n" \
"ds_read_b128 v[88:91], v0 offset:38912\n" \
"v_add_u32 v13, s66, v13\n" \
"ds_write_b64 v13, v[52:53]\n" \
"ds_read_b128 v[16:19], v7 offset:384\n" \
"ds_read_b128 v[20:23], v7 offset:416\n" \
"v_mfma_f32_32x32x16_bf16 v[64:79], v[92:95], v[236:239], v[64:79]\n" \
"ds_read_b128 v[92:95], v0 offset:39936\n" \
"ds_read_b128 v[24:27], v7 offset:448\n" \
"ds_read_b128 v[28:31], v7 offset:480\n" \
"s_waitcnt lgkmcnt(7)\n" \
"v_mfma_f32_32x32x16_bf16 v[64:79], v[80:83], v[240:243], v[64:79]\n" \
"ds_read_b128 v[80:83], v0 offset:40960\n" \
"v_mfma_f32_32x32x16_bf16 v[64:79], v[84:87], v[244:247], v[64:79]\n" \
"ds_read_b128 v[84:87], v0 offset:41984\n" \
"s_waitcnt lgkmcnt(4)\n" \
"v_mfma_f32_32x32x16_bf16 v[64:79], v[88:91], v[248:251], v[64:79]\n" \
"ds_read_b128 v[88:91], v0 offset:43008\n" \
"v_mul_f32 v16, v11, v16\n" \
"v_mul_f32 v17, v11, v17\n" \
"v_mul_f32 v18, v11, v18\n" \
"v_mul_f32 v19, v11, v19\n" \
"v_mfma_f32_32x32x16_bf16 v[64:79], v[92:95], v[252:255], v[64:79]\n" \
"ds_read_b128 v[92:95], v0 offset:44032\n" \
"v_mul_f32 v20, v11, v20\n" \
"v_mul_f32 v21, v11, v21\n" \
"v_mul_f32 v22, v11, v22\n" \
"v_mul_f32 v23, v11, v23\n" \
"s_barrier\n" \
"s_waitcnt lgkmcnt(2)\n" \
"v_mfma_f32_32x32x16_bf16 v[32:47], v[96:99], v[80:83], 0\n" \
"ds_read_b128 v[80:83], v0 offset:45056\n" \
"v_mul_f32 v24, v11, v24\n" \
"v_mul_f32 v25, v11, v25\n" \
"v_mul_f32 v26, v11, v26\n" \
"v_mul_f32 v27, v11, v27\n" \
"v_mfma_f32_32x32x16_bf16 v[32:47], v[100:103], v[84:87], v[32:47]\n" \
"ds_read_b128 v[84:87], v0 offset:46080\n" \
"v_mul_f32 v28, v11, v28\n" \
"v_mul_f32 v29, v11, v29\n" \
"v_mul_f32 v30, v11, v30\n" \
"v_mul_f32 v31, v11, v31\n" \
"s_waitcnt lgkmcnt(2)\n" \
"v_mfma_f32_32x32x16_bf16 v[32:47], v[104:107], v[88:91], v[32:47]\n" \
"ds_read_b128 v[88:91], v0 offset:47104\n" \
"v_fma_f32 v64, v64, v10, v16\n" \
"v_fma_f32 v65, v65, v10, v17\n" \
"v_fma_f32 v66, v66, v10, v18\n" \
"v_mfma_f32_32x32x16_bf16 v[32:47], v[108:111], v[92:95], v[32:47]\n" \
"ds_read_b128 v[92:95], v0 offset:48128\n" \
"v_fma_f32 v67, v67, v10, v19\n" \
"v_fma_f32 v68, v68, v10, v20\n" \
"v_fma_f32 v69, v69, v10, v21\n" \
"v_fma_f32 v70, v70, v10, v22\n" \
"s_waitcnt lgkmcnt(2)\n" \
"v_mfma_f32_32x32x16_bf16 v[32:47], v[112:115], v[80:83], v[32:47]\n" \
"ds_read_b128 v[80:83], v0 offset:49152\n" \
"v_fma_f32 v71, v71, v10, v23\n" \
"v_fma_f32 v72, v72, v10, v24\n" \
"v_fma_f32 v73, v73, v10, v25\n" \
"v_mfma_f32_32x32x16_bf16 v[32:47], v[116:119], v[84:87], v[32:47]\n" \
"ds_read_b128 v[84:87], v0 offset:50176\n" \
"v_fma_f32 v74, v74, v10, v26\n" \
"v_fma_f32 v75, v75, v10, v27\n" \
"v_fma_f32 v76, v76, v10, v28\n" \
"v_fma_f32 v77, v77, v10, v29\n" \
"s_waitcnt lgkmcnt(2)\n" \
"v_mfma_f32_32x32x16_bf16 v[32:47], v[120:123], v[88:91], v[32:47]\n" \
"ds_read_b128 v[88:91], v0 offset:51200\n" \
"v_fma_f32 v78, v78, v10, v30\n" \
"v_fma_f32 v79, v79, v10, v31\n" \
"v_cvt_pk_bf16_f32 v16, v64, v65\n" \
"v_mfma_f32_32x32x16_bf16 v[32:47], v[124:127], v[92:95], v[32:47]\n" \
"ds_read_b128 v[92:95], v0 offset:52224\n" \
"v_cvt_pk_bf16_f32 v17, v66, v67\n" \
"v_cvt_pk_bf16_f32 v18, v68, v69\n" \
"v_cvt_pk_bf16_f32 v19, v70, v71\n" \
"v_cvt_pk_bf16_f32 v64, v72, v73\n" \
"s_waitcnt lgkmcnt(2)\n" \
"v_mfma_f32_32x32x16_bf16 v[32:47], v[128:131], v[80:83], v[32:47]\n" \
"ds_read_b128 v[80:83], v0 offset:53248\n" \
"v_cvt_pk_bf16_f32 v65, v74, v75\n" \
"v_cvt_pk_bf16_f32 v66, v76, v77\n" \
"v_cvt_pk_bf16_f32 v67, v78, v79\n" \
"v_mfma_f32_32x32x16_bf16 v[32:47], v[132:135], v[84:87], v[32:47]\n" \
"ds_read_b128 v[84:87], v0 offset:54272\n" \
"v_add_u32 v13, s66, v0\n" \
"ds_write_b128 v13, v[64:67]\n" \
"v_cndmask_b32 v20, 0, v52, s[30:31]\n" \
"v_cndmask_b32 v21, 0, v53, s[30:31]\n" \
"s_waitcnt lgkmcnt(3)\n" \
"v_mfma_f32_32x32x16_bf16 v[32:47], v[136:139], v[88:91], v[32:47]\n" \
"ds_read_b128 v[88:91], v0 offset:55296\n" \
"v_add_u32 v96, s65, v7\n" \
"v_subrev_u32 v96, 160512, v96\n" \
"v_bfe_u32 v97, v0, 2, 7\n" \
"v_mfma_f32_32x32x16_bf16 v[32:47], v[140:143], v[92:95], v[32:47]\n" \
"ds_read_b128 v[92:95], v0 offset:56320\n" \
"v_add_u32 v97, 160640, v97\n" \
"ds_read_b32 v97, v97\n" \
"ds_read_b128 v[112:115], v96 offset:0\n" \
"ds_read_b128 v[116:119], v96 offset:32\n" \
"s_waitcnt lgkmcnt(6)\n" \
"v_mfma_f32_32x32x16_bf16 v[32:47], v[144:147], v[80:83], v[32:47]\n" \
"ds_read_b128 v[80:83], v0 offset:57344\n" \
"ds_read_b128 v[120:123], v96 offset:64\n" \
"ds_read_b128 v[124:127], v96 offset:96\n" \
"ds_read_b128 v[128:131], v96 offset:128\n" \
"v_mfma_f32_32x32x16_bf16 v[32:47], v[148:151], v[84:87], v[32:47]\n" \
"ds_read_b128 v[84:87], v0 offset:58368\n" \
"ds_read_b128 v[132:135], v96 offset:160\n" \
"ds_read_b128 v[136:139], v96 offset:192\n" \
"ds_read_b128 v[140:143], v96 offset:224\n" \
"v_and_b32 v14, 0xf0, v0\n" \
"s_waitcnt lgkmcnt(11)\n" \
"v_mfma_f32_32x32x16_bf16 v[32:47], v[152:155], v[88:91], v[32:47]\n" \
"ds_read_b128 v[88:91], v0 offset:59392\n" \
"v_lshrrev_b32 v13, 9, v0\n" \
"v_lshl_add_u32 v14, v13, 8, v14\n" \
"v_bfe_u32 v13, v0, 8, 1\n" \
"v_mfma_f32_32x32x16_bf16 v[32:47], v[156:159], v[92:95], v[32:47]\n" \
"ds_read_b128 v[92:95], v0 offset:60416\n" \
"v_lshl_add_u32 v14, v13, 3, v14\n" \
"buffer_load_dwordx4 v[100:103], v0, %0, s49 offen sc1\n" \
"s_waitcnt lgkmcnt(5)\n" \
"v_mfma_f32_32x32x16_bf16 v[32:47], v[160:163], v[80:83], v[32:47]\n" \
"ds_read_b128 v[80:83], v1 offset:0\n" \
"buffer_load_dwordx4 v[104:107], v0, %0, s49 offen offset:1024 sc1\n" \
"v_mfma_f32_32x32x16_bf16 v[32:47], v[164:167], v[84:87], v[32:47]\n" \
"ds_read_b128 v[84:87], v1 offset:1024\n" \
"buffer_load_dwordx2 v[108:109], v14, %0, s49 offen offset:2048 sc1\n" \
"v_mul_f32 v112, v97, v112\n" \
"v_mul_f32 v113, v97, v113\n" \
"s_waitcnt lgkmcnt(2)\n" \
"v_mfma_f32_32x32x16_bf16 v[32:47], v[168:171], v[88:91], v[32:47]\n" \
"ds_read_b128 v[88:91], v1 offset:2048\n" \
"v_mul_f32 v114, v97, v114\n" \
"v_mul_f32 v115, v97, v115\n" \
"v_mul_f32 v116, v97, v116\n" \
"v_mfma_f32_32x32x16_bf16 v[32:47], v[172:175], v[92:95], v[32:47]\n" \
"ds_read_b128 v[92:95], v1 offset:3072\n" \
"v_mul_f32 v117, v97, v117\n" \
"v_mul_f32 v118, v97, v118\n" \
"v_mul_f32 v119, v97, v119\n" \
"v_mul_f32 v120, v97, v120\n" \
"s_waitcnt lgkmcnt(2)\n" \
"v_mfma_f32_32x32x16_bf16 v[32:47], v[176:179], v[80:83], v[32:47]\n" \
"ds_read_b128 v[80:83], v1 offset:4096\n" \
"v_mul_f32 v121, v97, v121\n" \
"v_mul_f32 v122, v97, v122\n" \
"v_mul_f32 v123, v97, v123\n" \
"v_mfma_f32_32x32x16_bf16 v[32:47], v[180:183], v[84:87], v[32:47]\n" \
"ds_read_b128 v[84:87], v1 offset:5120\n" \
"v_mul_f32 v124, v97, v124\n" \
"v_mul_f32 v125, v97, v125\n" \
"v_mul_f32 v126, v97, v126\n" \
"v_mul_f32 v127, v97, v127\n" \
"s_waitcnt lgkmcnt(2)\n" \
"v_mfma_f32_32x32x16_bf16 v[32:47], v[184:187], v[88:91], v[32:47]\n" \
"ds_read_b128 v[88:91], v1 offset:6144\n" \
"v_lshrrev_b32 v13, 9, v0\n" \
"v_mul_u32_u24 v13, 144, v13\n" \
"v_add3_u32 v98, v13, v0, s23\n" \
"v_mfma_f32_32x32x16_bf16 v[32:47], v[188:191], v[92:95], v[32:47]\n" \
"ds_read_b128 v[92:95], v1 offset:7168\n" \
"v_bfe_u32 v28, v0, 4, 5\n" \
"v_add_u32 v6, 4, v28\n" \
"v_add_u32 v29, 20, v28\n" \
"s_waitcnt lgkmcnt(2)\n" \
"v_mfma_f32_32x32x16_bf16 v[32:47], v[192:195], v[80:83], v[32:47]\n" \
"ds_read_b128 v[80:83], v1 offset:8192\n" \
"v_cmp_gt_u32 vcc, 16, v28\n" \
"s_nop 1\n" \
"v_cndmask_b32 v6, v29, v6, vcc\n" \
"v_cmp_gt_u32 vcc, 20, v28\n" \
"v_mfma_f32_32x32x16_bf16 v[32:47], v[196:199], v[84:87], v[32:47]\n" \
"ds_read_b128 v[84:87], v1 offset:9216\n" \
"v_cndmask_b32 v6, 40, v6, vcc\n" \
"v_subrev_u32 v29, 12, v29\n" \
"v_cmp_gt_u32 vcc, 28, v28\n" \
"s_waitcnt lgkmcnt(2)\n" \
"v_mfma_f32_32x32x16_bf16 v[32:47], v[200:203], v[88:91], v[32:47]\n" \
"ds_read_b128 v[88:91], v1 offset:10240\n" \
"v_cndmask_b32 v6, v29, v6, vcc\n" \
"v_lshrrev_b32 v28, 9, v0\n" \
"v_mad_u32_u24 v6, v28, 41, v6\n" \
"v_mfma_f32_32x32x16_bf16 v[32:47], v[204:207], v[92:95], v[32:47]\n" \
"ds_read_b128 v[92:95], v1 offset:11264\n" \
"v_lshl_add_u32 v6, v6, 4, s23\n" \
"v_and_b32 v10, 0xf0, v0\n" \
"v_lshl_add_u32 v9, v28, 8, v10\n" \
"s_waitcnt lgkmcnt(2)\n" \
"v_mfma_f32_32x32x16_bf16 v[32:47], v[208:211], v[80:83], v[32:47]\n" \
"ds_read_b128 v[80:83], v1 offset:12288\n" \
"v_lshl_add_u32 v10, v28, 31, v10\n" \
"v_bfe_u32 v28, v0, 8, 1\n" \
"v_lshl_add_u32 v9, v28, 14, v9\n" \
"v_mfma_f32_32x32x16_bf16 v[32:47], v[212:215], v[84:87], v[32:47]\n" \
"ds_read_b128 v[84:87], v1 offset:13312\n" \
"v_lshl_add_u32 v10, v28, 12, v10\n" \
"s_waitcnt lgkmcnt(2)\n" \
"v_mfma_f32_32x32x16_bf16 v[32:47], v[216:219], v[88:91], v[32:47]\n" \
"ds_read_b128 v[88:91], v1 offset:14336\n" \
"v_mfma_f32_32x32x16_bf16 v[32:47], v[220:223], v[92:95], v[32:47]\n" \
"ds_read_b128 v[92:95], v1 offset:15360\n" \
"s_waitcnt lgkmcnt(2)\n" \
"v_mfma_f32_32x32x16_bf16 v[32:47], v[224:227], v[80:83], v[32:47]\n" \
"ds_read_b128 v[80:83], v1 offset:16384\n" \
"v_mfma_f32_32x32x16_bf16 v[32:47], v[228:231], v[84:87], v[32:47]\n" \
"ds_read_b128 v[84:87], v1 offset:17408\n" \
"s_waitcnt lgkmcnt(2)\n" \
"v_mfma_f32_32x32x16_bf16 v[32:47], v[232:235], v[88:91], v[32:47]\n" \
"ds_read_b128 v[88:91], v1 offset:18432\n" \
"v_mfma_f32_32x32x16_bf16 v[32:47], v[236:239], v[92:95], v[32:47]\n" \
"ds_read_b128 v[92:95], v1 offset:19456\n" \
"s_waitcnt lgkmcnt(2)\n" \
"v_mfma_f32_32x32x16_bf16 v[32:47], v[240:243], v[80:83], v[32:47]\n" \
"v_mfma_f32_32x32x16_bf16 v[32:47], v[244:247], v[84:87], v[32:47]\n" \
"s_waitcnt lgkmcnt(0)\n" \
"v_mfma_f32_32x32x16_bf16 v[32:47], v[248:251], v[88:91], v[32:47]\n" \
"v_mfma_f32_32x32x16_bf16 v[32:47], v[252:255], v[92:95], v[32:47]\n" \
"s_nop 11\n" \
"v_fma_f32 v32, v32, v128, v112\n" \
"v_fma_f32 v33, v33, v129, v113\n" \
"v_fma_f32 v34, v34, v130, v114\n" \
"v_fma_f32 v35, v35, v131, v115\n" \
"v_fma_f32 v36, v36, v132, v116\n" \
"v_fma_f32 v37, v37, v133, v117\n" \
"v_fma_f32 v38, v38, v134, v118\n" \
"v_fma_f32 v39, v39, v135, v119\n" \
"v_fma_f32 v40, v40, v136, v120\n" \
"v_fma_f32 v41, v41, v137, v121\n" \
"v_fma_f32 v42, v42, v138, v122\n" \
"v_fma_f32 v43, v43, v139, v123\n" \
"v_fma_f32 v44, v44, v140, v124\n" \
"v_fma_f32 v45, v45, v141, v125\n" \
"v_fma_f32 v46, v46, v142, v126\n" \
"v_fma_f32 v47, v47, v143, v127\n" \
"v_cvt_pk_bf16_f32 v32, v32, v33\n" \
"v_cvt_pk_bf16_f32 v33, v34, v35\n" \
"v_cvt_pk_bf16_f32 v34, v36, v37\n" \
"v_cvt_pk_bf16_f32 v35, v38, v39\n" \
"v_bfi_b32 v32, 1, s24, v32\n" \
"buffer_store_dwordx4 v[32:35], v0, %0, s40 offen offset:3072 sc1\n" \
"v_cvt_pk_bf16_f32 v36, v40, v41\n" \
"v_cvt_pk_bf16_f32 v37, v42, v43\n" \
"v_cvt_pk_bf16_f32 v38, v44, v45\n" \
"v_cvt_pk_bf16_f32 v39, v46, v47\n" \
"v_bfi_b32 v36, 1, s24, v36\n" \
"buffer_store_dwordx4 v[36:39], v0, %0, s41 offen offset:0 sc1\n" \
"ds_write_b128 v98, v[32:35] offset:21056\n" \
"ds_write_b128 v98, v[36:39] offset:22368\n" \
"v_mov_b32 v27, 0xffff\n" \
"v_cndmask_b32 v88, 0, v27, s[68:69]\n" \
"v_cndmask_b32 v89, 0, v27, s[72:73]\n" \
"v_cndmask_b32 v90, 0, v27, s[76:77]\n" \
"v_cndmask_b32 v91, 0, v27, s[80:81]\n" \
"v_cndmask_b32 v92, 0, v27, s[84:85]\n" \
"v_cndmask_b32 v93, 0, v27, s[88:89]\n" \
"v_cndmask_b32 v94, 0, v27, s[92:93]\n" \
"v_cndmask_b32 v95, 0, v27, s[96:97]\n" \
"v_cndmask_b32 v88, v88, -1, s[70:71]\n" \
"v_cndmask_b32 v89, v89, -1, s[74:75]\n" \
"v_cndmask_b32 v90, v90, -1, s[78:79]\n" \
"v_cndmask_b32 v91, v91, -1, s[82:83]\n" \
"v_cndmask_b32 v92, v92, -1, s[86:87]\n" \
"v_cndmask_b32 v93, v93, -1, s[90:91]\n" \
"v_cndmask_b32 v94, v94, -1, s[94:95]\n" \
"v_cndmask_b32 v95, v95, -1, s[98:99]\n" \
"s_waitcnt lgkmcnt(0)\n" \
"s_barrier\n" \
"ds_read_b128 v[56:59], v4 offset:20480\n" \
"ds_read_b64 v[60:61], v5 offset:22528\n" \
"ds_read_b128 v[62:65], v4 offset:0\n" \
"ds_read_b64 v[66:67], v5 offset:2048\n" \
"ds_read_b128 v[68:71], v4 offset:2560\n" \
"ds_read_b64 v[72:73], v5 offset:4608\n" \
"s_mov_b32 m0, s60\n" \
"s_add_u32 s25, s59, 4096\n" \
"s_add_u32 s26, s59, 8192\n" \
"s_setprio 0\n" \
"v_mov_b32 v22, 0xbf00bf00\n" \
"v_mov_b32 v23, 0xbf00bf00\n" \
"s_waitcnt lgkmcnt(5)\n" \
"v_mfma_f32_32x32x16_bf16 v[240:255], v[56:59], v[16:19], 0\n" \
"s_waitcnt lgkmcnt(4)\n" \
"v_mfma_f32_32x32x8_bf16 v[240:255], v[60:61], v[20:21], v[240:255]\n" \
"ds_read_b128 v[56:59], v4 offset:5120\n" \
"ds_read_b64 v[60:61], v5 offset:7168\n" \
"s_waitcnt lgkmcnt(5)\n" \
"v_mfma_f32_32x32x16_bf16 v[112:127], v[62:65], v[16:19], 0\n" \
"s_waitcnt lgkmcnt(4)\n" \
"v_mfma_f32_32x32x8_bf16 v[112:127], v[66:67], v[20:21], v[112:127]\n" \
"ds_read_b128 v[62:65], v4 offset:7680\n" \
"ds_read_b64 v[66:67], v5 offset:9728\n" \
"s_waitcnt lgkmcnt(5)\n" \
"v_mfma_f32_32x32x16_bf16 v[128:143], v[68:71], v[16:19], 0\n" \
"s_waitcnt lgkmcnt(4)\n" \
"v_mfma_f32_32x32x8_bf16 v[128:143], v[72:73], v[20:21], v[128:143]\n" \
"ds_read_b128 v[68:71], v4 offset:10240\n" \
"ds_read_b64 v[72:73], v5 offset:12288\n" \
"s_waitcnt lgkmcnt(5)\n" \
"v_mfma_f32_32x32x16_bf16 v[144:159], v[56:59], v[16:19], 0\n" \
"s_nop 1\n" \
"v_cndmask_b32 v112, v112, v240, s[68:69]\n" \
"v_cndmask_b32 v113, v113, v241, s[70:71]\n" \
"v_cndmask_b32 v114, v114, v242, s[72:73]\n" \
"v_cndmask_b32 v115, v115, v243, s[74:75]\n" \
"v_cndmask_b32 v116, v116, v244, s[76:77]\n" \
"v_cndmask_b32 v117, v117, v245, s[78:79]\n" \
"s_waitcnt lgkmcnt(4)\n" \
"v_mfma_f32_32x32x8_bf16 v[144:159], v[60:61], v[20:21], v[144:159]\n" \
"ds_read_b128 v[56:59], v4 offset:12800\n" \
"ds_read_b64 v[60:61], v5 offset:14848\n" \
"v_cndmask_b32 v118, v118, v246, s[80:81]\n" \
"v_cndmask_b32 v119, v119, v247, s[82:83]\n" \
"v_cndmask_b32 v120, v120, v248, s[84:85]\n" \
"v_cndmask_b32 v121, v121, v249, s[86:87]\n" \
"v_cndmask_b32 v122, v122, v250, s[88:89]\n" \
"v_cndmask_b32 v123, v123, v251, s[90:91]\n" \
"s_waitcnt lgkmcnt(5)\n" \
"v_mfma_f32_32x32x16_bf16 v[160:175], v[62:65], v[16:19], 0\n" \
"v_cndmask_b32 v124, v124, v252, s[92:93]\n" \
"v_cndmask_b32 v125, v125, v253, s[94:95]\n" \
"v_cndmask_b32 v126, v126, v254, s[96:97]\n" \
"v_cndmask_b32 v127, v127, v255, s[98:99]\n" \
"v_max_f32 v26, v112, v113\n" \
"v_max_f32 v27, v114, v115\n" \
"s_waitcnt lgkmcnt(4)\n" \
"v_mfma_f32_32x32x8_bf16 v[160:175], v[66:67], v[20:21], v[160:175]\n" \
"ds_read_b128 v[62:65], v4 offset:15360\n" \
"ds_read_b64 v[66:67], v5 offset:17408\n" \
"v_max3_f32 v26, v116, v117, v26\n" \
"v_max3_f32 v27, v118, v119, v27\n" \
"v_max3_f32 v26, v120, v121, v26\n" \
"v_max3_f32 v27, v122, v123, v27\n" \
"v_max3_f32 v26, v124, v125, v26\n" \
"v_max3_f32 v27, v126, v127, v27\n" \
"s_waitcnt lgkmcnt(5)\n" \
"v_mfma_f32_32x32x16_bf16 v[176:191], v[68:71], v[16:19], 0\n" \
"v_max3_f32 v26, v128, v129, v26\n" \
"v_max3_f32 v27, v130, v131, v27\n" \
"v_max3_f32 v26, v132, v133, v26\n" \
"v_max3_f32 v27, v134, v135, v27\n" \
"v_max3_f32 v26, v136, v137, v26\n" \
"s_waitcnt lgkmcnt(4)\n" \
"v_mfma_f32_32x32x8_bf16 v[176:191], v[72:73], v[20:21], v[176:191]\n" \
"ds_read_b128 v[68:71], v4 offset:17920\n" \
"ds_read_b64 v[72:73], v5 offset:19968\n" \
"v_max3_f32 v27, v138, v139, v27\n" \
"v_max3_f32 v26, v140, v141, v26\n" \
"v_max3_f32 v27, v142, v143, v27\n" \
"v_max3_f32 v26, v144, v145, v26\n" \
"v_max3_f32 v27, v146, v147, v27\n" \
"s_waitcnt lgkmcnt(5)\n" \
"v_mfma_f32_32x32x16_bf16 v[192:207], v[56:59], v[16:19], 0\n" \
"v_max3_f32 v26, v148, v149, v26\n" \
"v_max3_f32 v27, v150, v151, v27\n" \
"v_max3_f32 v26, v152, v153, v26\n" \
"v_max3_f32 v27, v154, v155, v27\n" \
"v_max3_f32 v26, v156, v157, v26\n" \
"s_waitcnt lgkmcnt(4)\n" \
"v_mfma_f32_32x32x8_bf16 v[192:207], v[60:61], v[20:21], v[192:207]\n" \
"v_max3_f32 v27, v158, v159, v27\n" \
"v_max3_f32 v26, v160, v161, v26\n" \
"v_max3_f32 v27, v162, v163, v27\n" \
"v_max3_f32 v26, v164, v165, v26\n" \
"v_max3_f32 v27, v166, v167, v27\n" \
"s_waitcnt lgkmcnt(3)\n" \
"v_mfma_f32_32x32x16_bf16 v[208:223], v[62:65], v[16:19], 0\n" \
"v_max3_f32 v26, v168, v169, v26\n" \
"v_max3_f32 v27, v170, v171, v27\n" \
"v_max3_f32 v26, v172, v173, v26\n" \
"v_max3_f32 v27, v174, v175, v27\n" \
"v_max3_f32 v26, v176, v177, v26\n" \
"s_waitcnt lgkmcnt(2)\n" \
"v_mfma_f32_32x32x8_bf16 v[208:223], v[66:67], v[20:21], v[208:223]\n" \
"v_max3_f32 v27, v178, v179, v27\n" \
"v_max3_f32 v26, v180, v181, v26\n" \
"v_max3_f32 v27, v182, v183, v27\n" \
"v_max3_f32 v26, v184, v185, v26\n" \
"v_max3_f32 v27, v186, v187, v27\n" \
"s_waitcnt lgkmcnt(1)\n" \
"v_mfma_f32_32x32x16_bf16 v[224:239], v[68:71], v[16:19], 0\n" \
"v_max3_f32 v26, v188, v189, v26\n" \
"v_max3_f32 v27, v190, v191, v27\n" \
"v_max3_f32 v26, v192, v193, v26\n" \
"v_max3_f32 v27, v194, v195, v27\n" \
"v_max3_f32 v26, v196, v197, v26\n" \
"s_waitcnt lgkmcnt(0)\n" \
"v_mfma_f32_32x32x8_bf16 v[224:239], v[72:73], v[20:21], v[224:239]\n" \
"v_max3_f32 v27, v198, v199, v27\n" \
"v_max3_f32 v26, v200, v201, v26\n" \
"v_max3_f32 v27, v202, v203, v27\n" \
"v_max3_f32 v26, v204, v205, v26\n" \
"v_max3_f32 v27, v206, v207, v27\n" \
"s_setprio 0\n" \
"v_max3_f32 v26, v208, v209, v26\n" \
"v_max3_f32 v27, v210, v211, v27\n" \
"v_max3_f32 v26, v212, v213, v26\n" \
"v_max3_f32 v27, v214, v215, v27\n" \
"v_max3_f32 v26, v216, v217, v26\n" \
"v_max3_f32 v27, v218, v219, v27\n" \
"v_max3_f32 v26, v220, v221, v26\n" \
"v_max3_f32 v27, v222, v223, v27\n" \
"v_max3_f32 v26, v224, v225, v26\n" \
"v_max3_f32 v27, v226, v227, v27\n" \
"v_max3_f32 v26, v228, v229, v26\n" \
"v_max3_f32 v27, v230, v231, v27\n" \
"v_max3_f32 v26, v232, v233, v26\n" \
"v_max3_f32 v27, v234, v235, v27\n" \
"v_max3_f32 v26, v236, v237, v26\n" \
"v_max3_f32 v27, v238, v239, v27\n" \
"v_max_f32 v26, v26, v27\n" \
"v_mov_b32 v27, v26\n" \
"s_nop 1\n" \
"v_permlane32_swap_b32 v26, v27\n" \
"v_max_f32 v26, v26, v27\n" \
"v_and_b32 v27, 0xffff0000, v26\n" \
"v_sub_f32 v28, v26, v27\n" \
"v_and_b32 v27, 0xffff0000, v28\n" \
"v_sub_f32 v25, v28, v27\n" \
"v_perm_b32 v24, v26, v28, s22\n" \
"s_nop 1\n" \
"v_mfma_f32_32x32x8_bf16 v[112:127], v[22:23], v[24:25], v[112:127]\n" \
"v_mfma_f32_32x32x8_bf16 v[128:143], v[22:23], v[24:25], v[128:143]\n" \
"s_barrier\n" \
"s_waitcnt vmcnt(4)\n" \
"v_lshlrev_b32 v32, 16, v100\n" \
"v_and_b32 v33, 0xffff0000, v100\n" \
"v_lshlrev_b32 v34, 16, v101\n" \
"v_and_b32 v35, 0xffff0000, v101\n" \
"v_lshlrev_b32 v36, 16, v102\n" \
"v_and_b32 v37, 0xffff0000, v102\n" \
"v_lshlrev_b32 v38, 16, v103\n" \
"v_and_b32 v39, 0xffff0000, v103\n" \
"s_waitcnt vmcnt(3)\n" \
"v_lshlrev_b32 v40, 16, v104\n" \
"v_and_b32 v41, 0xffff0000, v104\n" \
"v_lshlrev_b32 v42, 16, v105\n" \
"v_and_b32 v43, 0xffff0000, v105\n" \
"v_lshlrev_b32 v44, 16, v106\n" \
"v_and_b32 v45, 0xffff0000, v106\n" \
"v_lshlrev_b32 v46, 16, v107\n" \
"v_and_b32 v47, 0xffff0000, v107\n" \
"s_waitcnt vmcnt(2)\n" \
"v_lshlrev_b32 v48, 16, v108\n" \
"v_and_b32 v49, 0xffff0000, v108\n" \
"v_lshlrev_b32 v50, 16, v109\n" \
"v_and_b32 v51, 0xffff0000, v109\n" \
"ds_read_b128 v[56:59], v6 offset:0\n" \
"ds_read_b128 v[60:63], v6 offset:1312\n" \
"ds_read_b128 v[64:67], v6 offset:20992\n" \
"ds_read_b128 v[68:71], v6 offset:22304\n" \
"v_exp_f32 v112, v112\n" \
"v_exp_f32 v113, v113\n" \
"v_mfma_f32_32x32x8_bf16 v[144:159], v[22:23], v[24:25], v[144:159]\n" \
"v_exp_f32 v114, v114\n" \
"v_cvt_pk_bf16_f32 v112, v112, v113\n" \
"s_add_u32 m0, s60, 0\n" \
"s_nop 0\n" \
"buffer_load_dwordx4 v0, %0, s59 offen offset:0 sc1 lds\n" \
"v_exp_f32 v115, v115\n" \
"v_exp_f32 v116, v116\n" \
"v_cvt_pk_bf16_f32 v113, v114, v115\n" \
"v_exp_f32 v117, v117\n" \
"v_exp_f32 v118, v118\n" \
"v_cvt_pk_bf16_f32 v114, v116, v117\n" \
"v_exp_f32 v119, v119\n" \
"v_exp_f32 v120, v120\n" \
"v_cvt_pk_bf16_f32 v115, v118, v119\n" \
"v_exp_f32 v121, v121\n" \
"s_add_u32 m0, s60, 0\n" \
"s_nop 0\n" \
"buffer_load_dwordx4 v0, %0, s59 offen offset:1024 sc1 lds\n" \
"v_exp_f32 v122, v122\n" \
"v_cvt_pk_bf16_f32 v116, v120, v121\n" \
"v_exp_f32 v123, v123\n" \
"v_exp_f32 v124, v124\n" \
"v_cvt_pk_bf16_f32 v117, v122, v123\n" \
"v_exp_f32 v125, v125\n" \
"v_exp_f32 v126, v126\n" \
"v_cvt_pk_bf16_f32 v118, v124, v125\n" \
"v_exp_f32 v127, v127\n" \
"s_nop 0\n" \
"v_cvt_pk_bf16_f32 v119, v126, v127\n" \
"v_and_b32 v240, v112, v88\n" \
"v_and_b32 v241, v113, v89\n" \
"v_and_b32 v242, v114, v90\n" \
"v_and_b32 v243, v115, v91\n" \
"v_and_b32 v244, v116, v92\n" \
"v_and_b32 v245, v117, v93\n" \
"v_and_b32 v246, v118, v94\n" \
"v_and_b32 v247, v119, v95\n" \
"v_xor_b32 v112, v112, v240\n" \
"v_xor_b32 v113, v113, v241\n" \
"v_xor_b32 v114, v114, v242\n" \
"v_xor_b32 v115, v115, v243\n" \
"v_xor_b32 v116, v116, v244\n" \
"v_xor_b32 v117, v117, v245\n" \
"v_xor_b32 v118, v118, v246\n" \
"v_xor_b32 v119, v119, v247\n" \
"v_exp_f32 v128, v128\n" \
"s_add_u32 m0, s60, 0\n" \
"s_nop 0\n" \
"buffer_load_dwordx4 v0, %0, s59 offen offset:2048 sc1 lds\n" \
"v_exp_f32 v129, v129\n" \
"v_mfma_f32_32x32x8_bf16 v[160:175], v[22:23], v[24:25], v[160:175]\n" \
"v_exp_f32 v130, v130\n" \
"v_cvt_pk_bf16_f32 v128, v128, v129\n" \
"v_exp_f32 v131, v131\n" \
"s_waitcnt lgkmcnt(3)\n" \
"v_mfma_f32_32x32x16_bf16 v[72:87], v[56:59], v[112:115], 0\n" \
"ds_read_b128 v[56:59], v6 offset:2624\n" \
"v_exp_f32 v132, v132\n" \
"v_cvt_pk_bf16_f32 v129, v130, v131\n" \
"v_exp_f32 v133, v133\n" \
"v_exp_f32 v134, v134\n" \
"v_cvt_pk_bf16_f32 v130, v132, v133\n" \
"s_waitcnt lgkmcnt(3)\n" \
"v_mfma_f32_32x32x16_bf16 v[72:87], v[60:63], v[116:119], v[72:87]\n" \
"ds_read_b128 v[60:63], v6 offset:3936\n" \
"v_exp_f32 v135, v135\n" \
"s_add_u32 m0, s60, 0\n" \
"s_nop 0\n" \
"buffer_load_dwordx4 v0, %0, s59 offen offset:3072 sc1 lds\n" \
"v_exp_f32 v136, v136\n" \
"v_cvt_pk_bf16_f32 v131, v134, v135\n" \
"v_exp_f32 v137, v137\n" \
"s_waitcnt lgkmcnt(3)\n" \
"v_mfma_f32_32x32x16_bf16 v[72:87], v[64:67], v[240:243], v[72:87]\n" \
"ds_read_b128 v[64:67], v6 offset:5248\n" \
"v_exp_f32 v138, v138\n" \
"v_cvt_pk_bf16_f32 v132, v136, v137\n" \
"v_exp_f32 v139, v139\n" \
"v_exp_f32 v140, v140\n" \
"v_cvt_pk_bf16_f32 v133, v138, v139\n" \
"s_waitcnt lgkmcnt(3)\n" \
"v_mfma_f32_32x32x16_bf16 v[72:87], v[68:71], v[244:247], v[72:87]\n" \
"ds_read_b128 v[68:71], v6 offset:6560\n" \
"v_exp_f32 v141, v141\n" \
"v_exp_f32 v142, v142\n" \
"v_cvt_pk_bf16_f32 v134, v140, v141\n" \
"s_add_u32 m0, s60, 4096\n" \
"s_nop 0\n" \
"buffer_load_dwordx4 v0, %0, s25 offen offset:0 sc1 lds\n" \
"v_exp_f32 v143, v143\n" \
"v_exp_f32 v144, v144\n" \
"v_cvt_pk_bf16_f32 v135, v142, v143\n" \
"v_exp_f32 v145, v145\n" \
"v_mfma_f32_32x32x8_bf16 v[176:191], v[22:23], v[24:25], v[176:191]\n" \
"v_exp_f32 v146, v146\n" \
"v_cvt_pk_bf16_f32 v144, v144, v145\n" \
"v_exp_f32 v147, v147\n" \
"s_waitcnt lgkmcnt(3)\n" \
"v_mfma_f32_32x32x16_bf16 v[72:87], v[56:59], v[128:131], v[72:87]\n" \
"ds_read_b128 v[56:59], v6 offset:7872\n" \
"v_exp_f32 v148, v148\n" \
"v_cvt_pk_bf16_f32 v145, v146, v147\n" \
"v_exp_f32 v149, v149\n" \
"s_add_u32 m0, s60, 4096\n" \
"s_nop 0\n" \
"buffer_load_dwordx4 v0, %0, s25 offen offset:1024 sc1 lds\n" \
"v_exp_f32 v150, v150\n" \
"v_cvt_pk_bf16_f32 v146, v148, v149\n" \
"s_waitcnt lgkmcnt(3)\n" \
"v_mfma_f32_32x32x16_bf16 v[72:87], v[60:63], v[132:135], v[72:87]\n" \
"ds_read_b128 v[60:63], v6 offset:9184\n" \
"v_exp_f32 v151, v151\n" \
"v_exp_f32 v152, v152\n" \
"v_cvt_pk_bf16_f32 v147, v150, v151\n" \
"v_exp_f32 v153, v153\n" \
"v_exp_f32 v154, v154\n" \
"v_cvt_pk_bf16_f32 v148, v152, v153\n" \
"v_exp_f32 v155, v155\n" \
"v_exp_f32 v156, v156\n" \
"v_cvt_pk_bf16_f32 v149, v154, v155\n" \
"s_add_u32 m0, s60, 4096\n" \
"s_nop 0\n" \
"buffer_load_dwordx4 v0, %0, s25 offen offset:2048 sc1 lds\n" \
"v_exp_f32 v157, v157\n" \
"v_exp_f32 v158, v158\n" \
"v_cvt_pk_bf16_f32 v150, v156, v157\n" \
"v_exp_f32 v159, v159\n" \
"v_exp_f32 v160, v160\n" \
"v_cvt_pk_bf16_f32 v151, v158, v159\n" \
"v_exp_f32 v161, v161\n" \
"v_mfma_f32_32x32x8_bf16 v[192:207], v[22:23], v[24:25], v[192:207]\n" \
"v_exp_f32 v162, v162\n" \
"v_cvt_pk_bf16_f32 v160, v160, v161\n" \
"v_exp_f32 v163, v163\n" \
"s_waitcnt lgkmcnt(3)\n" \
"v_mfma_f32_32x32x16_bf16 v[72:87], v[64:67], v[144:147], v[72:87]\n" \
"ds_read_b128 v[64:67], v6 offset:10496\n" \
"s_add_u32 m0, s60, 4096\n" \
"s_nop 0\n" \
"buffer_load_dwordx4 v0, %0, s25 offen offset:3072 sc1 lds\n" \
"v_exp_f32 v164, v164\n" \
"v_cvt_pk_bf16_f32 v161, v162, v163\n" \
"v_exp_f32 v165, v165\n" \
"v_exp_f32 v166, v166\n" \
"v_cvt_pk_bf16_f32 v162, v164, v165\n" \
"s_waitcnt lgkmcnt(3)\n" \
"v_mfma_f32_32x32x16_bf16 v[72:87], v[68:71], v[148:151], v[72:87]\n" \
"ds_read_b128 v[68:71], v6 offset:11808\n" \
"v_exp_f32 v167, v167\n" \
"v_exp_f32 v168, v168\n" \
"v_cvt_pk_bf16_f32 v163, v166, v167\n" \
"v_exp_f32 v169, v169\n" \
"v_exp_f32 v170, v170\n" \
"v_cvt_pk_bf16_f32 v164, v168, v169\n" \
"s_add_u32 m0, s60, 8192\n" \
"s_nop 0\n" \
"buffer_load_dwordx4 v0, %0, s26 offen offset:0 sc1 lds\n" \
"v_exp_f32 v171, v171\n" \
"v_exp_f32 v172, v172\n" \
"v_cvt_pk_bf16_f32 v165, v170, v171\n" \
"v_exp_f32 v173, v173\n" \
"v_exp_f32 v174, v174\n" \
"v_cvt_pk_bf16_f32 v166, v172, v173\n" \
"v_exp_f32 v175, v175\n" \
"v_exp_f32 v176, v176\n" \
"v_cvt_pk_bf16_f32 v167, v174, v175\n" \
"v_exp_f32 v177, v177\n" \
"v_mfma_f32_32x32x8_bf16 v[208:223], v[22:23], v[24:25], v[208:223]\n" \
"s_add_u32 m0, s60, 8192\n" \
"s_nop 0\n" \
"buffer_load_dwordx4 v0, %0, s26 offen offset:1024 sc1 lds\n" \
"v_exp_f32 v178, v178\n" \
"v_cvt_pk_bf16_f32 v176, v176, v177\n" \
"v_exp_f32 v179, v179\n" \
"s_waitcnt lgkmcnt(3)\n" \
"v_mfma_f32_32x32x16_bf16 v[72:87], v[56:59], v[160:163], v[72:87]\n" \
"ds_read_b128 v[56:59], v6 offset:13120\n" \
"v_exp_f32 v180, v180\n" \
"v_cvt_pk_bf16_f32 v177, v178, v179\n" \
"v_exp_f32 v181, v181\n" \
"v_exp_f32 v182, v182\n" \
"v_cvt_pk_bf16_f32 v178, v180, v181\n" \
"s_waitcnt lgkmcnt(3)\n" \
"v_mfma_f32_32x32x16_bf16 v[72:87], v[60:63], v[164:167], v[72:87]\n" \
"ds_read_b128 v[60:63], v6 offset:14432\n" \
"v_exp_f32 v183, v183\n" \
"v_exp_f32 v184, v184\n" \
"v_cvt_pk_bf16_f32 v179, v182, v183\n" \
"v_exp_f32 v185, v185\n" \
"v_exp_f32 v186, v186\n" \
"v_cvt_pk_bf16_f32 v180, v184, v185\n" \
"v_exp_f32 v187, v187\n" \
"v_exp_f32 v188, v188\n" \
"v_cvt_pk_bf16_f32 v181, v186, v187\n" \
"v_exp_f32 v189, v189\n" \
"v_exp_f32 v190, v190\n" \
"v_cvt_pk_bf16_f32 v182, v188, v189\n" \
"v_exp_f32 v191, v191\n" \
"v_exp_f32 v192, v192\n" \
"v_cvt_pk_bf16_f32 v183, v190, v191\n" \
"v_exp_f32 v193, v193\n" \
"v_mfma_f32_32x32x8_bf16 v[224:239], v[22:23], v[24:25], v[224:239]\n" \
"v_exp_f32 v194, v194\n" \
"v_cvt_pk_bf16_f32 v192, v192, v193\n" \
"v_exp_f32 v195, v195\n" \
"s_waitcnt lgkmcnt(3)\n" \
"v_mfma_f32_32x32x16_bf16 v[72:87], v[64:67], v[176:179], v[72:87]\n" \
"ds_read_b128 v[64:67], v6 offset:15744\n" \
"v_exp_f32 v196, v196\n" \
"v_cvt_pk_bf16_f32 v193, v194, v195\n" \
"v_exp_f32 v197, v197\n" \
"v_exp_f32 v198, v198\n" \
"v_cvt_pk_bf16_f32 v194, v196, v197\n" \
"s_waitcnt lgkmcnt(3)\n" \
"v_mfma_f32_32x32x16_bf16 v[72:87], v[68:71], v[180:183], v[72:87]\n" \
"ds_read_b128 v[68:71], v6 offset:17056\n" \
"v_exp_f32 v199, v199\n" \
"v_exp_f32 v200, v200\n" \
"v_cvt_pk_bf16_f32 v195, v198, v199\n" \
"v_exp_f32 v201, v201\n" \
"v_exp_f32 v202, v202\n" \
"v_cvt_pk_bf16_f32 v196, v200, v201\n" \
"v_exp_f32 v203, v203\n" \
"v_exp_f32 v204, v204\n" \
"v_cvt_pk_bf16_f32 v197, v202, v203\n" \
"v_exp_f32 v205, v205\n" \
"v_exp_f32 v206, v206\n" \
"v_cvt_pk_bf16_f32 v198, v204, v205\n" \
"v_exp_f32 v207, v207\n" \
"v_exp_f32 v208, v208\n" \
"v_cvt_pk_bf16_f32 v199, v206, v207\n" \
"v_exp_f32 v209, v209\n" \
"v_exp_f32 v210, v210\n" \
"v_cvt_pk_bf16_f32 v208, v208, v209\n" \
"v_exp_f32 v211, v211\n" \
"s_waitcnt lgkmcnt(3)\n" \
"v_mfma_f32_32x32x16_bf16 v[72:87], v[56:59], v[192:195], v[72:87]\n" \
"ds_read_b128 v[56:59], v6 offset:18368\n" \
"v_exp_f32 v212, v212\n" \
"v_cvt_pk_bf16_f32 v209, v210, v211\n" \
"v_exp_f32 v213, v213\n" \
"v_exp_f32 v214, v214\n" \
"v_cvt_pk_bf16_f32 v210, v212, v213\n" \
"s_waitcnt lgkmcnt(3)\n" \
"v_mfma_f32_32x32x16_bf16 v[72:87], v[60:63], v[196:199], v[72:87]\n" \
"ds_read_b128 v[60:63], v6 offset:19680\n" \
"v_exp_f32 v215, v215\n" \
"v_exp_f32 v216, v216\n" \
"v_cvt_pk_bf16_f32 v211, v214, v215\n" \
"v_exp_f32 v217, v217\n" \
"v_exp_f32 v218, v218\n" \
"v_cvt_pk_bf16_f32 v212, v216, v217\n" \
"buffer_load_dwordx4 v[112:115], v0, %0, s44 offen offset:0 sc1\n" \
"v_exp_f32 v219, v219\n" \
"v_exp_f32 v220, v220\n" \
"v_cvt_pk_bf16_f32 v213, v218, v219\n" \
"buffer_load_dwordx4 v[116:119], v0, %0, s45 offen offset:0 sc1\n" \
"v_exp_f32 v221, v221\n" \
"v_exp_f32 v222, v222\n" \
"v_cvt_pk_bf16_f32 v214, v220, v221\n" \
"buffer_load_dwordx4 v[120:123], v0, %0, s44 offen offset:1024 sc1\n" \
"v_exp_f32 v223, v223\n" \
"v_exp_f32 v224, v224\n" \
"v_cvt_pk_bf16_f32 v215, v222, v223\n" \
"buffer_load_dwordx4 v[124:127], v0, %0, s45 offen offset:1024 sc1\n" \
"v_exp_f32 v225, v225\n" \
"v_exp_f32 v226, v226\n" \
"v_cvt_pk_bf16_f32 v224, v224, v225\n" \
"buffer_load_dwordx4 v[128:131], v0, %0, s44 offen offset:2048 sc1\n" \
"v_exp_f32 v227, v227\n" \
"s_waitcnt lgkmcnt(3)\n" \
"v_mfma_f32_32x32x16_bf16 v[72:87], v[64:67], v[208:211], v[72:87]\n" \
"v_exp_f32 v228, v228\n" \
"v_cvt_pk_bf16_f32 v225, v226, v227\n" \
"buffer_load_dwordx4 v[132:135], v0, %0, s45 offen offset:2048 sc1\n" \
"v_exp_f32 v229, v229\n" \
"v_exp_f32 v230, v230\n" \
"v_cvt_pk_bf16_f32 v226, v228, v229\n" \
"s_waitcnt lgkmcnt(2)\n" \
"v_mfma_f32_32x32x16_bf16 v[72:87], v[68:71], v[212:215], v[72:87]\n" \
"buffer_load_dwordx4 v[136:139], v0, %0, s44 offen offset:3072 sc1\n" \
"v_exp_f32 v231, v231\n" \
"v_exp_f32 v232, v232\n" \
"v_cvt_pk_bf16_f32 v227, v230, v231\n" \
"buffer_load_dwordx4 v[140:143], v0, %0, s45 offen offset:3072 sc1\n" \
"v_exp_f32 v233, v233\n" \
"v_exp_f32 v234, v234\n" \
"v_cvt_pk_bf16_f32 v228, v232, v233\n" \
"buffer_load_dwordx4 v[144:147], v0, %0, s48 offen offset:0 sc1\n" \
"v_exp_f32 v235, v235\n" \
"v_exp_f32 v236, v236\n" \
"v_cvt_pk_bf16_f32 v229, v234, v235\n" \
"s_waitcnt lgkmcnt(1)\n" \
"v_mfma_f32_32x32x16_bf16 v[72:87], v[56:59], v[224:227], v[72:87]\n" \
"buffer_load_dwordx4 v[148:151], v0, %0, s48 offen offset:2048 sc1\n" \
"v_exp_f32 v237, v237\n" \
"v_exp_f32 v238, v238\n" \
"v_cvt_pk_bf16_f32 v230, v236, v237\n" \
"v_exp_f32 v239, v239\n" \
"s_nop 0\n" \
"v_cvt_pk_bf16_f32 v231, v238, v239\n" \
"ds_read_b128 v[96:99], v1 offset:24576\n" \
"ds_read_b128 v[100:103], v1 offset:22528\n" \
"ds_read_b128 v[104:107], v1 offset:23552\n" \
"ds_read_b128 v[108:111], v1 offset:20480\n" \
"s_waitcnt lgkmcnt(4)\n" \
"v_mfma_f32_32x32x16_bf16 v[72:87], v[60:63], v[228:231], v[72:87]\n" \
"s_nop 11\n" \
"v_cndmask_b32 v26, v80, v84, s[30:31]\n" \
"v_rcp_f32 v26, v26\n" \
"s_nop 0\n" \
"v_mul_f32 v72, v72, v26\n" \
"v_mul_f32 v73, v73, v26\n" \
"v_mul_f32 v74, v74, v26\n" \
"v_mul_f32 v75, v75, v26\n" \
"v_mul_f32 v76, v76, v26\n" \
"v_mul_f32 v77, v77, v26\n" \
"v_mul_f32 v78, v78, v26\n" \
"v_mul_f32 v79, v79, v26\n" \
"v_mul_f32 v80, v80, v26\n" \
"v_mul_f32 v81, v81, v26\n" \
"v_mul_f32 v82, v82, v26\n" \
"v_mul_f32 v83, v83, v26\n" \
"v_cvt_pk_bf16_f32 v88, v72, v73\n" \
"v_cvt_pk_bf16_f32 v89, v74, v75\n" \
"v_cvt_pk_bf16_f32 v90, v76, v77\n" \
"v_cvt_pk_bf16_f32 v91, v78, v79\n" \
"v_bfi_b32 v88, 1, s24, v88\n" \
"buffer_store_dwordx4 v[88:91], v9, %0, s42 offen sc1\n" \
"v_cvt_pk_bf16_f32 v92, v80, v81\n" \
"v_cvt_pk_bf16_f32 v93, v82, v83\n" \
"v_bfi_b32 v92, 1, s24, v92\n" \
"buffer_store_dwordx2 v[92:93], v10, %0, s43 offen sc1\n" \
"s_waitcnt vmcnt(12)\n" \
"s_barrier\n" \
"ds_read_b128 v[56:59], v1 offset:21504\n" \
"ds_read_b128 v[60:63], v1 offset:29696\n" \
"ds_read_b128 v[64:67], v1 offset:27648\n" \
"ds_read_b128 v[68:71], v1 offset:28672\n" \
"s_waitcnt vmcnt(10)\n" \
"v_bitop3_b32 v12, v112, v116, s24 bitop3:0x7e\n" \
"v_and_b32 v12, 1, v12\n" \
"v_cmp_ne_u32 vcc, 0, v12\n" \
"s_cbranch_vccnz .Lc45_%=\n" \
".Lc46_%=:\n" \
"s_waitcnt lgkmcnt(6)\n" \
"v_mfma_f32_16x16x32_bf16 v[48:51], v[96:99], v[112:115], v[48:51]\n" \
"ds_read_b128 v[96:99], v1 offset:25600\n" \
"ds_read_b128 v[152:155], v0 offset:45056\n" \
"v_and_b32 v4, 0xf0, v0\n" \
"v_mfma_f32_16x16x32_bf16 v[40:43], v[100:103], v[112:115], v[40:43]\n" \
"ds_read_b128 v[100:103], v1 offset:26624\n" \
"v_lshrrev_b32 v13, 9, v0\n" \
"v_lshl_add_u32 v4, v13, 8, v4\n" \
"s_waitcnt lgkmcnt(7)\n" \
"v_mfma_f32_16x16x32_bf16 v[44:47], v[104:107], v[112:115], v[44:47]\n" \
"ds_read_b128 v[104:107], v0 offset:4096\n" \
"ds_read_b128 v[156:159], v0 offset:43008\n" \
"v_bfe_u32 v13, v0, 8, 1\n" \
"v_mfma_f32_16x16x32_bf16 v[32:35], v[108:111], v[112:115], v[32:35]\n" \
"ds_read_b128 v[108:111], v0 offset:2048\n" \
"v_lshl_add_u32 v4, v13, 3, v4\n" \
"v_and_b32 v13, 8, v4\n" \
"s_waitcnt lgkmcnt(8)\n" \
"v_mfma_f32_16x16x32_bf16 v[36:39], v[56:59], v[112:115], v[36:39]\n" \
"ds_read_b128 v[56:59], v0 offset:3072\n" \
"ds_read_b128 v[160:163], v0 offset:44032\n" \
"v_lshl_add_u32 v4, v13, 28, v4\n" \
"v_mfma_f32_16x16x32_bf16 v[48:51], v[60:63], v[116:119], v[48:51]\n" \
"ds_read_b128 v[60:63], v0 offset:0\n" \
"v_bfe_u32 v5, v0, 1, 7\n" \
"v_bfe_u32 v13, v0, 8, 1\n" \
"s_waitcnt lgkmcnt(9)\n" \
"v_mfma_f32_16x16x32_bf16 v[40:43], v[64:67], v[116:119], v[40:43]\n" \
"ds_read_b128 v[64:67], v0 offset:1024\n" \
"ds_read_b128 v[164:167], v0 offset:40960\n" \
"v_lshl_add_u32 v5, v13, 2, v5\n" \
"v_mfma_f32_16x16x32_bf16 v[44:47], v[68:71], v[116:119], v[44:47]\n" \
"ds_read_b128 v[68:71], v0 offset:9216\n" \
"v_lshrrev_b32 v13, 9, v0\n" \
"v_lshl_add_u32 v5, v13, 31, v5\n" \
"s_waitcnt lgkmcnt(9)\n" \
"v_mfma_f32_16x16x32_bf16 v[32:35], v[96:99], v[116:119], v[32:35]\n" \
"ds_read_b128 v[96:99], v0 offset:7168\n" \
"s_waitcnt vmcnt(8)\n" \
"v_bitop3_b32 v12, v120, v124, s24 bitop3:0x7e\n" \
"v_and_b32 v12, 1, v12\n" \
"v_cmp_ne_u32 vcc, 0, v12\n" \
"ds_read_b128 v[168:171], v0 offset:41984\n" \
"v_mfma_f32_16x16x32_bf16 v[36:39], v[100:103], v[116:119], v[36:39]\n" \
"ds_read_b128 v[100:103], v0 offset:8192\n" \
"s_cbranch_vccnz .Lc47_%=\n" \
".Lc48_%=:\n" \
"s_waitcnt lgkmcnt(9)\n" \
"v_mfma_f32_16x16x32_bf16 v[48:51], v[104:107], v[120:123], v[48:51]\n" \
"ds_read_b128 v[104:107], v0 offset:5120\n" \
"ds_read_b128 v[172:175], v0 offset:55296\n" \
"v_mfma_f32_16x16x32_bf16 v[40:43], v[108:111], v[120:123], v[40:43]\n" \
"ds_read_b128 v[108:111], v0 offset:6144\n" \
"s_waitcnt lgkmcnt(9)\n" \
"v_mfma_f32_16x16x32_bf16 v[44:47], v[56:59], v[120:123], v[44:47]\n" \
"ds_read_b128 v[56:59], v0 offset:14336\n" \
"ds_read_b128 v[176:179], v0 offset:53248\n" \
"v_mfma_f32_16x16x32_bf16 v[32:35], v[60:63], v[120:123], v[32:35]\n" \
"ds_read_b128 v[60:63], v0 offset:12288\n" \
"s_waitcnt lgkmcnt(9)\n" \
"v_mfma_f32_16x16x32_bf16 v[36:39], v[64:67], v[120:123], v[36:39]\n" \
"ds_read_b128 v[64:67], v0 offset:13312\n" \
"ds_read_b128 v[180:183], v0 offset:54272\n" \
"v_mfma_f32_16x16x32_bf16 v[48:51], v[68:71], v[124:127], v[48:51]\n" \
"ds_read_b128 v[68:71], v0 offset:10240\n" \
"s_waitcnt lgkmcnt(9)\n" \
"v_mfma_f32_16x16x32_bf16 v[40:43], v[96:99], v[124:127], v[40:43]\n" \
"ds_read_b128 v[96:99], v0 offset:11264\n" \
"ds_read_b128 v[184:187], v0 offset:51200\n" \
"v_mfma_f32_16x16x32_bf16 v[44:47], v[100:103], v[124:127], v[44:47]\n" \
"ds_read_b128 v[100:103], v0 offset:19456\n" \
"s_waitcnt lgkmcnt(9)\n" \
"v_mfma_f32_16x16x32_bf16 v[32:35], v[104:107], v[124:127], v[32:35]\n" \
"ds_read_b128 v[104:107], v0 offset:17408\n" \
"s_waitcnt vmcnt(6)\n" \
"v_bitop3_b32 v12, v128, v132, s24 bitop3:0x7e\n" \
"v_and_b32 v12, 1, v12\n" \
"v_cmp_ne_u32 vcc, 0, v12\n" \
"ds_read_b128 v[188:191], v0 offset:52224\n" \
"v_mfma_f32_16x16x32_bf16 v[36:39], v[108:111], v[124:127], v[36:39]\n" \
"ds_read_b128 v[108:111], v0 offset:18432\n" \
"s_cbranch_vccnz .Lc49_%=\n" \
".Lc50_%=:\n" \
"s_waitcnt lgkmcnt(9)\n" \
"v_mfma_f32_16x16x32_bf16 v[48:51], v[56:59], v[128:131], v[48:51]\n" \
"ds_read_b128 v[56:59], v0 offset:15360\n" \
"ds_read_b128 v[192:195], v1 offset:4096\n" \
"v_mfma_f32_16x16x32_bf16 v[40:43], v[60:63], v[128:131], v[40:43]\n" \
"ds_read_b128 v[60:63], v0 offset:16384\n" \
"s_waitcnt lgkmcnt(9)\n" \
"v_mfma_f32_16x16x32_bf16 v[44:47], v[64:67], v[128:131], v[44:47]\n" \
"ds_read_b128 v[64:67], v0 offset:24576\n" \
"ds_read_b128 v[196:199], v1 offset:2048\n" \
"v_mfma_f32_16x16x32_bf16 v[32:35], v[68:71], v[128:131], v[32:35]\n" \
"ds_read_b128 v[68:71], v0 offset:22528\n" \
"s_waitcnt lgkmcnt(9)\n" \
"v_mfma_f32_16x16x32_bf16 v[36:39], v[96:99], v[128:131], v[36:39]\n" \
"ds_read_b128 v[96:99], v0 offset:23552\n" \
"ds_read_b128 v[200:203], v1 offset:3072\n" \
"v_mfma_f32_16x16x32_bf16 v[48:51], v[100:103], v[132:135], v[48:51]\n" \
"ds_read_b128 v[100:103], v0 offset:20480\n" \
"s_waitcnt lgkmcnt(9)\n" \
"v_mfma_f32_16x16x32_bf16 v[40:43], v[104:107], v[132:135], v[40:43]\n" \
"ds_read_b128 v[104:107], v0 offset:21504\n" \
"ds_read_b128 v[204:207], v1 offset:0\n" \
"v_mfma_f32_16x16x32_bf16 v[44:47], v[108:111], v[132:135], v[44:47]\n" \
"ds_read_b128 v[108:111], v0 offset:29696\n" \
"s_waitcnt lgkmcnt(9)\n" \
"v_mfma_f32_16x16x32_bf16 v[32:35], v[56:59], v[132:135], v[32:35]\n" \
"ds_read_b128 v[56:59], v0 offset:27648\n" \
"s_waitcnt vmcnt(4)\n" \
"v_bitop3_b32 v12, v136, v140, s24 bitop3:0x7e\n" \
"v_and_b32 v12, 1, v12\n" \
"v_cmp_ne_u32 vcc, 0, v12\n" \
"ds_read_b128 v[208:211], v1 offset:1024\n" \
"v_mfma_f32_16x16x32_bf16 v[36:39], v[60:63], v[132:135], v[36:39]\n" \
"ds_read_b128 v[60:63], v0 offset:28672\n" \
"s_cbranch_vccnz .Lc51_%=\n" \
".Lc52_%=:\n" \
"s_waitcnt lgkmcnt(9)\n" \
"v_mfma_f32_16x16x32_bf16 v[48:51], v[64:67], v[136:139], v[48:51]\n" \
"ds_read_b128 v[64:67], v0 offset:25600\n" \
"ds_read_b128 v[212:215], v1 offset:14336\n" \
"v_mfma_f32_16x16x32_bf16 v[40:43], v[68:71], v[136:139], v[40:43]\n" \
"ds_read_b128 v[68:71], v0 offset:26624\n" \
"s_waitcnt lgkmcnt(9)\n" \
"v_mfma_f32_16x16x32_bf16 v[44:47], v[96:99], v[136:139], v[44:47]\n" \
"ds_read_b128 v[96:99], v0 offset:34816\n" \
"ds_read_b128 v[216:219], v1 offset:34816\n" \
"v_mfma_f32_16x16x32_bf16 v[32:35], v[100:103], v[136:139], v[32:35]\n" \
"ds_read_b128 v[100:103], v0 offset:32768\n" \
"s_waitcnt lgkmcnt(9)\n" \
"v_mfma_f32_16x16x32_bf16 v[36:39], v[104:107], v[136:139], v[36:39]\n" \
"ds_read_b128 v[104:107], v0 offset:33792\n" \
"ds_read_b128 v[220:223], v1 offset:12288\n" \
"v_mfma_f32_16x16x32_bf16 v[48:51], v[108:111], v[140:143], v[48:51]\n" \
"ds_read_b128 v[108:111], v0 offset:30720\n" \
"s_waitcnt lgkmcnt(9)\n" \
"v_mfma_f32_16x16x32_bf16 v[40:43], v[56:59], v[140:143], v[40:43]\n" \
"ds_read_b128 v[56:59], v0 offset:31744\n" \
"ds_read_b128 v[224:227], v1 offset:32768\n" \
"v_mfma_f32_16x16x32_bf16 v[44:47], v[60:63], v[140:143], v[44:47]\n" \
"ds_read_b128 v[60:63], v0 offset:39936\n" \
"s_waitcnt lgkmcnt(9)\n" \
"v_mfma_f32_16x16x32_bf16 v[32:35], v[64:67], v[140:143], v[32:35]\n" \
"ds_read_b128 v[64:67], v0 offset:37888\n" \
"s_waitcnt vmcnt(2)\n" \
"v_bitop3_b32 v12, v144, v148, s24 bitop3:0x7e\n" \
"v_bitop3_b32 v12, v12, v146, s24 bitop3:0xf6\n" \
"v_bitop3_b32 v12, v12, v150, s24 bitop3:0xf6\n" \
"v_and_b32 v12, 1, v12\n" \
"v_cmp_ne_u32 vcc, 0, v12\n" \
"ds_read_b128 v[228:231], v1 offset:13312\n" \
"v_mfma_f32_16x16x32_bf16 v[36:39], v[68:71], v[140:143], v[36:39]\n" \
"ds_read_b128 v[68:71], v0 offset:38912\n" \
"s_cbranch_vccnz .Lc53_%=\n" \
".Lc54_%=:\n" \
"s_waitcnt lgkmcnt(9)\n" \
"v_mfma_f32_16x16x32_bf16 v[48:51], v[96:99], v[144:147], v[48:51]\n" \
"ds_read_b128 v[96:99], v0 offset:35840\n" \
"ds_read_b128 v[232:235], v1 offset:33792\n" \
"v_mfma_f32_16x16x32_bf16 v[40:43], v[100:103], v[144:147], v[40:43]\n" \
"ds_read_b128 v[100:103], v0 offset:36864\n" \
"s_waitcnt lgkmcnt(9)\n" \
"v_mfma_f32_16x16x32_bf16 v[44:47], v[104:107], v[144:147], v[44:47]\n" \
"ds_read_b128 v[104:107], v0 offset:50176\n" \
"buffer_load_dwordx4 v[16:19], v0, %0, s46 offen offset:0 sc1\n" \
"ds_read_b128 v[236:239], v1 offset:10240\n" \
"v_mfma_f32_16x16x32_bf16 v[32:35], v[108:111], v[144:147], v[32:35]\n" \
"ds_read_b128 v[108:111], v0 offset:48128\n" \
"s_waitcnt lgkmcnt(9)\n" \
"v_mfma_f32_16x16x32_bf16 v[36:39], v[56:59], v[144:147], v[36:39]\n" \
"ds_read_b128 v[56:59], v0 offset:49152\n" \
"ds_read_b128 v[240:243], v1 offset:30720\n" \
"v_mfma_f32_16x16x32_bf16 v[48:51], v[60:63], v[148:151], v[48:51]\n" \
"ds_read_b128 v[60:63], v0 offset:46080\n" \
"s_waitcnt lgkmcnt(9)\n" \
"v_mfma_f32_16x16x32_bf16 v[40:43], v[64:67], v[148:151], v[40:43]\n" \
"ds_read_b128 v[64:67], v0 offset:47104\n" \
"buffer_load_dwordx4 v[20:23], v0, %0, s47 offen offset:0 sc1\n" \
"ds_read_b128 v[244:247], v1 offset:11264\n" \
"v_mfma_f32_16x16x32_bf16 v[44:47], v[68:71], v[148:151], v[44:47]\n" \
"ds_read_b128 v[68:71], v0 offset:60416\n" \
"s_waitcnt lgkmcnt(9)\n" \
"v_mfma_f32_16x16x32_bf16 v[32:35], v[96:99], v[148:151], v[32:35]\n" \
"ds_read_b128 v[96:99], v0 offset:58368\n" \
"ds_read_b128 v[248:251], v1 offset:31744\n" \
"v_mfma_f32_16x16x32_bf16 v[36:39], v[100:103], v[148:151], v[36:39]\n" \
"ds_read_b128 v[100:103], v0 offset:59392\n" \
"s_barrier\n" \
"buffer_load_dwordx4 v[24:27], v0, %0, s46 offen offset:1024 sc1\n" \
"buffer_load_dwordx4 v[28:31], v0, %0, s47 offen offset:1024 sc1\n" \
"buffer_load_dwordx4 v[76:79], v0, %0, s46 offen offset:2048 sc1\n" \
"buffer_load_dwordx4 v[80:83], v0, %0, s47 offen offset:2048 sc1\n" \
"buffer_load_dwordx4 v[84:87], v0, %0, s46 offen offset:3072 sc1\n" \
"buffer_load_dwordx4 v[88:91], v0, %0, s47 offen offset:3072 sc1\n" \
"buffer_load_dwordx4 v[92:95], v0, %0, s48 offen offset:1024 sc1\n" \
"buffer_load_dwordx4 v[252:255], v0, %0, s48 offen offset:3072 sc1\n" \
"v_bfe_u32 v13, v0, 8, 1\n" \
"v_lshlrev_b32 v13, 10, v13\n" \
"v_and_b32 v12, 0x200, v0\n" \
"v_add_u32 v13, v13, v12\n" \
"v_bfe_u32 v12, v0, 1, 7\n" \
"v_add_u32 v13, v13, v12\n" \
"v_bfe_u32 v12, v0, 8, 1\n" \
"v_mul_u32_u24 v12, 34816, v12\n" \
"v_add_u32 v6, v3, v12\n" \
"s_waitcnt vmcnt(8)\n" \
"v_bitop3_b32 v12, v16, v20, s24 bitop3:0x7e\n" \
"v_and_b32 v12, 1, v12\n" \
"v_cmp_ne_u32 vcc, 0, v12\n" \
"s_cbranch_vccnz .Lc55_%=\n" \
".Lc56_%=:\n" \
"v_mfma_f32_16x16x32_bf16 v[48:51], v[152:155], v[16:19], v[48:51]\n" \
"s_waitcnt lgkmcnt(11)\n" \
"v_mfma_f32_16x16x32_bf16 v[48:51], v[104:107], v[20:23], v[48:51]\n" \
"ds_read_b128 v[104:107], v0 offset:56320\n" \
"v_mfma_f32_16x16x32_bf16 v[40:43], v[156:159], v[16:19], v[40:43]\n" \
"s_waitcnt lgkmcnt(10)\n" \
"v_mfma_f32_16x16x32_bf16 v[40:43], v[108:111], v[20:23], v[40:43]\n" \
"ds_read_b128 v[108:111], v0 offset:57344\n" \
"v_mfma_f32_16x16x32_bf16 v[44:47], v[160:163], v[16:19], v[44:47]\n" \
"s_waitcnt lgkmcnt(10)\n" \
"v_mfma_f32_16x16x32_bf16 v[44:47], v[56:59], v[20:23], v[44:47]\n" \
"ds_read_b128 v[56:59], v1 offset:9216\n" \
"v_mfma_f32_16x16x32_bf16 v[32:35], v[164:167], v[16:19], v[32:35]\n" \
"s_waitcnt lgkmcnt(9)\n" \
"v_mfma_f32_16x16x32_bf16 v[32:35], v[60:63], v[20:23], v[32:35]\n" \
"ds_read_b128 v[60:63], v1 offset:7168\n" \
"v_mfma_f32_16x16x32_bf16 v[36:39], v[168:171], v[16:19], v[36:39]\n" \
"s_waitcnt vmcnt(6)\n" \
"v_bitop3_b32 v12, v24, v28, s24 bitop3:0x7e\n" \
"v_and_b32 v12, 1, v12\n" \
"v_cmp_ne_u32 vcc, 0, v12\n" \
"s_waitcnt lgkmcnt(9)\n" \
"v_mfma_f32_16x16x32_bf16 v[36:39], v[64:67], v[20:23], v[36:39]\n" \
"ds_read_b128 v[64:67], v1 offset:8192\n" \
"s_cbranch_vccnz .Lc57_%=\n" \
".Lc58_%=:\n" \
"v_mfma_f32_16x16x32_bf16 v[48:51], v[172:175], v[24:27], v[48:51]\n" \
"s_waitcnt lgkmcnt(8)\n" \
"v_mfma_f32_16x16x32_bf16 v[48:51], v[68:71], v[28:31], v[48:51]\n" \
"ds_read_b128 v[68:71], v1 offset:5120\n" \
"v_mfma_f32_16x16x32_bf16 v[40:43], v[176:179], v[24:27], v[40:43]\n" \
"s_waitcnt lgkmcnt(8)\n" \
"v_mfma_f32_16x16x32_bf16 v[40:43], v[96:99], v[28:31], v[40:43]\n" \
"ds_read_b128 v[96:99], v1 offset:6144\n" \
"v_mfma_f32_16x16x32_bf16 v[44:47], v[180:183], v[24:27], v[44:47]\n" \
"s_waitcnt lgkmcnt(7)\n" \
"v_mfma_f32_16x16x32_bf16 v[44:47], v[100:103], v[28:31], v[44:47]\n" \
"ds_read_b128 v[100:103], v1 offset:19456\n" \
"v_mfma_f32_16x16x32_bf16 v[32:35], v[184:187], v[24:27], v[32:35]\n" \
"s_waitcnt lgkmcnt(7)\n" \
"v_mfma_f32_16x16x32_bf16 v[32:35], v[104:107], v[28:31], v[32:35]\n" \
"ds_read_b128 v[104:107], v1 offset:39936\n" \
"v_mfma_f32_16x16x32_bf16 v[36:39], v[188:191], v[24:27], v[36:39]\n" \
"s_waitcnt vmcnt(4)\n" \
"v_bitop3_b32 v12, v76, v80, s24 bitop3:0x7e\n" \
"v_and_b32 v12, 1, v12\n" \
"v_cmp_ne_u32 vcc, 0, v12\n" \
"s_waitcnt lgkmcnt(7)\n" \
"v_mfma_f32_16x16x32_bf16 v[36:39], v[108:111], v[28:31], v[36:39]\n" \
"ds_read_b128 v[108:111], v1 offset:17408\n" \
"s_cbranch_vccnz .Lc59_%=\n" \
".Lc60_%=:\n" \
"v_mfma_f32_16x16x32_bf16 v[48:51], v[192:195], v[76:79], v[48:51]\n" \
"s_waitcnt lgkmcnt(7)\n" \
"v_mfma_f32_16x16x32_bf16 v[48:51], v[56:59], v[80:83], v[48:51]\n" \
"ds_read_b128 v[56:59], v1 offset:37888\n" \
"v_mfma_f32_16x16x32_bf16 v[40:43], v[196:199], v[76:79], v[40:43]\n" \
"s_waitcnt lgkmcnt(7)\n" \
"v_mfma_f32_16x16x32_bf16 v[40:43], v[60:63], v[80:83], v[40:43]\n" \
"ds_read_b128 v[60:63], v1 offset:18432\n" \
"v_mfma_f32_16x16x32_bf16 v[44:47], v[200:203], v[76:79], v[44:47]\n" \
"s_waitcnt lgkmcnt(7)\n" \
"v_mfma_f32_16x16x32_bf16 v[44:47], v[64:67], v[80:83], v[44:47]\n" \
"ds_read_b128 v[64:67], v1 offset:38912\n" \
"v_mfma_f32_16x16x32_bf16 v[32:35], v[204:207], v[76:79], v[32:35]\n" \
"s_waitcnt lgkmcnt(7)\n" \
"v_mfma_f32_16x16x32_bf16 v[32:35], v[68:71], v[80:83], v[32:35]\n" \
"ds_read_b128 v[68:71], v1 offset:15360\n" \
"v_mfma_f32_16x16x32_bf16 v[36:39], v[208:211], v[76:79], v[36:39]\n" \
"s_waitcnt vmcnt(2)\n" \
"v_bitop3_b32 v12, v84, v88, s24 bitop3:0x7e\n" \
"v_and_b32 v12, 1, v12\n" \
"v_cmp_ne_u32 vcc, 0, v12\n" \
"s_waitcnt lgkmcnt(7)\n" \
"v_mfma_f32_16x16x32_bf16 v[36:39], v[96:99], v[80:83], v[36:39]\n" \
"ds_read_b128 v[96:99], v1 offset:35840\n" \
"s_cbranch_vccnz .Lc61_%=\n" \
".Lc62_%=:\n" \
"v_mfma_f32_16x16x32_bf16 v[48:51], v[212:215], v[84:87], v[48:51]\n" \
"s_waitcnt vmcnt(0)\n" \
"v_bitop3_b32 v12, v92, v252, s24 bitop3:0x7e\n" \
"v_bitop3_b32 v12, v12, v94, s24 bitop3:0xf6\n" \
"v_bitop3_b32 v12, v12, v254, s24 bitop3:0xf6\n" \
"v_and_b32 v12, 1, v12\n" \
"v_cmp_ne_u32 vcc, 0, v12\n" \
"s_waitcnt lgkmcnt(7)\n" \
"v_mfma_f32_16x16x32_bf16 v[48:51], v[100:103], v[88:91], v[48:51]\n" \
"ds_read_b128 v[100:103], v1 offset:16384\n" \
"s_cbranch_vccnz .Lc63_%=\n" \
".Lc64_%=:\n" \
"v_mfma_f32_16x16x32_bf16 v[48:51], v[216:219], v[92:95], v[48:51]\n" \
"s_waitcnt lgkmcnt(7)\n" \
"v_mfma_f32_16x16x32_bf16 v[48:51], v[104:107], v[252:255], v[48:51]\n" \
"ds_read_b128 v[104:107], v1 offset:36864\n" \
"v_mfma_f32_16x16x32_bf16 v[40:43], v[220:223], v[84:87], v[40:43]\n" \
"s_waitcnt lgkmcnt(7)\n" \
"v_mfma_f32_16x16x32_bf16 v[40:43], v[108:111], v[88:91], v[40:43]\n" \
"v_mfma_f32_16x16x32_bf16 v[40:43], v[224:227], v[92:95], v[40:43]\n" \
"s_nop 2\n" \
"v_cvt_pk_bf16_f32 v120, v48, v49\n" \
"v_cvt_pk_bf16_f32 v121, v50, v51\n" \
"s_waitcnt lgkmcnt(6)\n" \
"v_mfma_f32_16x16x32_bf16 v[40:43], v[56:59], v[252:255], v[40:43]\n" \
"v_cvt_pk_bf16_f32 v122, v48, v49\n" \
"v_cvt_pk_bf16_f32 v123, v50, v51\n" \
"v_mfma_f32_16x16x32_bf16 v[44:47], v[228:231], v[84:87], v[44:47]\n" \
"v_cvt_pk_bf16_f32 v124, v48, v49\n" \
"v_cvt_pk_bf16_f32 v125, v50, v51\n" \
"s_waitcnt lgkmcnt(5)\n" \
"v_mfma_f32_16x16x32_bf16 v[44:47], v[60:63], v[88:91], v[44:47]\n" \
"v_or_b32 v120, 1, v120\n" \
"v_mfma_f32_16x16x32_bf16 v[44:47], v[232:235], v[92:95], v[44:47]\n" \
"v_permlane16_swap_b32 v121, v123\n" \
"v_permlane16_swap_b32 v120, v122\n" \
"s_waitcnt lgkmcnt(4)\n" \
"v_mfma_f32_16x16x32_bf16 v[44:47], v[64:67], v[252:255], v[44:47]\n" \
"buffer_store_dwordx4 v[120:123], v4, %0, s49 offen offset:2048 sc1\n" \
"v_mfma_f32_16x16x32_bf16 v[32:35], v[236:239], v[84:87], v[32:35]\n" \
"v_cvt_pk_bf16_f32 v116, v40, v41\n" \
"v_cvt_pk_bf16_f32 v117, v42, v43\n" \
"s_waitcnt lgkmcnt(3)\n" \
"v_mfma_f32_16x16x32_bf16 v[32:35], v[68:71], v[88:91], v[32:35]\n" \
"v_mfma_f32_16x16x32_bf16 v[32:35], v[240:243], v[92:95], v[32:35]\n" \
"s_nop 0\n" \
"v_cvt_pk_bf16_f32 v118, v44, v45\n" \
"v_cvt_pk_bf16_f32 v119, v46, v47\n" \
"s_waitcnt lgkmcnt(2)\n" \
"v_mfma_f32_16x16x32_bf16 v[32:35], v[96:99], v[252:255], v[32:35]\n" \
"v_or_b32 v116, 1, v116\n" \
"v_mfma_f32_16x16x32_bf16 v[36:39], v[244:247], v[84:87], v[36:39]\n" \
"buffer_store_dwordx4 v[116:119], v0, %0, s49 offen offset:1024 sc1\n" \
"s_waitcnt lgkmcnt(1)\n" \
"v_mfma_f32_16x16x32_bf16 v[36:39], v[100:103], v[88:91], v[36:39]\n" \
"v_mfma_f32_16x16x32_bf16 v[36:39], v[248:251], v[92:95], v[36:39]\n" \
"s_nop 1\n" \
"v_cvt_pk_bf16_f32 v112, v32, v33\n" \
"v_cvt_pk_bf16_f32 v113, v34, v35\n" \
"s_waitcnt lgkmcnt(0)\n" \
"v_mfma_f32_16x16x32_bf16 v[36:39], v[104:107], v[252:255], v[36:39]\n" \
"s_nop 7\n" \
"v_cvt_pk_bf16_f32 v114, v36, v37\n" \
"v_cvt_pk_bf16_f32 v115, v38, v39\n" \
"v_or_b32 v112, 1, v112\n" \
"buffer_store_dwordx4 v[112:115], v0, %0, s49 offen offset:0 sc1\n" \
"s_barrier\n" \
"s_mov_b32 s25, 0x3f803f80\n" \
"v_dot2_f32_bf16 v126, v112, s25, 0\n" \
"v_dot2_f32_bf16 v127, v112, v112, 0\n" \
"v_dot2_f32_bf16 v126, v113, s25, v126\n" \
"v_dot2_f32_bf16 v127, v113, v113, v127\n" \
"v_dot2_f32_bf16 v126, v114, s25, v126\n" \
"v_dot2_f32_bf16 v127, v114, v114, v127\n" \
"v_dot2_f32_bf16 v126, v115, s25, v126\n" \
"v_dot2_f32_bf16 v127, v115, v115, v127\n" \
"v_dot2_f32_bf16 v126, v116, s25, v126\n" \
"v_dot2_f32_bf16 v127, v116, v116, v127\n" \
"v_dot2_f32_bf16 v126, v117, s25, v126\n" \
"v_dot2_f32_bf16 v127, v117, v117, v127\n" \
"v_dot2_f32_bf16 v126, v118, s25, v126\n" \
"v_dot2_f32_bf16 v127, v118, v118, v127\n" \
"v_dot2_f32_bf16 v126, v119, s25, v126\n" \
"v_dot2_f32_bf16 v127, v119, v119, v127\n" \
"v_dot2_f32_bf16 v126, v124, s25, v126\n" \
"v_dot2_f32_bf16 v127, v124, v124, v127\n" \
"v_dot2_f32_bf16 v126, v125, s25, v126\n" \
"v_dot2_f32_bf16 v127, v125, v125, v127\n" \
"s_nop 2\n" \
"v_permlane16_swap_b32 v126, v127\n" \
"v_add_f32 v126, v126, v127\n" \
"v_mov_b32 v127, v126\n" \
"s_nop 1\n" \
"v_permlane32_swap_b32 v126, v127\n" \
"v_add_f32 v126, v126, v127\n" \
"v_or_b32 v126, 1, v126\n" \
"buffer_store_dword v126, v5, %0, s51 offen sc1\n" \
"s_sleep 3\n" \
"buffer_load_dwordx4 v[96:99], v3, %0, s34 offen offset:2048 sc1\n" \
"buffer_load_dwordx4 v[100:103], v3, %0, s35 offen offset:512 sc1\n" \
"buffer_load_dwordx4 v[104:107], v3, %0, s35 offen offset:3072 sc1\n" \
"buffer_load_dwordx4 v[108:111], v3, %0, s36 offen offset:1536 sc1\n" \
"buffer_load_dwordx4 v[112:115], v3, %0, s37 offen offset:0 sc1\n" \
"buffer_load_dwordx4 v[116:119], v3, %0, s37 offen offset:2560 sc1\n" \
"buffer_load_dwordx4 v[120:123], v3, %0, s38 offen offset:1024 sc1\n" \
"buffer_load_dwordx4 v[124:127], v3, %0, s38 offen offset:3584 sc1\n" \
"buffer_load_dwordx4 v[128:131], v3, %0, s34 offen offset:1536 sc1\n" \
"buffer_load_dwordx4 v[132:135], v3, %0, s35 offen offset:0 sc1\n" \
"ds_read_b128 v[80:83], v1 offset:40960\n" \
"ds_read_b128 v[84:87], v1 offset:51200\n" \
"ds_read_b128 v[88:91], v2 offset:0\n" \
"ds_read_b128 v[92:95], v1 offset:41984\n" \
"s_waitcnt vmcnt(8)\n" \
"v_bitop3_b32 v12, v96, v100, 1 bitop3:42\n" \
"v_cmp_ne_u32 vcc, 0, v12\n" \
"s_cbranch_vccnz .Lc65_%=\n" \
".Lc66_%=:\n" \
"s_waitcnt lgkmcnt(2)\n" \
"v_mfma_f32_32x32x16_bf16 v[32:47], v[80:83], v[96:99], 0\n" \
"ds_read_b128 v[80:83], v1 offset:52224\n" \
"v_mfma_f32_32x32x16_bf16 v[48:63], v[84:87], v[96:99], 0\n" \
"ds_read_b128 v[84:87], v2 offset:1024\n" \
"buffer_load_dwordx4 v[136:139], v3, %0, s35 offen offset:2560 sc1\n" \
"s_waitcnt lgkmcnt(2)\n" \
"v_mfma_f32_32x32x16_bf16 v[64:79], v[88:91], v[96:99], 0\n" \
"ds_read_b128 v[88:91], v1 offset:43008\n" \
"v_mfma_f32_32x32x16_bf16 v[32:47], v[92:95], v[100:103], v[32:47]\n" \
"ds_read_b128 v[92:95], v1 offset:53248\n" \
"buffer_load_dwordx4 v[140:143], v3, %0, s36 offen offset:1024 sc1\n" \
"s_waitcnt lgkmcnt(2)\n" \
"v_mfma_f32_32x32x16_bf16 v[48:63], v[80:83], v[100:103], v[48:63]\n" \
"ds_read_b128 v[80:83], v2 offset:2048\n" \
"s_waitcnt vmcnt(8)\n" \
"v_bitop3_b32 v12, v104, v108, 1 bitop3:42\n" \
"v_cmp_ne_u32 vcc, 0, v12\n" \
"v_mfma_f32_32x32x16_bf16 v[64:79], v[84:87], v[100:103], v[64:79]\n" \
"ds_read_b128 v[84:87], v1 offset:44032\n" \
"buffer_load_dwordx4 v[144:147], v3, %0, s36 offen offset:3584 sc1\n" \
"s_cbranch_vccnz .Lc67_%=\n" \
".Lc68_%=:\n" \
"s_waitcnt lgkmcnt(2)\n" \
"v_mfma_f32_32x32x16_bf16 v[32:47], v[88:91], v[104:107], v[32:47]\n" \
"ds_read_b128 v[88:91], v1 offset:54272\n" \
"v_mfma_f32_32x32x16_bf16 v[48:63], v[92:95], v[104:107], v[48:63]\n" \
"ds_read_b128 v[92:95], v2 offset:3072\n" \
"buffer_load_dwordx4 v[148:151], v3, %0, s37 offen offset:2048 sc1\n" \
"s_waitcnt lgkmcnt(2)\n" \
"v_mfma_f32_32x32x16_bf16 v[64:79], v[80:83], v[104:107], v[64:79]\n" \
"ds_read_b128 v[80:83], v1 offset:45056\n" \
"v_mfma_f32_32x32x16_bf16 v[32:47], v[84:87], v[108:111], v[32:47]\n" \
"ds_read_b128 v[84:87], v1 offset:55296\n" \
"buffer_load_dwordx4 v[152:155], v3, %0, s38 offen offset:512 sc1\n" \
"s_waitcnt lgkmcnt(2)\n" \
"v_mfma_f32_32x32x16_bf16 v[48:63], v[88:91], v[108:111], v[48:63]\n" \
"ds_read_b128 v[88:91], v2 offset:4096\n" \
"s_waitcnt vmcnt(9)\n" \
"v_bitop3_b32 v12, v112, v116, 1 bitop3:42\n" \
"v_cmp_ne_u32 vcc, 0, v12\n" \
"v_mfma_f32_32x32x16_bf16 v[64:79], v[92:95], v[108:111], v[64:79]\n" \
"ds_read_b128 v[92:95], v1 offset:46080\n" \
"buffer_load_dwordx4 v[156:159], v3, %0, s38 offen offset:3072 sc1\n" \
"s_cbranch_vccnz .Lc69_%=\n" \
".Lc70_%=:\n" \
"s_waitcnt lgkmcnt(2)\n" \
"v_mfma_f32_32x32x16_bf16 v[32:47], v[80:83], v[112:115], v[32:47]\n" \
"ds_read_b128 v[80:83], v1 offset:56320\n" \
"v_mfma_f32_32x32x16_bf16 v[48:63], v[84:87], v[112:115], v[48:63]\n" \
"ds_read_b128 v[84:87], v2 offset:5120\n" \
"buffer_load_dwordx4 v[160:163], v3, %0, s34 offen offset:1024 sc1\n" \
"s_waitcnt lgkmcnt(2)\n" \
"v_mfma_f32_32x32x16_bf16 v[64:79], v[88:91], v[112:115], v[64:79]\n" \
"ds_read_b128 v[88:91], v1 offset:47104\n" \
"buffer_load_dwordx2 v[16:17], v13, %0, s39 offen offset:0 sc1\n" \
"buffer_load_dwordx2 v[18:19], v13, %0, s39 offen offset:128 sc1\n" \
"v_mfma_f32_32x32x16_bf16 v[32:47], v[92:95], v[116:119], v[32:47]\n" \
"ds_read_b128 v[92:95], v1 offset:57344\n" \
"buffer_load_dwordx2 v[20:21], v13, %0, s39 offen offset:256 sc1\n" \
"buffer_load_dwordx2 v[22:23], v13, %0, s39 offen offset:384 sc1\n" \
"s_waitcnt lgkmcnt(2)\n" \
"v_mfma_f32_32x32x16_bf16 v[48:63], v[80:83], v[116:119], v[48:63]\n" \
"ds_read_b128 v[80:83], v2 offset:6144\n" \
"buffer_load_dwordx4 v[164:167], v3, %0, s34 offen offset:3584 sc1\n" \
"s_waitcnt vmcnt(14)\n" \
"v_bitop3_b32 v12, v120, v124, 1 bitop3:42\n" \
"v_cmp_ne_u32 vcc, 0, v12\n" \
"v_mfma_f32_32x32x16_bf16 v[64:79], v[84:87], v[116:119], v[64:79]\n" \
"ds_read_b128 v[84:87], v1 offset:48128\n" \
"s_cbranch_vccnz .Lc71_%=\n" \
".Lc72_%=:\n" \
"s_waitcnt lgkmcnt(2)\n" \
"v_mfma_f32_32x32x16_bf16 v[32:47], v[88:91], v[120:123], v[32:47]\n" \
"ds_read_b128 v[88:91], v1 offset:58368\n" \
"buffer_load_dwordx4 v[168:171], v3, %0, s35 offen offset:2048 sc1\n" \
"v_mfma_f32_32x32x16_bf16 v[48:63], v[92:95], v[120:123], v[48:63]\n" \
"ds_read_b128 v[92:95], v2 offset:7168\n" \
"s_waitcnt lgkmcnt(2)\n" \
"v_mfma_f32_32x32x16_bf16 v[64:79], v[80:83], v[120:123], v[64:79]\n" \
"ds_read_b128 v[80:83], v1 offset:49152\n" \
"buffer_load_dwordx4 v[172:175], v3, %0, s36 offen offset:512 sc1\n" \
"v_mfma_f32_32x32x16_bf16 v[32:47], v[84:87], v[124:127], v[32:47]\n" \
"ds_read_b128 v[84:87], v1 offset:59392\n" \
"s_waitcnt lgkmcnt(2)\n" \
"v_mfma_f32_32x32x16_bf16 v[48:63], v[88:91], v[124:127], v[48:63]\n" \
"ds_read_b128 v[88:91], v2 offset:8192\n" \
"buffer_load_dwordx4 v[176:179], v3, %0, s36 offen offset:3072 sc1\n" \
"s_waitcnt vmcnt(15)\n" \
"v_bitop3_b32 v12, v128, v132, 1 bitop3:42\n" \
"v_cmp_ne_u32 vcc, 0, v12\n" \
"v_mfma_f32_32x32x16_bf16 v[64:79], v[92:95], v[124:127], v[64:79]\n" \
"ds_read_b128 v[92:95], v1 offset:50176\n" \
"s_cbranch_vccnz .Lc73_%=\n" \
".Lc74_%=:\n" \
"s_waitcnt lgkmcnt(2)\n" \
"v_mfma_f32_32x32x16_bf16 v[32:47], v[80:83], v[128:131], v[32:47]\n" \
"ds_read_b128 v[80:83], v1 offset:60416\n" \
"buffer_load_dwordx4 v[180:183], v3, %0, s37 offen offset:1536 sc1\n" \
"v_mfma_f32_32x32x16_bf16 v[48:63], v[84:87], v[128:131], v[48:63]\n" \
"ds_read_b128 v[84:87], v2 offset:9216\n" \
"s_waitcnt lgkmcnt(2)\n" \
"v_mfma_f32_32x32x16_bf16 v[64:79], v[88:91], v[128:131], v[64:79]\n" \
"ds_read_b128 v[88:91], v2 offset:10240\n" \
"buffer_load_dwordx4 v[184:187], v3, %0, s38 offen offset:0 sc1\n" \
"v_mfma_f32_32x32x16_bf16 v[32:47], v[92:95], v[132:135], v[32:47]\n" \
"ds_read_b128 v[92:95], v2 offset:20480\n" \
"s_waitcnt lgkmcnt(2)\n" \
"v_mfma_f32_32x32x16_bf16 v[48:63], v[80:83], v[132:135], v[48:63]\n" \
"ds_read_b128 v[80:83], v1 offset:20480\n" \
"buffer_load_dwordx4 v[188:191], v3, %0, s38 offen offset:2560 sc1\n" \
"s_waitcnt vmcnt(16)\n" \
"v_bitop3_b32 v12, v136, v140, 1 bitop3:42\n" \
"v_cmp_ne_u32 vcc, 0, v12\n" \
"v_mfma_f32_32x32x16_bf16 v[64:79], v[84:87], v[132:135], v[64:79]\n" \
"ds_read_b128 v[84:87], v2 offset:11264\n" \
"s_cbranch_vccnz .Lc75_%=\n" \
".Lc76_%=:\n" \
"s_waitcnt lgkmcnt(2)\n" \
"v_mfma_f32_32x32x16_bf16 v[32:47], v[88:91], v[136:139], v[32:47]\n" \
"ds_read_b128 v[88:91], v2 offset:21504\n" \
"buffer_load_dwordx4 v[192:195], v3, %0, s34 offen offset:512 sc1\n" \
"v_mfma_f32_32x32x16_bf16 v[48:63], v[92:95], v[136:139], v[48:63]\n" \
"ds_read_b128 v[92:95], v1 offset:21504\n" \
"s_waitcnt lgkmcnt(2)\n" \
"v_mfma_f32_32x32x16_bf16 v[64:79], v[80:83], v[136:139], v[64:79]\n" \
"ds_read_b128 v[80:83], v2 offset:12288\n" \
"buffer_load_dwordx4 v[196:199], v3, %0, s34 offen offset:3072 sc1\n" \
"v_mfma_f32_32x32x16_bf16 v[32:47], v[84:87], v[140:143], v[32:47]\n" \
"ds_read_b128 v[84:87], v2 offset:22528\n" \
"s_waitcnt lgkmcnt(2)\n" \
"v_mfma_f32_32x32x16_bf16 v[48:63], v[88:91], v[140:143], v[48:63]\n" \
"ds_read_b128 v[88:91], v1 offset:22528\n" \
"buffer_load_dwordx4 v[200:203], v3, %0, s35 offen offset:1536 sc1\n" \
"s_waitcnt vmcnt(17)\n" \
"v_bitop3_b32 v12, v144, v148, 1 bitop3:42\n" \
"v_cmp_ne_u32 vcc, 0, v12\n" \
"v_mfma_f32_32x32x16_bf16 v[64:79], v[92:95], v[140:143], v[64:79]\n" \
"ds_read_b128 v[92:95], v2 offset:13312\n" \
"s_cbranch_vccnz .Lc77_%=\n" \
".Lc78_%=:\n" \
"s_waitcnt lgkmcnt(2)\n" \
"v_mfma_f32_32x32x16_bf16 v[32:47], v[80:83], v[144:147], v[32:47]\n" \
"ds_read_b128 v[80:83], v2 offset:23552\n" \
"buffer_load_dwordx4 v[204:207], v3, %0, s36 offen offset:0 sc1\n" \
"v_mfma_f32_32x32x16_bf16 v[48:63], v[84:87], v[144:147], v[48:63]\n" \
"ds_read_b128 v[84:87], v2 offset:14336\n" \
"s_waitcnt lgkmcnt(2)\n" \
"v_mfma_f32_32x32x16_bf16 v[64:79], v[88:91], v[144:147], v[64:79]\n" \
"ds_read_b128 v[88:91], v2 offset:24576\n" \
"buffer_load_dwordx4 v[208:211], v3, %0, s36 offen offset:2560 sc1\n" \
"v_mfma_f32_32x32x16_bf16 v[32:47], v[92:95], v[148:151], v[32:47]\n" \
"ds_read_b128 v[92:95], v2 offset:15360\n" \
"s_waitcnt vmcnt(17)\n" \
"v_bitop3_b32 v12, v152, v156, 1 bitop3:42\n" \
"v_cmp_ne_u32 vcc, 0, v12\n" \
"s_waitcnt lgkmcnt(2)\n" \
"v_mfma_f32_32x32x16_bf16 v[48:63], v[80:83], v[148:151], v[48:63]\n" \
"ds_read_b128 v[80:83], v2 offset:25600\n" \
"buffer_load_dwordx4 v[212:215], v3, %0, s37 offen offset:1024 sc1\n" \
"s_cbranch_vccnz .Lc79_%=\n" \
".Lc80_%=:\n" \
"v_mfma_f32_32x32x16_bf16 v[32:47], v[84:87], v[152:155], v[32:47]\n" \
"ds_read_b128 v[84:87], v2 offset:16384\n" \
"s_waitcnt lgkmcnt(2)\n" \
"v_mfma_f32_32x32x16_bf16 v[48:63], v[88:91], v[152:155], v[48:63]\n" \
"ds_read_b128 v[88:91], v2 offset:26624\n" \
"buffer_load_dwordx4 v[216:219], v3, %0, s37 offen offset:3584 sc1\n" \
"v_mfma_f32_32x32x16_bf16 v[32:47], v[92:95], v[156:159], v[32:47]\n" \
"ds_read_b128 v[92:95], v2 offset:17408\n" \
"s_waitcnt vmcnt(13)\n" \
"v_bitop3_b32 v12, v160, v164, 1 bitop3:42\n" \
"v_cmp_ne_u32 vcc, 0, v12\n" \
"s_waitcnt lgkmcnt(2)\n" \
"v_mfma_f32_32x32x16_bf16 v[48:63], v[80:83], v[156:159], v[48:63]\n" \
"ds_read_b128 v[80:83], v2 offset:27648\n" \
"buffer_load_dwordx4 v[220:223], v3, %0, s38 offen offset:2048 sc1\n" \
"s_cbranch_vccnz .Lc81_%=\n" \
".Lc82_%=:\n" \
"v_mfma_f32_32x32x16_bf16 v[32:47], v[84:87], v[160:163], v[32:47]\n" \
"ds_read_b128 v[84:87], v2 offset:18432\n" \
"s_waitcnt lgkmcnt(2)\n" \
"v_mfma_f32_32x32x16_bf16 v[48:63], v[88:91], v[160:163], v[48:63]\n" \
"ds_read_b128 v[88:91], v2 offset:28672\n" \
"buffer_load_dwordx4 v[224:227], v3, %0, s34 offen offset:0 sc1\n" \
"v_mfma_f32_32x32x16_bf16 v[32:47], v[92:95], v[164:167], v[32:47]\n" \
"ds_read_b128 v[92:95], v2 offset:19456\n" \
"s_waitcnt vmcnt(13)\n" \
"v_bitop3_b32 v12, v168, v172, 1 bitop3:42\n" \
"v_cmp_ne_u32 vcc, 0, v12\n" \
"s_waitcnt lgkmcnt(2)\n" \
"v_mfma_f32_32x32x16_bf16 v[48:63], v[80:83], v[164:167], v[48:63]\n" \
"ds_read_b128 v[80:83], v2 offset:29696\n" \
"buffer_load_dwordx4 v[228:231], v3, %0, s34 offen offset:2560 sc1\n" \
"s_cbranch_vccnz .Lc83_%=\n" \
".Lc84_%=:\n" \
"v_mfma_f32_32x32x16_bf16 v[32:47], v[84:87], v[168:171], v[32:47]\n" \
"ds_read_b128 v[84:87], v0 offset:0\n" \
"s_waitcnt lgkmcnt(2)\n" \
"v_mfma_f32_32x32x16_bf16 v[48:63], v[88:91], v[168:171], v[48:63]\n" \
"ds_read_b128 v[88:91], v0 offset:10240\n" \
"buffer_load_dwordx4 v[232:235], v3, %0, s35 offen offset:1024 sc1\n" \
"v_mfma_f32_32x32x16_bf16 v[32:47], v[92:95], v[172:175], v[32:47]\n" \
"ds_read_b128 v[92:95], v0 offset:1024\n" \
"s_waitcnt vmcnt(13)\n" \
"v_bitop3_b32 v12, v176, v180, 1 bitop3:42\n" \
"v_cmp_ne_u32 vcc, 0, v12\n" \
"s_waitcnt lgkmcnt(2)\n" \
"v_mfma_f32_32x32x16_bf16 v[48:63], v[80:83], v[172:175], v[48:63]\n" \
"ds_read_b128 v[80:83], v0 offset:11264\n" \
"buffer_load_dwordx4 v[236:239], v3, %0, s35 offen offset:3584 sc1\n" \
"s_cbranch_vccnz .Lc85_%=\n" \
".Lc86_%=:\n" \
"v_mfma_f32_32x32x16_bf16 v[32:47], v[84:87], v[176:179], v[32:47]\n" \
"ds_read_b128 v[84:87], v0 offset:2048\n" \
"s_waitcnt lgkmcnt(2)\n" \
"v_mfma_f32_32x32x16_bf16 v[48:63], v[88:91], v[176:179], v[48:63]\n" \
"ds_read_b128 v[88:91], v0 offset:12288\n" \
"buffer_load_dwordx4 v[240:243], v3, %0, s36 offen offset:2048 sc1\n" \
"v_mfma_f32_32x32x16_bf16 v[32:47], v[92:95], v[180:183], v[32:47]\n" \
"ds_read_b128 v[92:95], v0 offset:3072\n" \
"s_waitcnt vmcnt(13)\n" \
"v_bitop3_b32 v12, v184, v188, 1 bitop3:42\n" \
"v_cmp_ne_u32 vcc, 0, v12\n" \
"s_waitcnt lgkmcnt(2)\n" \
"v_mfma_f32_32x32x16_bf16 v[48:63], v[80:83], v[180:183], v[48:63]\n" \
"ds_read_b128 v[80:83], v0 offset:13312\n" \
"buffer_load_dwordx4 v[244:247], v3, %0, s37 offen offset:512 sc1\n" \
"s_cbranch_vccnz .Lc87_%=\n" \
".Lc88_%=:\n" \
"v_mfma_f32_32x32x16_bf16 v[32:47], v[84:87], v[184:187], v[32:47]\n" \
"ds_read_b128 v[84:87], v0 offset:4096\n" \
"v_bitop3_b32 v14, v16, v17, v18 bitop3:128\n" \
"v_bitop3_b32 v14, v14, v19, v20 bitop3:128\n" \
"v_bitop3_b32 v14, v14, v21, v22 bitop3:128\n" \
"s_waitcnt lgkmcnt(2)\n" \
"v_mfma_f32_32x32x16_bf16 v[48:63], v[88:91], v[184:187], v[48:63]\n" \
"ds_read_b128 v[88:91], v0 offset:14336\n" \
"buffer_load_dwordx4 v[248:251], v3, %0, s37 offen offset:3072 sc1\n" \
"v_bitop3_b32 v14, v14, v23, 1 bitop3:42\n" \
"v_mfma_f32_32x32x16_bf16 v[32:47], v[92:95], v[188:191], v[32:47]\n" \
"ds_read_b128 v[92:95], v0 offset:5120\n" \
"s_waitcnt vmcnt(13)\n" \
"v_bitop3_b32 v12, v192, v196, 1 bitop3:42\n" \
"v_cmp_ne_u32 vcc, 0, v12\n" \
"v_cmp_ne_u32 s[28:29], 0, v14\n" \
"s_nop 0\n" \
"s_cmp_lg_u64 s[28:29], 0\n" \
"s_cbranch_scc1 .Lc89_%=\n" \
".Lc90_%=:\n" \
"s_waitcnt lgkmcnt(2)\n" \
"v_mfma_f32_32x32x16_bf16 v[48:63], v[80:83], v[188:191], v[48:63]\n" \
"ds_read_b128 v[80:83], v0 offset:15360\n" \
"buffer_load_dwordx4 v[252:255], v3, %0, s38 offen offset:1536 sc1\n" \
"s_cbranch_vccnz .Lc91_%=\n" \
".Lc92_%=:\n" \
"v_mfma_f32_32x32x16_bf16 v[32:47], v[84:87], v[192:195], v[32:47]\n" \
"ds_read_b128 v[84:87], v0 offset:6144\n" \
"v_add_f32 v16, v16, v18\n" \
"v_add_f32 v17, v17, v19\n" \
"v_add_f32 v20, v20, v22\n" \
"s_waitcnt lgkmcnt(2)\n" \
"v_mfma_f32_32x32x16_bf16 v[48:63], v[88:91], v[192:195], v[48:63]\n" \
"ds_read_b128 v[88:91], v0 offset:16384\n" \
"v_add_f32 v21, v21, v23\n" \
"v_add_f32 v16, v16, v20\n" \
"v_add_f32 v17, v17, v21\n" \
"v_mfma_f32_32x32x16_bf16 v[32:47], v[92:95], v[196:199], v[32:47]\n" \
"ds_read_b128 v[92:95], v0 offset:7168\n" \
"s_waitcnt vmcnt(12)\n" \
"v_bitop3_b32 v12, v200, v204, 1 bitop3:42\n" \
"v_cmp_ne_u32 vcc, 0, v12\n" \
"s_barrier\n" \
"s_waitcnt lgkmcnt(2)\n" \
"v_mfma_f32_32x32x16_bf16 v[48:63], v[80:83], v[196:199], v[48:63]\n" \
"ds_read_b128 v[80:83], v0 offset:17408\n" \
"s_cbranch_vccnz .Lc93_%=\n" \
".Lc94_%=:\n" \
"v_permlane32_swap_b32 v16, v17\n" \
"v_mfma_f32_32x32x16_bf16 v[32:47], v[84:87], v[200:203], v[32:47]\n" \
"ds_read_b128 v[84:87], v0 offset:8192\n" \
"v_add_f32 v16, v16, v17\n" \
"v_mov_b32 v17, v16\n" \
"s_waitcnt lgkmcnt(2)\n" \
"v_mfma_f32_32x32x16_bf16 v[48:63], v[88:91], v[200:203], v[48:63]\n" \
"ds_read_b128 v[88:91], v0 offset:18432\n" \
"v_permlane32_swap_b32 v16, v17\n" \
"v_mfma_f32_32x32x16_bf16 v[32:47], v[92:95], v[204:207], v[32:47]\n" \
"ds_read_b128 v[92:95], v0 offset:9216\n" \
"s_waitcnt vmcnt(10)\n" \
"v_bitop3_b32 v12, v208, v212, 1 bitop3:42\n" \
"v_cmp_ne_u32 vcc, 0, v12\n" \
"v_mul_f32 v16, 0x3acccccd, v16\n" \
"s_waitcnt lgkmcnt(2)\n" \
"v_mfma_f32_32x32x16_bf16 v[48:63], v[80:83], v[204:207], v[48:63]\n" \
"ds_read_b128 v[80:83], v0 offset:19456\n" \
"s_cbranch_vccnz .Lc95_%=\n" \
".Lc96_%=:\n" \
"v_mul_f32 v17, 0x3acccccd, v17\n" \
"v_fma_f32 v17, -v16, v16, v17\n" \
"v_mfma_f32_32x32x16_bf16 v[32:47], v[84:87], v[208:211], v[32:47]\n" \
"ds_read_b128 v[84:87], v0 offset:20480\n" \
"v_max_f32 v17, 0, v17\n" \
"v_add_f32 v17, 0x3727c5ac, v17\n" \
"s_waitcnt lgkmcnt(2)\n" \
"v_mfma_f32_32x32x16_bf16 v[48:63], v[88:91], v[208:211], v[48:63]\n" \
"ds_read_b128 v[88:91], v0 offset:30720\n" \
"v_rsq_f32 v18, v17\n" \
"v_mov_b32 v11, v17\n" \
"v_mfma_f32_32x32x16_bf16 v[32:47], v[92:95], v[212:215], v[32:47]\n" \
"ds_read_b128 v[92:95], v0 offset:21504\n" \
"s_waitcnt vmcnt(8)\n" \
"v_bitop3_b32 v12, v216, v220, 1 bitop3:42\n" \
"v_cmp_ne_u32 vcc, 0, v12\n" \
"v_xor_b32 v8, 0x80000000, v16\n" \
"s_waitcnt lgkmcnt(2)\n" \
"v_mfma_f32_32x32x16_bf16 v[48:63], v[80:83], v[212:215], v[48:63]\n" \
"ds_read_b128 v[80:83], v0 offset:31744\n" \
"s_cbranch_vccnz .Lc97_%=\n" \
".Lc98_%=:\n" \
"v_mul_f32 v9, 0xc0135761, v18\n" \
"v_mul_f32 v18, v18, v18\n" \
"v_mfma_f32_32x32x16_bf16 v[32:47], v[84:87], v[216:219], v[32:47]\n" \
"ds_read_b128 v[84:87], v0 offset:22528\n" \
"v_mul_f32 v18, 0x3d372713, v18\n" \
"v_mul_f32 v10, v9, v18\n" \
"s_waitcnt lgkmcnt(2)\n" \
"v_mfma_f32_32x32x16_bf16 v[48:63], v[88:91], v[216:219], v[48:63]\n" \
"ds_read_b128 v[88:91], v0 offset:32768\n" \
"v_mfma_f32_32x32x16_bf16 v[32:47], v[92:95], v[220:223], v[32:47]\n" \
"ds_read_b128 v[92:95], v0 offset:23552\n" \
"s_waitcnt vmcnt(6)\n" \
"v_bitop3_b32 v12, v224, v228, 1 bitop3:42\n" \
"v_cmp_ne_u32 vcc, 0, v12\n" \
"s_waitcnt lgkmcnt(2)\n" \
"v_mfma_f32_32x32x16_bf16 v[48:63], v[80:83], v[220:223], v[48:63]\n" \
"ds_read_b128 v[80:83], v0 offset:33792\n" \
"s_cbranch_vccnz .Lc99_%=\n" \
".Lc100_%=:\n" \
"v_mfma_f32_32x32x16_bf16 v[32:47], v[84:87], v[224:227], v[32:47]\n" \
"ds_read_b128 v[84:87], v0 offset:24576\n" \
"s_waitcnt lgkmcnt(2)\n" \
"v_mfma_f32_32x32x16_bf16 v[48:63], v[88:91], v[224:227], v[48:63]\n" \
"ds_read_b128 v[88:91], v0 offset:34816\n" \
"v_mfma_f32_32x32x16_bf16 v[32:47], v[92:95], v[228:231], v[32:47]\n" \
"ds_read_b128 v[92:95], v0 offset:25600\n" \
"s_waitcnt vmcnt(4)\n" \
"v_bitop3_b32 v12, v232, v236, 1 bitop3:42\n" \
"v_cmp_ne_u32 vcc, 0, v12\n" \
"s_waitcnt lgkmcnt(2)\n" \
"v_mfma_f32_32x32x16_bf16 v[48:63], v[80:83], v[228:231], v[48:63]\n" \
"ds_read_b128 v[80:83], v0 offset:35840\n" \
"s_cbranch_vccnz .Lc101_%=\n" \
".Lc102_%=:\n" \
"v_mfma_f32_32x32x16_bf16 v[32:47], v[84:87], v[232:235], v[32:47]\n" \
"ds_read_b128 v[84:87], v0 offset:26624\n" \
"s_waitcnt lgkmcnt(2)\n" \
"v_mfma_f32_32x32x16_bf16 v[48:63], v[88:91], v[232:235], v[48:63]\n" \
"ds_read_b128 v[88:91], v0 offset:36864\n" \
"v_mfma_f32_32x32x16_bf16 v[32:47], v[92:95], v[236:239], v[32:47]\n" \
"ds_read_b128 v[92:95], v0 offset:27648\n" \
"s_waitcnt vmcnt(2)\n" \
"v_bitop3_b32 v12, v240, v244, 1 bitop3:42\n" \
"v_cmp_ne_u32 vcc, 0, v12\n" \
"s_waitcnt lgkmcnt(2)\n" \
"v_mfma_f32_32x32x16_bf16 v[48:63], v[80:83], v[236:239], v[48:63]\n" \
"ds_read_b128 v[80:83], v0 offset:37888\n" \
"s_cbranch_vccnz .Lc103_%=\n" \
".Lc104_%=:\n" \
"v_mfma_f32_32x32x16_bf16 v[32:47], v[84:87], v[240:243], v[32:47]\n" \
"ds_read_b128 v[84:87], v0 offset:28672\n" \
"s_waitcnt lgkmcnt(2)\n" \
"v_mfma_f32_32x32x16_bf16 v[48:63], v[88:91], v[240:243], v[48:63]\n" \
"ds_read_b128 v[88:91], v0 offset:38912\n" \
"v_mfma_f32_32x32x16_bf16 v[32:47], v[92:95], v[244:247], v[32:47]\n" \
"ds_read_b128 v[92:95], v0 offset:29696\n" \
"s_waitcnt vmcnt(0)\n" \
"v_bitop3_b32 v12, v248, v252, 1 bitop3:42\n" \
"v_cmp_ne_u32 vcc, 0, v12\n" \
"s_waitcnt lgkmcnt(2)\n" \
"v_mfma_f32_32x32x16_bf16 v[48:63], v[80:83], v[244:247], v[48:63]\n" \
"ds_read_b128 v[80:83], v0 offset:39936\n" \
"s_cbranch_vccnz .Lc105_%=\n" \
".Lc106_%=:\n" \
"v_mfma_f32_32x32x16_bf16 v[32:47], v[84:87], v[248:251], v[32:47]\n" \
"ds_read_b128 v[84:87], v1 offset:23552\n" \
"s_waitcnt lgkmcnt(2)\n" \
"v_mfma_f32_32x32x16_bf16 v[48:63], v[88:91], v[248:251], v[48:63]\n" \
"ds_read_b128 v[88:91], v1 offset:24576\n" \
"v_mfma_f32_32x32x16_bf16 v[32:47], v[92:95], v[252:255], v[32:47]\n" \
"ds_read_b128 v[92:95], v1 offset:25600\n" \
"s_waitcnt lgkmcnt(2)\n" \
"v_mfma_f32_32x32x16_bf16 v[48:63], v[80:83], v[252:255], v[48:63]\n" \
"ds_read_b128 v[80:83], v1 offset:26624\n" \
"ds_read_b128 v[20:23], v7 offset:512\n" \
"ds_read_b128 v[24:27], v7 offset:576\n" \
"s_barrier\n" \
"v_mfma_f32_32x32x16_bf16 v[64:79], v[84:87], v[148:151], v[64:79]\n" \
"ds_read_b128 v[84:87], v1 offset:27648\n" \
"s_waitcnt lgkmcnt(4)\n" \
"v_mfma_f32_32x32x16_bf16 v[64:79], v[88:91], v[152:155], v[64:79]\n" \
"ds_read_b128 v[88:91], v1 offset:28672\n" \
"s_waitcnt lgkmcnt(3)\n" \
"v_fmac_f32 v32, v8, v20\n" \
"v_fmac_f32 v33, v8, v21\n" \
"v_mul_f32 v16, v32, v32\n" \
"v_mul_f32 v17, v33, v33\n" \
"v_mfma_f32_32x32x16_bf16 v[64:79], v[92:95], v[156:159], v[64:79]\n" \
"ds_read_b128 v[92:95], v1 offset:29696\n" \
"v_fma_f32 v16, v16, v10, v9\n" \
"v_fma_f32 v17, v17, v10, v9\n" \
"v_mul_f32 v16, v16, v32\n" \
"v_mul_f32 v17, v17, v33\n" \
"s_waitcnt lgkmcnt(2)\n" \
"v_mfma_f32_32x32x16_bf16 v[64:79], v[80:83], v[160:163], v[64:79]\n" \
"ds_read_b128 v[80:83], v0 offset:40960\n" \
"v_exp_f32 v16, v16\n" \
"v_fmac_f32 v40, v8, v24\n" \
"v_fmac_f32 v41, v8, v25\n" \
"v_mfma_f32_32x32x16_bf16 v[64:79], v[84:87], v[164:167], v[64:79]\n" \
"ds_read_b128 v[84:87], v0 offset:41984\n" \
"v_exp_f32 v17, v17\n" \
"v_fma_f32 v16, v16, v11, v11\n" \
"v_mul_f32 v32, v32, v40\n" \
"s_waitcnt lgkmcnt(2)\n" \
"v_mfma_f32_32x32x16_bf16 v[64:79], v[88:91], v[168:171], v[64:79]\n" \
"ds_read_b128 v[88:91], v0 offset:43008\n" \
"v_rcp_f32 v16, v16\n" \
"v_fma_f32 v17, v17, v11, v11\n" \
"v_mul_f32 v33, v33, v41\n" \
"v_mfma_f32_32x32x16_bf16 v[64:79], v[92:95], v[172:175], v[64:79]\n" \
"ds_read_b128 v[92:95], v0 offset:44032\n" \
"v_rcp_f32 v17, v17\n" \
"v_mul_f32 v32, v32, v16\n" \
"v_mul_f32 v33, v33, v17\n" \
"s_waitcnt lgkmcnt(2)\n" \
"v_mfma_f32_32x32x16_bf16 v[64:79], v[80:83], v[176:179], v[64:79]\n" \
"ds_read_b128 v[80:83], v0 offset:45056\n" \
"v_fmac_f32 v34, v8, v22\n" \
"v_fmac_f32 v35, v8, v23\n" \
"v_cvt_pk_bf16_f32 v28, v32, v33\n" \
"ds_read_b128 v[20:23], v7 offset:544\n" \
"v_mul_f32 v16, v34, v34\n" \
"v_mul_f32 v17, v35, v35\n" \
"v_mfma_f32_32x32x16_bf16 v[64:79], v[84:87], v[180:183], v[64:79]\n" \
"ds_read_b128 v[84:87], v0 offset:46080\n" \
"v_fma_f32 v16, v16, v10, v9\n" \
"v_fma_f32 v17, v17, v10, v9\n" \
"v_mul_f32 v16, v16, v34\n" \
"v_mul_f32 v17, v17, v35\n" \
"s_waitcnt lgkmcnt(3)\n" \
"v_mfma_f32_32x32x16_bf16 v[64:79], v[88:91], v[184:187], v[64:79]\n" \
"ds_read_b128 v[88:91], v0 offset:47104\n" \
"v_exp_f32 v16, v16\n" \
"v_fmac_f32 v42, v8, v26\n" \
"v_fmac_f32 v43, v8, v27\n" \
"v_mfma_f32_32x32x16_bf16 v[64:79], v[92:95], v[188:191], v[64:79]\n" \
"ds_read_b128 v[92:95], v0 offset:48128\n" \
"ds_read_b128 v[24:27], v7 offset:608\n" \
"v_exp_f32 v17, v17\n" \
"v_fma_f32 v16, v16, v11, v11\n" \
"v_mul_f32 v34, v34, v42\n" \
"s_waitcnt lgkmcnt(3)\n" \
"v_mfma_f32_32x32x16_bf16 v[64:79], v[80:83], v[192:195], v[64:79]\n" \
"ds_read_b128 v[80:83], v0 offset:49152\n" \
"v_rcp_f32 v16, v16\n" \
"v_fma_f32 v17, v17, v11, v11\n" \
"v_mul_f32 v35, v35, v43\n" \
"v_mfma_f32_32x32x16_bf16 v[64:79], v[84:87], v[196:199], v[64:79]\n" \
"ds_read_b128 v[84:87], v0 offset:50176\n" \
"v_rcp_f32 v17, v17\n" \
"v_mul_f32 v34, v34, v16\n" \
"v_mul_f32 v35, v35, v17\n" \
"s_waitcnt lgkmcnt(3)\n" \
"v_mfma_f32_32x32x16_bf16 v[64:79], v[88:91], v[200:203], v[64:79]\n" \
"ds_read_b128 v[88:91], v0 offset:51200\n" \
"v_fmac_f32 v36, v8, v20\n" \
"v_fmac_f32 v37, v8, v21\n" \
"v_cvt_pk_bf16_f32 v29, v34, v35\n" \
"v_mul_f32 v16, v36, v36\n" \
"v_mul_f32 v17, v37, v37\n" \
"v_mfma_f32_32x32x16_bf16 v[64:79], v[92:95], v[204:207], v[64:79]\n" \
"ds_read_b128 v[92:95], v0 offset:52224\n" \
"v_fma_f32 v16, v16, v10, v9\n" \
"v_fma_f32 v17, v17, v10, v9\n" \
"v_mul_f32 v16, v16, v36\n" \
"v_mul_f32 v17, v17, v37\n" \
"s_waitcnt lgkmcnt(2)\n" \
"v_mfma_f32_32x32x16_bf16 v[64:79], v[80:83], v[208:211], v[64:79]\n" \
"ds_read_b128 v[80:83], v0 offset:53248\n" \
"v_exp_f32 v16, v16\n" \
"v_fmac_f32 v44, v8, v24\n" \
"v_fmac_f32 v45, v8, v25\n" \
"v_mfma_f32_32x32x16_bf16 v[64:79], v[84:87], v[212:215], v[64:79]\n" \
"ds_read_b128 v[84:87], v0 offset:54272\n" \
"v_exp_f32 v17, v17\n" \
"v_fma_f32 v16, v16, v11, v11\n" \
"v_mul_f32 v36, v36, v44\n" \
"s_waitcnt lgkmcnt(2)\n" \
"v_mfma_f32_32x32x16_bf16 v[64:79], v[88:91], v[216:219], v[64:79]\n" \
"ds_read_b128 v[88:91], v0 offset:55296\n" \
"v_rcp_f32 v16, v16\n" \
"v_fma_f32 v17, v17, v11, v11\n" \
"v_mul_f32 v37, v37, v45\n" \
"v_mfma_f32_32x32x16_bf16 v[64:79], v[92:95], v[220:223], v[64:79]\n" \
"ds_read_b128 v[92:95], v0 offset:56320\n" \
"v_rcp_f32 v17, v17\n" \
"v_mul_f32 v36, v36, v16\n" \
"v_mul_f32 v37, v37, v17\n" \
"s_waitcnt lgkmcnt(2)\n" \
"v_mfma_f32_32x32x16_bf16 v[64:79], v[80:83], v[224:227], v[64:79]\n" \
"ds_read_b128 v[80:83], v0 offset:57344\n" \
"v_fmac_f32 v38, v8, v22\n" \
"v_fmac_f32 v39, v8, v23\n" \
"v_cvt_pk_bf16_f32 v30, v36, v37\n" \
"v_mul_f32 v16, v38, v38\n" \
"v_mul_f32 v17, v39, v39\n" \
"v_mfma_f32_32x32x16_bf16 v[64:79], v[84:87], v[228:231], v[64:79]\n" \
"ds_read_b128 v[84:87], v0 offset:58368\n" \
"v_fma_f32 v16, v16, v10, v9\n" \
"v_fma_f32 v17, v17, v10, v9\n" \
"v_mul_f32 v16, v16, v38\n" \
"v_mul_f32 v17, v17, v39\n" \
"s_waitcnt lgkmcnt(2)\n" \
"v_mfma_f32_32x32x16_bf16 v[64:79], v[88:91], v[232:235], v[64:79]\n" \
"ds_read_b128 v[88:91], v0 offset:59392\n" \
"v_exp_f32 v16, v16\n" \
"v_fmac_f32 v46, v8, v26\n" \
"v_fmac_f32 v47, v8, v27\n" \
"v_mfma_f32_32x32x16_bf16 v[64:79], v[92:95], v[236:239], v[64:79]\n" \
"ds_read_b128 v[92:95], v0 offset:60416\n" \
"v_exp_f32 v17, v17\n" \
"v_fma_f32 v16, v16, v11, v11\n" \
"v_mul_f32 v38, v38, v46\n" \
"s_waitcnt lgkmcnt(2)\n" \
"v_mfma_f32_32x32x16_bf16 v[64:79], v[80:83], v[240:243], v[64:79]\n" \
"ds_read_b128 v[80:83], v1 offset:0\n" \
"v_rcp_f32 v16, v16\n" \
"v_fma_f32 v17, v17, v11, v11\n" \
"v_mul_f32 v39, v39, v47\n" \
"v_mfma_f32_32x32x16_bf16 v[64:79], v[84:87], v[244:247], v[64:79]\n" \
"ds_read_b128 v[84:87], v1 offset:1024\n" \
"v_rcp_f32 v17, v17\n" \
"v_mul_f32 v38, v38, v16\n" \
"v_mul_f32 v39, v39, v17\n" \
"s_waitcnt lgkmcnt(2)\n" \
"v_mfma_f32_32x32x16_bf16 v[64:79], v[88:91], v[248:251], v[64:79]\n" \
"ds_read_b128 v[88:91], v1 offset:2048\n" \
"v_cvt_pk_bf16_f32 v31, v38, v39\n" \
"v_bfi_b32 v28, 1, s24, v28\n" \
"buffer_store_dwordx4 v[28:31], v6, %0, s52 offen sc1\n" \
"v_mfma_f32_32x32x16_bf16 v[64:79], v[92:95], v[252:255], v[64:79]\n" \
"ds_read_b128 v[92:95], v1 offset:3072\n" \
"s_add_u32 s52, s52, 4096\n" \
"ds_read_b128 v[20:23], v7 offset:640\n" \
"ds_read_b128 v[24:27], v7 offset:704\n" \
"s_barrier\n" \
"s_waitcnt lgkmcnt(4)\n" \
"v_mfma_f32_32x32x16_bf16 v[32:47], v[80:83], v[96:99], 0\n" \
"ds_read_b128 v[80:83], v1 offset:4096\n" \
"v_mfma_f32_32x32x16_bf16 v[32:47], v[84:87], v[100:103], v[32:47]\n" \
"ds_read_b128 v[84:87], v1 offset:5120\n" \
"s_waitcnt lgkmcnt(3)\n" \
"v_fmac_f32 v48, v8, v20\n" \
"v_fmac_f32 v49, v8, v21\n" \
"v_mul_f32 v16, v48, v48\n" \
"v_mul_f32 v17, v49, v49\n" \
"v_mfma_f32_32x32x16_bf16 v[32:47], v[88:91], v[104:107], v[32:47]\n" \
"ds_read_b128 v[88:91], v1 offset:6144\n" \
"v_fma_f32 v16, v16, v10, v9\n" \
"v_fma_f32 v17, v17, v10, v9\n" \
"v_mul_f32 v16, v16, v48\n" \
"v_mul_f32 v17, v17, v49\n" \
"v_mfma_f32_32x32x16_bf16 v[32:47], v[92:95], v[108:111], v[32:47]\n" \
"ds_read_b128 v[92:95], v1 offset:7168\n" \
"v_exp_f32 v16, v16\n" \
"s_waitcnt lgkmcnt(4)\n" \
"v_fmac_f32 v56, v8, v24\n" \
"v_fmac_f32 v57, v8, v25\n" \
"s_waitcnt lgkmcnt(2)\n" \
"v_mfma_f32_32x32x16_bf16 v[32:47], v[80:83], v[112:115], v[32:47]\n" \
"ds_read_b128 v[80:83], v1 offset:8192\n" \
"v_exp_f32 v17, v17\n" \
"v_fma_f32 v16, v16, v11, v11\n" \
"v_mul_f32 v48, v48, v56\n" \
"v_mfma_f32_32x32x16_bf16 v[32:47], v[84:87], v[116:119], v[32:47]\n" \
"ds_read_b128 v[84:87], v1 offset:9216\n" \
"v_rcp_f32 v16, v16\n" \
"v_fma_f32 v17, v17, v11, v11\n" \
"v_mul_f32 v49, v49, v57\n" \
"s_waitcnt lgkmcnt(2)\n" \
"v_mfma_f32_32x32x16_bf16 v[32:47], v[88:91], v[120:123], v[32:47]\n" \
"ds_read_b128 v[88:91], v1 offset:10240\n" \
"v_rcp_f32 v17, v17\n" \
"v_mul_f32 v48, v48, v16\n" \
"v_mul_f32 v49, v49, v17\n" \
"v_mfma_f32_32x32x16_bf16 v[32:47], v[92:95], v[124:127], v[32:47]\n" \
"ds_read_b128 v[92:95], v1 offset:11264\n" \
"v_fmac_f32 v50, v8, v22\n" \
"v_fmac_f32 v51, v8, v23\n" \
"v_cvt_pk_bf16_f32 v28, v48, v49\n" \
"ds_read_b128 v[20:23], v7 offset:672\n" \
"v_mul_f32 v16, v50, v50\n" \
"v_mul_f32 v17, v51, v51\n" \
"s_waitcnt lgkmcnt(3)\n" \
"v_mfma_f32_32x32x16_bf16 v[32:47], v[80:83], v[128:131], v[32:47]\n" \
"ds_read_b128 v[80:83], v1 offset:12288\n" \
"v_fma_f32 v16, v16, v10, v9\n" \
"v_fma_f32 v17, v17, v10, v9\n" \
"v_mul_f32 v16, v16, v50\n" \
"v_mul_f32 v17, v17, v51\n" \
"v_mfma_f32_32x32x16_bf16 v[32:47], v[84:87], v[132:135], v[32:47]\n" \
"ds_read_b128 v[84:87], v1 offset:13312\n" \
"v_exp_f32 v16, v16\n" \
"v_fmac_f32 v58, v8, v26\n" \
"v_fmac_f32 v59, v8, v27\n" \
"s_waitcnt lgkmcnt(3)\n" \
"v_mfma_f32_32x32x16_bf16 v[32:47], v[88:91], v[136:139], v[32:47]\n" \
"ds_read_b128 v[88:91], v1 offset:14336\n" \
"ds_read_b128 v[24:27], v7 offset:736\n" \
"v_exp_f32 v17, v17\n" \
"v_fma_f32 v16, v16, v11, v11\n" \
"v_mul_f32 v50, v50, v58\n" \
"v_mfma_f32_32x32x16_bf16 v[32:47], v[92:95], v[140:143], v[32:47]\n" \
"ds_read_b128 v[92:95], v1 offset:15360\n" \
"v_rcp_f32 v16, v16\n" \
"v_fma_f32 v17, v17, v11, v11\n" \
"v_mul_f32 v51, v51, v59\n" \
"s_waitcnt lgkmcnt(3)\n" \
"v_mfma_f32_32x32x16_bf16 v[32:47], v[80:83], v[144:147], v[32:47]\n" \
"ds_read_b128 v[80:83], v1 offset:16384\n" \
"v_rcp_f32 v17, v17\n" \
"v_mul_f32 v50, v50, v16\n" \
"v_mul_f32 v51, v51, v17\n" \
"v_mfma_f32_32x32x16_bf16 v[32:47], v[84:87], v[148:151], v[32:47]\n" \
"ds_read_b128 v[84:87], v1 offset:17408\n" \
"v_fmac_f32 v52, v8, v20\n" \
"v_fmac_f32 v53, v8, v21\n" \
"v_cvt_pk_bf16_f32 v29, v50, v51\n" \
"v_mul_f32 v16, v52, v52\n" \
"v_mul_f32 v17, v53, v53\n" \
"s_waitcnt lgkmcnt(2)\n" \
"v_mfma_f32_32x32x16_bf16 v[32:47], v[88:91], v[152:155], v[32:47]\n" \
"ds_read_b128 v[88:91], v1 offset:18432\n" \
"v_fma_f32 v16, v16, v10, v9\n" \
"v_fma_f32 v17, v17, v10, v9\n" \
"v_mul_f32 v16, v16, v52\n" \
"v_mul_f32 v17, v17, v53\n" \
"v_mfma_f32_32x32x16_bf16 v[32:47], v[92:95], v[156:159], v[32:47]\n" \
"ds_read_b128 v[92:95], v1 offset:19456\n" \
"v_exp_f32 v16, v16\n" \
"v_fmac_f32 v60, v8, v24\n" \
"v_fmac_f32 v61, v8, v25\n" \
"s_waitcnt lgkmcnt(2)\n" \
"v_mfma_f32_32x32x16_bf16 v[32:47], v[80:83], v[160:163], v[32:47]\n" \
"ds_read_b128 v[80:83], v1 offset:30720\n" \
"v_exp_f32 v17, v17\n" \
"v_fma_f32 v16, v16, v11, v11\n" \
"v_mul_f32 v52, v52, v60\n" \
"v_mfma_f32_32x32x16_bf16 v[32:47], v[84:87], v[164:167], v[32:47]\n" \
"ds_read_b128 v[84:87], v1 offset:31744\n" \
"v_rcp_f32 v16, v16\n" \
"v_fma_f32 v17, v17, v11, v11\n" \
"v_mul_f32 v53, v53, v61\n" \
"s_waitcnt lgkmcnt(2)\n" \
"v_mfma_f32_32x32x16_bf16 v[32:47], v[88:91], v[168:171], v[32:47]\n" \
"ds_read_b128 v[88:91], v1 offset:32768\n" \
"v_rcp_f32 v17, v17\n" \
"v_mul_f32 v52, v52, v16\n" \
"v_mul_f32 v53, v53, v17\n" \
"v_mfma_f32_32x32x16_bf16 v[32:47], v[92:95], v[172:175], v[32:47]\n" \
"ds_read_b128 v[92:95], v1 offset:33792\n" \
"v_fmac_f32 v54, v8, v22\n" \
"v_fmac_f32 v55, v8, v23\n" \
"v_cvt_pk_bf16_f32 v30, v52, v53\n" \
"v_mul_f32 v16, v54, v54\n" \
"v_mul_f32 v17, v55, v55\n" \
"s_waitcnt lgkmcnt(2)\n" \
"v_mfma_f32_32x32x16_bf16 v[32:47], v[80:83], v[176:179], v[32:47]\n" \
"ds_read_b128 v[80:83], v1 offset:34816\n" \
"v_fma_f32 v16, v16, v10, v9\n" \
"v_fma_f32 v17, v17, v10, v9\n" \
"v_mul_f32 v16, v16, v54\n" \
"v_mul_f32 v17, v17, v55\n" \
"v_mfma_f32_32x32x16_bf16 v[32:47], v[84:87], v[180:183], v[32:47]\n" \
"ds_read_b128 v[84:87], v1 offset:35840\n" \
"v_exp_f32 v16, v16\n" \
"v_fmac_f32 v62, v8, v26\n" \
"v_fmac_f32 v63, v8, v27\n" \
"s_waitcnt lgkmcnt(2)\n" \
"v_mfma_f32_32x32x16_bf16 v[32:47], v[88:91], v[184:187], v[32:47]\n" \
"ds_read_b128 v[88:91], v1 offset:36864\n" \
"v_exp_f32 v17, v17\n" \
"v_fma_f32 v16, v16, v11, v11\n" \
"v_mul_f32 v54, v54, v62\n" \
"v_mfma_f32_32x32x16_bf16 v[32:47], v[92:95], v[188:191], v[32:47]\n" \
"ds_read_b128 v[92:95], v1 offset:37888\n" \
"v_rcp_f32 v16, v16\n" \
"v_fma_f32 v17, v17, v11, v11\n" \
"v_mul_f32 v55, v55, v63\n" \
"s_waitcnt lgkmcnt(2)\n" \
"v_mfma_f32_32x32x16_bf16 v[32:47], v[80:83], v[192:195], v[32:47]\n" \
"ds_read_b128 v[80:83], v1 offset:38912\n" \
"v_rcp_f32 v17, v17\n" \
"v_mul_f32 v54, v54, v16\n" \
"v_mul_f32 v55, v55, v17\n" \
"v_mfma_f32_32x32x16_bf16 v[32:47], v[84:87], v[196:199], v[32:47]\n" \
"ds_read_b128 v[84:87], v1 offset:39936\n" \
"v_cvt_pk_bf16_f32 v31, v54, v55\n" \
"v_bfi_b32 v28, 1, s24, v28\n" \
"buffer_store_dwordx4 v[28:31], v6, %0, s52 offen sc1\n" \
"s_waitcnt lgkmcnt(2)\n" \
"v_mfma_f32_32x32x16_bf16 v[32:47], v[88:91], v[200:203], v[32:47]\n" \
"ds_read_b128 v[88:91], v1 offset:40960\n" \
"s_add_u32 s52, s52, 4096\n" \
"ds_read_b128 v[20:23], v7 offset:768\n" \
"ds_read_b128 v[24:27], v7 offset:832\n" \
"v_mfma_f32_32x32x16_bf16 v[32:47], v[92:95], v[204:207], v[32:47]\n" \
"ds_read_b128 v[92:95], v1 offset:41984\n" \
"s_waitcnt lgkmcnt(4)\n" \
"v_mfma_f32_32x32x16_bf16 v[32:47], v[80:83], v[208:211], v[32:47]\n" \
"ds_read_b128 v[80:83], v1 offset:43008\n" \
"s_waitcnt lgkmcnt(3)\n" \
"v_fmac_f32 v64, v8, v20\n" \
"v_fmac_f32 v65, v8, v21\n" \
"v_mul_f32 v16, v64, v64\n" \
"v_mul_f32 v17, v65, v65\n" \
"v_mfma_f32_32x32x16_bf16 v[32:47], v[84:87], v[212:215], v[32:47]\n" \
"ds_read_b128 v[84:87], v1 offset:44032\n" \
"v_fma_f32 v16, v16, v10, v9\n" \
"v_fma_f32 v17, v17, v10, v9\n" \
"v_mul_f32 v16, v16, v64\n" \
"v_mul_f32 v17, v17, v65\n" \
"s_waitcnt lgkmcnt(2)\n" \
"v_mfma_f32_32x32x16_bf16 v[32:47], v[88:91], v[216:219], v[32:47]\n" \
"ds_read_b128 v[88:91], v1 offset:45056\n" \
"v_exp_f32 v16, v16\n" \
"v_fmac_f32 v72, v8, v24\n" \
"v_fmac_f32 v73, v8, v25\n" \
"v_mfma_f32_32x32x16_bf16 v[32:47], v[92:95], v[220:223], v[32:47]\n" \
"ds_read_b128 v[92:95], v1 offset:46080\n" \
"v_exp_f32 v17, v17\n" \
"v_fma_f32 v16, v16, v11, v11\n" \
"v_mul_f32 v64, v64, v72\n" \
"s_waitcnt lgkmcnt(2)\n" \
"v_mfma_f32_32x32x16_bf16 v[32:47], v[80:83], v[224:227], v[32:47]\n" \
"ds_read_b128 v[80:83], v1 offset:47104\n" \
"v_rcp_f32 v16, v16\n" \
"v_fma_f32 v17, v17, v11, v11\n" \
"v_mul_f32 v65, v65, v73\n" \
"v_mfma_f32_32x32x16_bf16 v[32:47], v[84:87], v[228:231], v[32:47]\n" \
"ds_read_b128 v[84:87], v1 offset:48128\n" \
"v_rcp_f32 v17, v17\n" \
"v_mul_f32 v64, v64, v16\n" \
"v_mul_f32 v65, v65, v17\n" \
"s_waitcnt lgkmcnt(2)\n" \
"v_mfma_f32_32x32x16_bf16 v[32:47], v[88:91], v[232:235], v[32:47]\n" \
"ds_read_b128 v[88:91], v1 offset:49152\n" \
"v_fmac_f32 v66, v8, v22\n" \
"v_fmac_f32 v67, v8, v23\n" \
"v_cvt_pk_bf16_f32 v28, v64, v65\n" \
"ds_read_b128 v[20:23], v7 offset:800\n" \
"v_mul_f32 v16, v66, v66\n" \
"v_mul_f32 v17, v67, v67\n" \
"v_mfma_f32_32x32x16_bf16 v[32:47], v[92:95], v[236:239], v[32:47]\n" \
"ds_read_b128 v[92:95], v1 offset:50176\n" \
"v_fma_f32 v16, v16, v10, v9\n" \
"v_fma_f32 v17, v17, v10, v9\n" \
"v_mul_f32 v16, v16, v66\n" \
"v_mul_f32 v17, v17, v67\n" \
"s_waitcnt lgkmcnt(3)\n" \
"v_mfma_f32_32x32x16_bf16 v[32:47], v[80:83], v[240:243], v[32:47]\n" \
"ds_read_b128 v[80:83], v1 offset:51200\n" \
"v_exp_f32 v16, v16\n" \
"v_fmac_f32 v74, v8, v26\n" \
"v_fmac_f32 v75, v8, v27\n" \
"v_mfma_f32_32x32x16_bf16 v[32:47], v[84:87], v[244:247], v[32:47]\n" \
"ds_read_b128 v[84:87], v1 offset:52224\n" \
"ds_read_b128 v[24:27], v7 offset:864\n" \
"v_exp_f32 v17, v17\n" \
"v_fma_f32 v16, v16, v11, v11\n" \
"v_mul_f32 v66, v66, v74\n" \
"s_waitcnt lgkmcnt(3)\n" \
"v_mfma_f32_32x32x16_bf16 v[32:47], v[88:91], v[248:251], v[32:47]\n" \
"ds_read_b128 v[88:91], v1 offset:53248\n" \
"v_rcp_f32 v16, v16\n" \
"v_fma_f32 v17, v17, v11, v11\n" \
"v_mul_f32 v67, v67, v75\n" \
"v_mfma_f32_32x32x16_bf16 v[32:47], v[92:95], v[252:255], v[32:47]\n" \
"ds_read_b128 v[92:95], v1 offset:54272\n" \
"v_rcp_f32 v17, v17\n" \
"v_mul_f32 v66, v66, v16\n" \
"v_mul_f32 v67, v67, v17\n" \
"s_barrier\n" \
"s_waitcnt lgkmcnt(3)\n" \
"v_mfma_f32_32x32x16_bf16 v[48:63], v[80:83], v[96:99], 0\n" \
"ds_read_b128 v[80:83], v1 offset:55296\n" \
"v_fmac_f32 v68, v8, v20\n" \
"v_fmac_f32 v69, v8, v21\n" \
"v_cvt_pk_bf16_f32 v29, v66, v67\n" \
"v_mul_f32 v16, v68, v68\n" \
"v_mul_f32 v17, v69, v69\n" \
"v_mfma_f32_32x32x16_bf16 v[48:63], v[84:87], v[100:103], v[48:63]\n" \
"ds_read_b128 v[84:87], v1 offset:56320\n" \
"v_fma_f32 v16, v16, v10, v9\n" \
"v_fma_f32 v17, v17, v10, v9\n" \
"v_mul_f32 v16, v16, v68\n" \
"v_mul_f32 v17, v17, v69\n" \
"s_waitcnt lgkmcnt(2)\n" \
"v_mfma_f32_32x32x16_bf16 v[48:63], v[88:91], v[104:107], v[48:63]\n" \
"ds_read_b128 v[88:91], v1 offset:57344\n" \
"v_exp_f32 v16, v16\n" \
"v_fmac_f32 v76, v8, v24\n" \
"v_fmac_f32 v77, v8, v25\n" \
"v_mfma_f32_32x32x16_bf16 v[48:63], v[92:95], v[108:111], v[48:63]\n" \
"ds_read_b128 v[92:95], v1 offset:58368\n" \
"v_exp_f32 v17, v17\n" \
"v_fma_f32 v16, v16, v11, v11\n" \
"v_mul_f32 v68, v68, v76\n" \
"s_waitcnt lgkmcnt(2)\n" \
"v_mfma_f32_32x32x16_bf16 v[48:63], v[80:83], v[112:115], v[48:63]\n" \
"ds_read_b128 v[80:83], v1 offset:59392\n" \
"v_rcp_f32 v16, v16\n" \
"v_fma_f32 v17, v17, v11, v11\n" \
"v_mul_f32 v69, v69, v77\n" \
"v_mfma_f32_32x32x16_bf16 v[48:63], v[84:87], v[116:119], v[48:63]\n" \
"ds_read_b128 v[84:87], v1 offset:60416\n" \
"v_rcp_f32 v17, v17\n" \
"v_mul_f32 v68, v68, v16\n" \
"v_mul_f32 v69, v69, v17\n" \
"s_waitcnt lgkmcnt(2)\n" \
"v_mfma_f32_32x32x16_bf16 v[48:63], v[88:91], v[120:123], v[48:63]\n" \
"ds_read_b128 v[88:91], v2 offset:0\n" \
"v_fmac_f32 v70, v8, v22\n" \
"v_fmac_f32 v71, v8, v23\n" \
"v_cvt_pk_bf16_f32 v30, v68, v69\n" \
"v_mul_f32 v16, v70, v70\n" \
"v_mul_f32 v17, v71, v71\n" \
"v_mfma_f32_32x32x16_bf16 v[48:63], v[92:95], v[124:127], v[48:63]\n" \
"ds_read_b128 v[92:95], v2 offset:1024\n" \
"v_fma_f32 v16, v16, v10, v9\n" \
"v_fma_f32 v17, v17, v10, v9\n" \
"v_mul_f32 v16, v16, v70\n" \
"v_mul_f32 v17, v17, v71\n" \
"s_waitcnt lgkmcnt(2)\n" \
"v_mfma_f32_32x32x16_bf16 v[48:63], v[80:83], v[128:131], v[48:63]\n" \
"ds_read_b128 v[80:83], v2 offset:2048\n" \
"v_exp_f32 v16, v16\n" \
"v_fmac_f32 v78, v8, v26\n" \
"v_fmac_f32 v79, v8, v27\n" \
"v_mfma_f32_32x32x16_bf16 v[48:63], v[84:87], v[132:135], v[48:63]\n" \
"ds_read_b128 v[84:87], v2 offset:3072\n" \
"v_exp_f32 v17, v17\n" \
"v_fma_f32 v16, v16, v11, v11\n" \
"v_mul_f32 v70, v70, v78\n" \
"s_waitcnt lgkmcnt(2)\n" \
"v_mfma_f32_32x32x16_bf16 v[48:63], v[88:91], v[136:139], v[48:63]\n" \
"ds_read_b128 v[88:91], v2 offset:4096\n" \
"v_rcp_f32 v16, v16\n" \
"v_fma_f32 v17, v17, v11, v11\n" \
"v_mul_f32 v71, v71, v79\n" \
"v_mfma_f32_32x32x16_bf16 v[48:63], v[92:95], v[140:143], v[48:63]\n" \
"ds_read_b128 v[92:95], v2 offset:5120\n" \
"v_rcp_f32 v17, v17\n" \
"v_mul_f32 v70, v70, v16\n" \
"v_mul_f32 v71, v71, v17\n" \
"s_waitcnt lgkmcnt(2)\n" \
"v_mfma_f32_32x32x16_bf16 v[48:63], v[80:83], v[144:147], v[48:63]\n" \
"ds_read_b128 v[80:83], v2 offset:6144\n" \
"v_cvt_pk_bf16_f32 v31, v70, v71\n" \
"v_bfi_b32 v28, 1, s24, v28\n" \
"buffer_store_dwordx4 v[28:31], v6, %0, s52 offen sc1\n" \
"v_mfma_f32_32x32x16_bf16 v[48:63], v[84:87], v[148:151], v[48:63]\n" \
"ds_read_b128 v[84:87], v2 offset:7168\n" \
"s_add_u32 s52, s52, 4096\n" \
"ds_read_b128 v[20:23], v7 offset:896\n" \
"ds_read_b128 v[24:27], v7 offset:960\n" \
"s_waitcnt lgkmcnt(4)\n" \
"v_mfma_f32_32x32x16_bf16 v[48:63], v[88:91], v[152:155], v[48:63]\n" \
"ds_read_b128 v[88:91], v2 offset:8192\n" \
"v_mfma_f32_32x32x16_bf16 v[48:63], v[92:95], v[156:159], v[48:63]\n" \
"ds_read_b128 v[92:95], v2 offset:9216\n" \
"s_waitcnt lgkmcnt(3)\n" \
"v_fmac_f32 v32, v8, v20\n" \
"v_fmac_f32 v33, v8, v21\n" \
"v_mul_f32 v16, v32, v32\n" \
"v_mul_f32 v17, v33, v33\n" \
"v_mfma_f32_32x32x16_bf16 v[48:63], v[80:83], v[160:163], v[48:63]\n" \
"ds_read_b128 v[80:83], v2 offset:10240\n" \
"v_fma_f32 v16, v16, v10, v9\n" \
"v_fma_f32 v17, v17, v10, v9\n" \
"v_mul_f32 v16, v16, v32\n" \
"v_mul_f32 v17, v17, v33\n" \
"v_mfma_f32_32x32x16_bf16 v[48:63], v[84:87], v[164:167], v[48:63]\n" \
"ds_read_b128 v[84:87], v2 offset:11264\n" \
"v_exp_f32 v16, v16\n" \
"s_waitcnt lgkmcnt(4)\n" \
"v_fmac_f32 v40, v8, v24\n" \
"v_fmac_f32 v41, v8, v25\n" \
"s_waitcnt lgkmcnt(2)\n" \
"v_mfma_f32_32x32x16_bf16 v[48:63], v[88:91], v[168:171], v[48:63]\n" \
"ds_read_b128 v[88:91], v2 offset:12288\n" \
"v_exp_f32 v17, v17\n" \
"v_fma_f32 v16, v16, v11, v11\n" \
"v_mul_f32 v32, v32, v40\n" \
"v_mfma_f32_32x32x16_bf16 v[48:63], v[92:95], v[172:175], v[48:63]\n" \
"ds_read_b128 v[92:95], v2 offset:13312\n" \
"v_rcp_f32 v16, v16\n" \
"v_fma_f32 v17, v17, v11, v11\n" \
"v_mul_f32 v33, v33, v41\n" \
"s_waitcnt lgkmcnt(2)\n" \
"v_mfma_f32_32x32x16_bf16 v[48:63], v[80:83], v[176:179], v[48:63]\n" \
"ds_read_b128 v[80:83], v2 offset:14336\n" \
"v_rcp_f32 v17, v17\n" \
"v_mul_f32 v32, v32, v16\n" \
"v_mul_f32 v33, v33, v17\n" \
"v_mfma_f32_32x32x16_bf16 v[48:63], v[84:87], v[180:183], v[48:63]\n" \
"ds_read_b128 v[84:87], v2 offset:15360\n" \
"v_fmac_f32 v34, v8, v22\n" \
"v_fmac_f32 v35, v8, v23\n" \
"v_cvt_pk_bf16_f32 v28, v32, v33\n" \
"ds_read_b128 v[20:23], v7 offset:928\n" \
"v_mul_f32 v16, v34, v34\n" \
"v_mul_f32 v17, v35, v35\n" \
"s_waitcnt lgkmcnt(3)\n" \
"v_mfma_f32_32x32x16_bf16 v[48:63], v[88:91], v[184:187], v[48:63]\n" \
"ds_read_b128 v[88:91], v2 offset:16384\n" \
"v_fma_f32 v16, v16, v10, v9\n" \
"v_fma_f32 v17, v17, v10, v9\n" \
"v_mul_f32 v16, v16, v34\n" \
"v_mul_f32 v17, v17, v35\n" \
"v_mfma_f32_32x32x16_bf16 v[48:63], v[92:95], v[188:191], v[48:63]\n" \
"ds_read_b128 v[92:95], v2 offset:17408\n" \
"v_exp_f32 v16, v16\n" \
"v_fmac_f32 v42, v8, v26\n" \
"v_fmac_f32 v43, v8, v27\n" \
"s_waitcnt lgkmcnt(3)\n" \
"v_mfma_f32_32x32x16_bf16 v[48:63], v[80:83], v[192:195], v[48:63]\n" \
"ds_read_b128 v[80:83], v2 offset:18432\n" \
"ds_read_b128 v[24:27], v7 offset:992\n" \
"v_exp_f32 v17, v17\n" \
"v_fma_f32 v16, v16, v11, v11\n" \
"v_mul_f32 v34, v34, v42\n" \
"v_mfma_f32_32x32x16_bf16 v[48:63], v[84:87], v[196:199], v[48:63]\n" \
"ds_read_b128 v[84:87], v2 offset:19456\n" \
"v_rcp_f32 v16, v16\n" \
"v_fma_f32 v17, v17, v11, v11\n" \
"v_mul_f32 v35, v35, v43\n" \
"s_waitcnt lgkmcnt(3)\n" \
"v_mfma_f32_32x32x16_bf16 v[48:63], v[88:91], v[200:203], v[48:63]\n" \
"ds_read_b128 v[88:91], v2 offset:20480\n" \
"v_rcp_f32 v17, v17\n" \
"v_mul_f32 v34, v34, v16\n" \
"v_mul_f32 v35, v35, v17\n" \
"v_mfma_f32_32x32x16_bf16 v[48:63], v[92:95], v[204:207], v[48:63]\n" \
"ds_read_b128 v[92:95], v2 offset:21504\n" \
"v_fmac_f32 v36, v8, v20\n" \
"v_fmac_f32 v37, v8, v21\n" \
"v_cvt_pk_bf16_f32 v29, v34, v35\n" \
"v_mul_f32 v16, v36, v36\n" \
"v_mul_f32 v17, v37, v37\n" \
"s_waitcnt lgkmcnt(2)\n" \
"v_mfma_f32_32x32x16_bf16 v[48:63], v[80:83], v[208:211], v[48:63]\n" \
"ds_read_b128 v[80:83], v2 offset:22528\n" \
"v_fma_f32 v16, v16, v10, v9\n" \
"v_fma_f32 v17, v17, v10, v9\n" \
"v_mul_f32 v16, v16, v36\n" \
"v_mul_f32 v17, v17, v37\n" \
"v_mfma_f32_32x32x16_bf16 v[48:63], v[84:87], v[212:215], v[48:63]\n" \
"ds_read_b128 v[84:87], v2 offset:23552\n" \
"v_exp_f32 v16, v16\n" \
"v_fmac_f32 v44, v8, v24\n" \
"v_fmac_f32 v45, v8, v25\n" \
"s_waitcnt lgkmcnt(2)\n" \
"v_mfma_f32_32x32x16_bf16 v[48:63], v[88:91], v[216:219], v[48:63]\n" \
"ds_read_b128 v[88:91], v2 offset:24576\n" \
"v_exp_f32 v17, v17\n" \
"v_fma_f32 v16, v16, v11, v11\n" \
"v_mul_f32 v36, v36, v44\n" \
"v_mfma_f32_32x32x16_bf16 v[48:63], v[92:95], v[220:223], v[48:63]\n" \
"ds_read_b128 v[92:95], v2 offset:25600\n" \
"v_rcp_f32 v16, v16\n" \
"v_fma_f32 v17, v17, v11, v11\n" \
"v_mul_f32 v37, v37, v45\n" \
"s_waitcnt lgkmcnt(2)\n" \
"v_mfma_f32_32x32x16_bf16 v[48:63], v[80:83], v[224:227], v[48:63]\n" \
"ds_read_b128 v[80:83], v2 offset:26624\n" \
"v_rcp_f32 v17, v17\n" \
"v_mul_f32 v36, v36, v16\n" \
"v_mul_f32 v37, v37, v17\n" \
"v_mfma_f32_32x32x16_bf16 v[48:63], v[84:87], v[228:231], v[48:63]\n" \
"ds_read_b128 v[84:87], v2 offset:27648\n" \
"v_fmac_f32 v38, v8, v22\n" \
"v_fmac_f32 v39, v8, v23\n" \
"v_cvt_pk_bf16_f32 v30, v36, v37\n" \
"v_mul_f32 v16, v38, v38\n" \
"v_mul_f32 v17, v39, v39\n" \
"s_waitcnt lgkmcnt(2)\n" \
"v_mfma_f32_32x32x16_bf16 v[48:63], v[88:91], v[232:235], v[48:63]\n" \
"ds_read_b128 v[88:91], v2 offset:28672\n" \
"v_fma_f32 v16, v16, v10, v9\n" \
"v_fma_f32 v17, v17, v10, v9\n" \
"v_mul_f32 v16, v16, v38\n" \
"v_mul_f32 v17, v17, v39\n" \
"v_mfma_f32_32x32x16_bf16 v[48:63], v[92:95], v[236:239], v[48:63]\n" \
"ds_read_b128 v[92:95], v2 offset:29696\n" \
"v_exp_f32 v16, v16\n" \
"v_fmac_f32 v46, v8, v26\n" \
"v_fmac_f32 v47, v8, v27\n" \
"s_waitcnt lgkmcnt(2)\n" \
"v_mfma_f32_32x32x16_bf16 v[48:63], v[80:83], v[240:243], v[48:63]\n" \
"ds_read_b128 v[80:83], v0 offset:0\n" \
"v_exp_f32 v17, v17\n" \
"v_fma_f32 v16, v16, v11, v11\n" \
"v_mul_f32 v38, v38, v46\n" \
"v_mfma_f32_32x32x16_bf16 v[48:63], v[84:87], v[244:247], v[48:63]\n" \
"ds_read_b128 v[84:87], v0 offset:1024\n" \
"v_rcp_f32 v16, v16\n" \
"v_fma_f32 v17, v17, v11, v11\n" \
"v_mul_f32 v39, v39, v47\n" \
"s_waitcnt lgkmcnt(2)\n" \
"v_mfma_f32_32x32x16_bf16 v[48:63], v[88:91], v[248:251], v[48:63]\n" \
"ds_read_b128 v[88:91], v0 offset:2048\n" \
"v_rcp_f32 v17, v17\n" \
"v_mul_f32 v38, v38, v16\n" \
"v_mul_f32 v39, v39, v17\n" \
"v_mfma_f32_32x32x16_bf16 v[48:63], v[92:95], v[252:255], v[48:63]\n" \
"ds_read_b128 v[92:95], v0 offset:3072\n" \
"v_cvt_pk_bf16_f32 v31, v38, v39\n" \
"v_bfi_b32 v28, 1, s24, v28\n" \
"buffer_store_dwordx4 v[28:31], v6, %0, s52 offen sc1\n" \
"s_barrier\n" \
"s_waitcnt lgkmcnt(2)\n" \
"v_mfma_f32_32x32x16_bf16 v[64:79], v[80:83], v[96:99], 0\n" \
"ds_read_b128 v[80:83], v0 offset:4096\n" \
"s_add_u32 s52, s52, 4096\n" \
"ds_read_b128 v[20:23], v7 offset:1024\n" \
"ds_read_b128 v[24:27], v7 offset:1088\n" \
"v_mfma_f32_32x32x16_bf16 v[64:79], v[84:87], v[100:103], v[64:79]\n" \
"ds_read_b128 v[84:87], v0 offset:5120\n" \
"s_waitcnt lgkmcnt(4)\n" \
"v_mfma_f32_32x32x16_bf16 v[64:79], v[88:91], v[104:107], v[64:79]\n" \
"ds_read_b128 v[88:91], v0 offset:6144\n" \
"s_waitcnt lgkmcnt(3)\n" \
"v_fmac_f32 v48, v8, v20\n" \
"v_fmac_f32 v49, v8, v21\n" \
"v_mul_f32 v16, v48, v48\n" \
"v_mul_f32 v17, v49, v49\n" \
"v_mfma_f32_32x32x16_bf16 v[64:79], v[92:95], v[108:111], v[64:79]\n" \
"ds_read_b128 v[92:95], v0 offset:7168\n" \
"v_fma_f32 v16, v16, v10, v9\n" \
"v_fma_f32 v17, v17, v10, v9\n" \
"v_mul_f32 v16, v16, v48\n" \
"v_mul_f32 v17, v17, v49\n" \
"s_waitcnt lgkmcnt(2)\n" \
"v_mfma_f32_32x32x16_bf16 v[64:79], v[80:83], v[112:115], v[64:79]\n" \
"ds_read_b128 v[80:83], v0 offset:8192\n" \
"v_exp_f32 v16, v16\n" \
"v_fmac_f32 v56, v8, v24\n" \
"v_fmac_f32 v57, v8, v25\n" \
"v_mfma_f32_32x32x16_bf16 v[64:79], v[84:87], v[116:119], v[64:79]\n" \
"ds_read_b128 v[84:87], v0 offset:9216\n" \
"v_exp_f32 v17, v17\n" \
"v_fma_f32 v16, v16, v11, v11\n" \
"v_mul_f32 v48, v48, v56\n" \
"s_waitcnt lgkmcnt(2)\n" \
"v_mfma_f32_32x32x16_bf16 v[64:79], v[88:91], v[120:123], v[64:79]\n" \
"ds_read_b128 v[88:91], v0 offset:10240\n" \
"v_rcp_f32 v16, v16\n" \
"v_fma_f32 v17, v17, v11, v11\n" \
"v_mul_f32 v49, v49, v57\n" \
"v_mfma_f32_32x32x16_bf16 v[64:79], v[92:95], v[124:127], v[64:79]\n" \
"ds_read_b128 v[92:95], v0 offset:11264\n" \
"v_rcp_f32 v17, v17\n" \
"v_mul_f32 v48, v48, v16\n" \
"v_mul_f32 v49, v49, v17\n" \
"s_waitcnt lgkmcnt(2)\n" \
"v_mfma_f32_32x32x16_bf16 v[64:79], v[80:83], v[128:131], v[64:79]\n" \
"ds_read_b128 v[80:83], v0 offset:12288\n" \
"v_fmac_f32 v50, v8, v22\n" \
"v_fmac_f32 v51, v8, v23\n" \
"v_cvt_pk_bf16_f32 v28, v48, v49\n" \
"ds_read_b128 v[20:23], v7 offset:1056\n" \
"v_mul_f32 v16, v50, v50\n" \
"v_mul_f32 v17, v51, v51\n" \
"v_mfma_f32_32x32x16_bf16 v[64:79], v[84:87], v[132:135], v[64:79]\n" \
"ds_read_b128 v[84:87], v0 offset:13312\n" \
"v_fma_f32 v16, v16, v10, v9\n" \
"v_fma_f32 v17, v17, v10, v9\n" \
"v_mul_f32 v16, v16, v50\n" \
"v_mul_f32 v17, v17, v51\n" \
"s_waitcnt lgkmcnt(3)\n" \
"v_mfma_f32_32x32x16_bf16 v[64:79], v[88:91], v[136:139], v[64:79]\n" \
"ds_read_b128 v[88:91], v0 offset:14336\n" \
"v_exp_f32 v16, v16\n" \
"v_fmac_f32 v58, v8, v26\n" \
"v_fmac_f32 v59, v8, v27\n" \
"v_mfma_f32_32x32x16_bf16 v[64:79], v[92:95], v[140:143], v[64:79]\n" \
"ds_read_b128 v[92:95], v0 offset:15360\n" \
"ds_read_b128 v[24:27], v7 offset:1120\n" \
"v_exp_f32 v17, v17\n" \
"v_fma_f32 v16, v16, v11, v11\n" \
"v_mul_f32 v50, v50, v58\n" \
"s_waitcnt lgkmcnt(3)\n" \
"v_mfma_f32_32x32x16_bf16 v[64:79], v[80:83], v[144:147], v[64:79]\n" \
"ds_read_b128 v[80:83], v0 offset:16384\n" \
"v_rcp_f32 v16, v16\n" \
"v_fma_f32 v17, v17, v11, v11\n" \
"v_mul_f32 v51, v51, v59\n" \
"v_mfma_f32_32x32x16_bf16 v[64:79], v[84:87], v[148:151], v[64:79]\n" \
"ds_read_b128 v[84:87], v0 offset:17408\n" \
"v_rcp_f32 v17, v17\n" \
"v_mul_f32 v50, v50, v16\n" \
"v_mul_f32 v51, v51, v17\n" \
"s_waitcnt lgkmcnt(3)\n" \
"v_mfma_f32_32x32x16_bf16 v[64:79], v[88:91], v[152:155], v[64:79]\n" \
"ds_read_b128 v[88:91], v0 offset:18432\n" \
"v_fmac_f32 v52, v8, v20\n" \
"v_fmac_f32 v53, v8, v21\n" \
"v_cvt_pk_bf16_f32 v29, v50, v51\n" \
"v_mul_f32 v16, v52, v52\n" \
"v_mul_f32 v17, v53, v53\n" \
"v_mfma_f32_32x32x16_bf16 v[64:79], v[92:95], v[156:159], v[64:79]\n" \
"ds_read_b128 v[92:95], v0 offset:19456\n" \
"v_fma_f32 v16, v16, v10, v9\n" \
"v_fma_f32 v17, v17, v10, v9\n" \
"v_mul_f32 v16, v16, v52\n" \
"v_mul_f32 v17, v17, v53\n" \
"s_waitcnt lgkmcnt(2)\n" \
"v_mfma_f32_32x32x16_bf16 v[64:79], v[80:83], v[160:163], v[64:79]\n" \
"ds_read_b128 v[80:83], v0 offset:20480\n" \
"v_exp_f32 v16, v16\n" \
"v_fmac_f32 v60, v8, v24\n" \
"v_fmac_f32 v61, v8, v25\n" \
"v_mfma_f32_32x32x16_bf16 v[64:79], v[84:87], v[164:167], v[64:79]\n" \
"ds_read_b128 v[84:87], v0 offset:21504\n" \
"v_exp_f32 v17, v17\n" \
"v_fma_f32 v16, v16, v11, v11\n" \
"v_mul_f32 v52, v52, v60\n" \
"s_waitcnt lgkmcnt(2)\n" \
"v_mfma_f32_32x32x16_bf16 v[64:79], v[88:91], v[168:171], v[64:79]\n" \
"ds_read_b128 v[88:91], v0 offset:22528\n" \
"v_rcp_f32 v16, v16\n" \
"v_fma_f32 v17, v17, v11, v11\n" \
"v_mul_f32 v53, v53, v61\n" \
"v_mfma_f32_32x32x16_bf16 v[64:79], v[92:95], v[172:175], v[64:79]\n" \
"ds_read_b128 v[92:95], v0 offset:23552\n" \
"v_rcp_f32 v17, v17\n" \
"v_mul_f32 v52, v52, v16\n" \
"v_mul_f32 v53, v53, v17\n" \
"s_waitcnt lgkmcnt(2)\n" \
"v_mfma_f32_32x32x16_bf16 v[64:79], v[80:83], v[176:179], v[64:79]\n" \
"ds_read_b128 v[80:83], v0 offset:24576\n" \
"v_fmac_f32 v54, v8, v22\n" \
"v_fmac_f32 v55, v8, v23\n" \
"v_cvt_pk_bf16_f32 v30, v52, v53\n" \
"v_mul_f32 v16, v54, v54\n" \
"v_mul_f32 v17, v55, v55\n" \
"v_mfma_f32_32x32x16_bf16 v[64:79], v[84:87], v[180:183], v[64:79]\n" \
"ds_read_b128 v[84:87], v0 offset:25600\n" \
"v_fma_f32 v16, v16, v10, v9\n" \
"v_fma_f32 v17, v17, v10, v9\n" \
"v_mul_f32 v16, v16, v54\n" \
"v_mul_f32 v17, v17, v55\n" \
"s_waitcnt lgkmcnt(2)\n" \
"v_mfma_f32_32x32x16_bf16 v[64:79], v[88:91], v[184:187], v[64:79]\n" \
"ds_read_b128 v[88:91], v0 offset:26624\n" \
"v_exp_f32 v16, v16\n" \
"v_fmac_f32 v62, v8, v26\n" \
"v_fmac_f32 v63, v8, v27\n" \
"v_mfma_f32_32x32x16_bf16 v[64:79], v[92:95], v[188:191], v[64:79]\n" \
"ds_read_b128 v[92:95], v0 offset:27648\n" \
"v_exp_f32 v17, v17\n" \
"v_fma_f32 v16, v16, v11, v11\n" \
"v_mul_f32 v54, v54, v62\n" \
"s_waitcnt lgkmcnt(2)\n" \
"v_mfma_f32_32x32x16_bf16 v[64:79], v[80:83], v[192:195], v[64:79]\n" \
"ds_read_b128 v[80:83], v0 offset:28672\n" \
"v_rcp_f32 v16, v16\n" \
"v_fma_f32 v17, v17, v11, v11\n" \
"v_mul_f32 v55, v55, v63\n" \
"v_mfma_f32_32x32x16_bf16 v[64:79], v[84:87], v[196:199], v[64:79]\n" \
"ds_read_b128 v[84:87], v0 offset:29696\n" \
"v_rcp_f32 v17, v17\n" \
"v_mul_f32 v54, v54, v16\n" \
"v_mul_f32 v55, v55, v17\n" \
"s_waitcnt lgkmcnt(2)\n" \
"v_mfma_f32_32x32x16_bf16 v[64:79], v[88:91], v[200:203], v[64:79]\n" \
"ds_read_b128 v[88:91], v0 offset:30720\n" \
"v_cvt_pk_bf16_f32 v31, v54, v55\n" \
"v_bfi_b32 v28, 1, s24, v28\n" \
"buffer_store_dwordx4 v[28:31], v6, %0, s52 offen sc1\n" \
"v_mfma_f32_32x32x16_bf16 v[64:79], v[92:95], v[204:207], v[64:79]\n" \
"ds_read_b128 v[92:95], v0 offset:31744\n" \
"s_add_u32 s52, s52, 4096\n" \
"s_waitcnt lgkmcnt(2)\n" \
"v_mfma_f32_32x32x16_bf16 v[64:79], v[80:83], v[208:211], v[64:79]\n" \
"ds_read_b128 v[80:83], v0 offset:32768\n" \
"v_mfma_f32_32x32x16_bf16 v[64:79], v[84:87], v[212:215], v[64:79]\n" \
"ds_read_b128 v[84:87], v0 offset:33792\n" \
"s_waitcnt lgkmcnt(2)\n" \
"v_mfma_f32_32x32x16_bf16 v[64:79], v[88:91], v[216:219], v[64:79]\n" \
"ds_read_b128 v[88:91], v0 offset:34816\n" \
"v_mfma_f32_32x32x16_bf16 v[64:79], v[92:95], v[220:223], v[64:79]\n" \
"ds_read_b128 v[92:95], v0 offset:35840\n" \
"s_waitcnt lgkmcnt(2)\n" \
"v_mfma_f32_32x32x16_bf16 v[64:79], v[80:83], v[224:227], v[64:79]\n" \
"ds_read_b128 v[80:83], v0 offset:36864\n" \
"v_mfma_f32_32x32x16_bf16 v[64:79], v[84:87], v[228:231], v[64:79]\n" \
"ds_read_b128 v[84:87], v0 offset:37888\n" \
"s_waitcnt lgkmcnt(2)\n" \
"v_mfma_f32_32x32x16_bf16 v[64:79], v[88:91], v[232:235], v[64:79]\n" \
"ds_read_b128 v[88:91], v0 offset:38912\n" \
"v_mfma_f32_32x32x16_bf16 v[64:79], v[92:95], v[236:239], v[64:79]\n" \
"ds_read_b128 v[92:95], v0 offset:39936\n" \
"s_waitcnt lgkmcnt(2)\n" \
"v_mfma_f32_32x32x16_bf16 v[64:79], v[80:83], v[240:243], v[64:79]\n" \
"ds_read_b128 v[80:83], v1 offset:20480\n" \
"v_mfma_f32_32x32x16_bf16 v[64:79], v[84:87], v[244:247], v[64:79]\n" \
"ds_read_b128 v[84:87], v1 offset:21504\n" \
"s_waitcnt lgkmcnt(2)\n" \
"v_mfma_f32_32x32x16_bf16 v[64:79], v[88:91], v[248:251], v[64:79]\n" \
"ds_read_b128 v[88:91], v1 offset:22528\n" \
"v_mfma_f32_32x32x16_bf16 v[64:79], v[92:95], v[252:255], v[64:79]\n" \
"ds_read_b128 v[92:95], v1 offset:23552\n" \
"ds_read_b128 v[20:23], v7 offset:1152\n" \
"ds_read_b128 v[24:27], v7 offset:1216\n" \
"s_barrier\n" \
"s_waitcnt lgkmcnt(4)\n" \
"v_mfma_f32_32x32x16_bf16 v[32:47], v[80:83], v[96:99], 0\n" \
"ds_read_b128 v[80:83], v1 offset:24576\n" \
"v_mfma_f32_32x32x16_bf16 v[32:47], v[84:87], v[100:103], v[32:47]\n" \
"ds_read_b128 v[84:87], v1 offset:25600\n" \
"s_waitcnt lgkmcnt(3)\n" \
"s_nop 1\n" \
"v_fmac_f32 v64, v8, v20\n" \
"v_fmac_f32 v65, v8, v21\n" \
"v_mul_f32 v16, v64, v64\n" \
"v_mul_f32 v17, v65, v65\n" \
"v_mfma_f32_32x32x16_bf16 v[32:47], v[88:91], v[104:107], v[32:47]\n" \
"ds_read_b128 v[88:91], v1 offset:26624\n" \
"v_fma_f32 v16, v16, v10, v9\n" \
"v_fma_f32 v17, v17, v10, v9\n" \
"v_mul_f32 v16, v16, v64\n" \
"v_mul_f32 v17, v17, v65\n" \
"v_mfma_f32_32x32x16_bf16 v[32:47], v[92:95], v[108:111], v[32:47]\n" \
"ds_read_b128 v[92:95], v1 offset:27648\n" \
"buffer_load_dwordx4 v[96:99], v0, %0, s53 offen offset:0 sc1\n" \
"v_exp_f32 v16, v16\n" \
"s_waitcnt lgkmcnt(2)\n" \
"v_mfma_f32_32x32x16_bf16 v[32:47], v[80:83], v[112:115], v[32:47]\n" \
"ds_read_b128 v[80:83], v1 offset:28672\n" \
"v_fmac_f32 v72, v8, v24\n" \
"v_fmac_f32 v73, v8, v25\n" \
"v_exp_f32 v17, v17\n" \
"v_mfma_f32_32x32x16_bf16 v[32:47], v[84:87], v[116:119], v[32:47]\n" \
"ds_read_b128 v[84:87], v1 offset:29696\n" \
"buffer_load_dwordx4 v[100:103], v0, %0, s54 offen offset:0 sc1\n" \
"v_fma_f32 v16, v16, v11, v11\n" \
"v_mul_f32 v64, v64, v72\n" \
"s_waitcnt lgkmcnt(2)\n" \
"v_mfma_f32_32x32x16_bf16 v[32:47], v[88:91], v[120:123], v[32:47]\n" \
"ds_read_b128 v[88:91], v0 offset:40960\n" \
"v_rcp_f32 v16, v16\n" \
"v_fma_f32 v17, v17, v11, v11\n" \
"v_mul_f32 v65, v65, v73\n" \
"v_mfma_f32_32x32x16_bf16 v[32:47], v[92:95], v[124:127], v[32:47]\n" \
"ds_read_b128 v[92:95], v0 offset:41984\n" \
"buffer_load_dwordx4 v[104:107], v0, %0, s53 offen offset:1024 sc1\n" \
"v_rcp_f32 v17, v17\n" \
"s_waitcnt lgkmcnt(2)\n" \
"v_mfma_f32_32x32x16_bf16 v[32:47], v[80:83], v[128:131], v[32:47]\n" \
"ds_read_b128 v[80:83], v0 offset:43008\n" \
"v_mul_f32 v64, v64, v16\n" \
"v_mul_f32 v65, v65, v17\n" \
"v_fmac_f32 v66, v8, v22\n" \
"v_fmac_f32 v67, v8, v23\n" \
"v_mfma_f32_32x32x16_bf16 v[32:47], v[84:87], v[132:135], v[32:47]\n" \
"ds_read_b128 v[84:87], v0 offset:44032\n" \
"buffer_load_dwordx4 v[108:111], v0, %0, s54 offen offset:1024 sc1\n" \
"v_cvt_pk_bf16_f32 v28, v64, v65\n" \
"ds_read_b128 v[20:23], v7 offset:1184\n" \
"v_mul_f32 v16, v66, v66\n" \
"v_mul_f32 v17, v67, v67\n" \
"s_waitcnt lgkmcnt(3)\n" \
"v_mfma_f32_32x32x16_bf16 v[32:47], v[88:91], v[136:139], v[32:47]\n" \
"ds_read_b128 v[88:91], v0 offset:45056\n" \
"v_fma_f32 v16, v16, v10, v9\n" \
"v_fma_f32 v17, v17, v10, v9\n" \
"v_mul_f32 v16, v16, v66\n" \
"v_mul_f32 v17, v17, v67\n" \
"v_mfma_f32_32x32x16_bf16 v[32:47], v[92:95], v[140:143], v[32:47]\n" \
"ds_read_b128 v[92:95], v0 offset:46080\n" \
"buffer_load_dwordx4 v[112:115], v0, %0, s53 offen offset:2048 sc1\n" \
"v_exp_f32 v16, v16\n" \
"s_waitcnt lgkmcnt(3)\n" \
"v_mfma_f32_32x32x16_bf16 v[32:47], v[80:83], v[144:147], v[32:47]\n" \
"ds_read_b128 v[80:83], v0 offset:47104\n" \
"v_fmac_f32 v74, v8, v26\n" \
"v_fmac_f32 v75, v8, v27\n" \
"ds_read_b128 v[24:27], v7 offset:1248\n" \
"v_exp_f32 v17, v17\n" \
"v_mfma_f32_32x32x16_bf16 v[32:47], v[84:87], v[148:151], v[32:47]\n" \
"ds_read_b128 v[84:87], v0 offset:48128\n" \
"buffer_load_dwordx4 v[116:119], v0, %0, s54 offen offset:2048 sc1\n" \
"v_fma_f32 v16, v16, v11, v11\n" \
"v_mul_f32 v66, v66, v74\n" \
"s_waitcnt lgkmcnt(3)\n" \
"v_mfma_f32_32x32x16_bf16 v[32:47], v[88:91], v[152:155], v[32:47]\n" \
"ds_read_b128 v[88:91], v0 offset:49152\n" \
"v_rcp_f32 v16, v16\n" \
"v_fma_f32 v17, v17, v11, v11\n" \
"v_mul_f32 v67, v67, v75\n" \
"v_mfma_f32_32x32x16_bf16 v[32:47], v[92:95], v[156:159], v[32:47]\n" \
"ds_read_b128 v[92:95], v0 offset:50176\n" \
"buffer_load_dwordx4 v[120:123], v0, %0, s53 offen offset:3072 sc1\n" \
"v_rcp_f32 v17, v17\n" \
"s_waitcnt lgkmcnt(2)\n" \
"v_mfma_f32_32x32x16_bf16 v[32:47], v[80:83], v[160:163], v[32:47]\n" \
"ds_read_b128 v[80:83], v0 offset:51200\n" \
"v_mul_f32 v66, v66, v16\n" \
"v_mul_f32 v67, v67, v17\n" \
"v_fmac_f32 v68, v8, v20\n" \
"v_fmac_f32 v69, v8, v21\n" \
"v_mfma_f32_32x32x16_bf16 v[32:47], v[84:87], v[164:167], v[32:47]\n" \
"ds_read_b128 v[84:87], v0 offset:52224\n" \
"buffer_load_dwordx4 v[124:127], v0, %0, s54 offen offset:3072 sc1\n" \
"s_add_u32 s53, s53, 4096\n" \
"s_add_u32 s54, s54, 4096\n" \
"s_waitcnt lgkmcnt(2)\n" \
"v_mfma_f32_32x32x16_bf16 v[32:47], v[88:91], v[168:171], v[32:47]\n" \
"ds_read_b128 v[88:91], v0 offset:53248\n" \
"v_cvt_pk_bf16_f32 v29, v66, v67\n" \
"v_mul_f32 v16, v68, v68\n" \
"v_mul_f32 v17, v69, v69\n" \
"v_fma_f32 v16, v16, v10, v9\n" \
"v_fma_f32 v17, v17, v10, v9\n" \
"v_mfma_f32_32x32x16_bf16 v[32:47], v[92:95], v[172:175], v[32:47]\n" \
"ds_read_b128 v[92:95], v0 offset:54272\n" \
"v_mul_f32 v16, v16, v68\n" \
"v_mul_f32 v17, v17, v69\n" \
"v_exp_f32 v16, v16\n" \
"s_waitcnt lgkmcnt(2)\n" \
"v_mfma_f32_32x32x16_bf16 v[32:47], v[80:83], v[176:179], v[32:47]\n" \
"ds_read_b128 v[80:83], v0 offset:55296\n" \
"v_fmac_f32 v76, v8, v24\n" \
"v_fmac_f32 v77, v8, v25\n" \
"v_exp_f32 v17, v17\n" \
"v_mfma_f32_32x32x16_bf16 v[32:47], v[84:87], v[180:183], v[32:47]\n" \
"ds_read_b128 v[84:87], v0 offset:56320\n" \
"v_fma_f32 v16, v16, v11, v11\n" \
"v_mul_f32 v68, v68, v76\n" \
"v_rcp_f32 v16, v16\n" \
"s_waitcnt lgkmcnt(2)\n" \
"v_mfma_f32_32x32x16_bf16 v[32:47], v[88:91], v[184:187], v[32:47]\n" \
"ds_read_b128 v[88:91], v0 offset:57344\n" \
"buffer_load_dwordx4 v[128:131], v0, %0, s53 offen offset:0 sc1\n" \
"v_fma_f32 v17, v17, v11, v11\n" \
"v_mul_f32 v69, v69, v77\n" \
"v_mfma_f32_32x32x16_bf16 v[32:47], v[92:95], v[188:191], v[32:47]\n" \
"ds_read_b128 v[92:95], v0 offset:58368\n" \
"v_rcp_f32 v17, v17\n" \
"v_mul_f32 v68, v68, v16\n" \
"v_mul_f32 v69, v69, v17\n" \
"s_waitcnt lgkmcnt(2)\n" \
"v_mfma_f32_32x32x16_bf16 v[32:47], v[80:83], v[192:195], v[32:47]\n" \
"ds_read_b128 v[80:83], v0 offset:59392\n" \
"buffer_load_dwordx4 v[132:135], v0, %0, s54 offen offset:0 sc1\n" \
"v_fmac_f32 v70, v8, v22\n" \
"v_fmac_f32 v71, v8, v23\n" \
"v_mfma_f32_32x32x16_bf16 v[32:47], v[84:87], v[196:199], v[32:47]\n" \
"ds_read_b128 v[84:87], v0 offset:60416\n" \
"v_cvt_pk_bf16_f32 v30, v68, v69\n" \
"v_mul_f32 v16, v70, v70\n" \
"v_mul_f32 v17, v71, v71\n" \
"v_fma_f32 v16, v16, v10, v9\n" \
"v_fma_f32 v17, v17, v10, v9\n" \
"s_waitcnt lgkmcnt(2)\n" \
"v_mfma_f32_32x32x16_bf16 v[32:47], v[88:91], v[200:203], v[32:47]\n" \
"ds_read_b128 v[88:91], v1 offset:0\n" \
"buffer_load_dwordx4 v[136:139], v0, %0, s53 offen offset:1024 sc1\n" \
"v_mul_f32 v16, v16, v70\n" \
"v_mul_f32 v17, v17, v71\n" \
"v_mfma_f32_32x32x16_bf16 v[32:47], v[92:95], v[204:207], v[32:47]\n" \
"ds_read_b128 v[92:95], v1 offset:1024\n" \
"v_exp_f32 v16, v16\n" \
"v_fmac_f32 v78, v8, v26\n" \
"v_fmac_f32 v79, v8, v27\n" \
"s_waitcnt lgkmcnt(2)\n" \
"v_mfma_f32_32x32x16_bf16 v[32:47], v[80:83], v[208:211], v[32:47]\n" \
"ds_read_b128 v[80:83], v1 offset:2048\n" \
"buffer_load_dwordx4 v[140:143], v0, %0, s54 offen offset:1024 sc1\n" \
"v_exp_f32 v17, v17\n" \
"v_mfma_f32_32x32x16_bf16 v[32:47], v[84:87], v[212:215], v[32:47]\n" \
"ds_read_b128 v[84:87], v1 offset:3072\n" \
"v_fma_f32 v16, v16, v11, v11\n" \
"v_mul_f32 v70, v70, v78\n" \
"v_rcp_f32 v16, v16\n" \
"s_waitcnt lgkmcnt(2)\n" \
"v_mfma_f32_32x32x16_bf16 v[32:47], v[88:91], v[216:219], v[32:47]\n" \
"ds_read_b128 v[88:91], v1 offset:4096\n" \
"buffer_load_dwordx4 v[144:147], v0, %0, s53 offen offset:2048 sc1\n" \
"v_fma_f32 v17, v17, v11, v11\n" \
"v_mul_f32 v71, v71, v79\n" \
"v_mfma_f32_32x32x16_bf16 v[32:47], v[92:95], v[220:223], v[32:47]\n" \
"ds_read_b128 v[92:95], v1 offset:5120\n" \
"v_add_u32 v216, s63, v0\n" \
"v_rcp_f32 v17, v17\n" \
"v_mul_f32 v70, v70, v16\n" \
"s_waitcnt lgkmcnt(2)\n" \
"v_mfma_f32_32x32x16_bf16 v[32:47], v[80:83], v[224:227], v[32:47]\n" \
"ds_read_b128 v[80:83], v1 offset:6144\n" \
"v_add_u32 v217, s63, v1\n" \
"buffer_load_dwordx4 v[148:151], v0, %0, s54 offen offset:2048 sc1\n" \
"v_mul_f32 v71, v71, v17\n" \
"v_mfma_f32_32x32x16_bf16 v[32:47], v[84:87], v[228:231], v[32:47]\n" \
"ds_read_b128 v[84:87], v1 offset:7168\n" \
"v_add_u32 v218, s63, v2\n" \
"v_cvt_pk_bf16_f32 v31, v70, v71\n" \
"v_bfi_b32 v28, 1, s24, v28\n" \
"s_waitcnt lgkmcnt(2)\n" \
"v_mfma_f32_32x32x16_bf16 v[32:47], v[88:91], v[232:235], v[32:47]\n" \
"ds_read_b128 v[88:91], v1 offset:8192\n" \
"buffer_load_dwordx4 v[152:155], v0, %0, s53 offen offset:3072 sc1\n" \
"buffer_store_dwordx4 v[28:31], v6, %0, s52 offen sc1\n" \
"v_mfma_f32_32x32x16_bf16 v[32:47], v[92:95], v[236:239], v[32:47]\n" \
"ds_read_b128 v[92:95], v1 offset:9216\n" \
"s_add_u32 s52, s52, 4096\n" \
"s_waitcnt lgkmcnt(2)\n" \
"v_mfma_f32_32x32x16_bf16 v[32:47], v[80:83], v[240:243], v[32:47]\n" \
"ds_read_b128 v[80:83], v217 offset:10240\n" \
"buffer_load_dwordx4 v[156:159], v0, %0, s54 offen offset:3072 sc1\n" \
"s_add_u32 s53, s53, 4096\n" \
"s_add_u32 s54, s54, 4096\n" \
"v_mfma_f32_32x32x16_bf16 v[32:47], v[84:87], v[244:247], v[32:47]\n" \
"ds_read_b128 v[84:87], v217 offset:11264\n" \
"s_waitcnt lgkmcnt(2)\n" \
"v_mfma_f32_32x32x16_bf16 v[32:47], v[88:91], v[248:251], v[32:47]\n" \
"ds_read_b128 v[88:91], v217 offset:12288\n" \
"v_mfma_f32_32x32x16_bf16 v[32:47], v[92:95], v[252:255], v[32:47]\n" \
"ds_read_b128 v[92:95], v217 offset:13312\n" \
"ds_read_b128 v[20:23], v7 offset:1280\n" \
"ds_read_b128 v[24:27], v7 offset:1344\n" \
"s_barrier\n" \
"s_xor_b32 s25, s63, 5120\n" \
"v_add_u32 v219, s25, v1\n" \
"v_add_u32 v220, s25, v2\n" \
"v_lshrrev_b32 v12, 1, v0\n" \
"v_add_u32 v221, s61, v12\n" \
"v_add_u32 v222, s62, v12\n" \
"s_mul_i32 s25, %4, 10240\n" \
"v_add_u32 v223, s25, v0\n" \
"v_bfe_u32 v224, v0, 4, 4\n" \
"v_mul_u32_u24 v224, 1280, v224\n" \
"v_lshrrev_b32 v225, 8, v0\n" \
"v_lshl_add_u32 v12, v225, 4, v224\n" \
"v_lshl_add_u32 v225, v225, 3, v224\n" \
"v_mov_b32 v224, v12\n" \
"s_waitcnt lgkmcnt(1)\n" \
"v_fmac_f32 v32, v8, v20\n" \
"v_fmac_f32 v33, v8, v21\n" \
"v_mul_f32 v16, v32, v32\n" \
"v_mul_f32 v17, v33, v33\n" \
"v_fma_f32 v16, v16, v10, v9\n" \
"v_fma_f32 v17, v17, v10, v9\n" \
"v_mul_f32 v16, v16, v32\n" \
"v_mul_f32 v17, v17, v33\n" \
"v_exp_f32 v16, v16\n" \
"s_waitcnt lgkmcnt(0)\n" \
"v_fmac_f32 v40, v8, v24\n" \
"v_fmac_f32 v41, v8, v25\n" \
"v_exp_f32 v17, v17\n" \
"v_fma_f32 v16, v16, v11, v11\n" \
"v_mul_f32 v32, v32, v40\n" \
"v_rcp_f32 v16, v16\n" \
"v_fma_f32 v17, v17, v11, v11\n" \
"v_mul_f32 v33, v33, v41\n" \
"v_rcp_f32 v17, v17\n" \
"v_mul_f32 v32, v32, v16\n" \
"v_mul_f32 v33, v33, v17\n" \
"v_fmac_f32 v34, v8, v22\n" \
"v_fmac_f32 v35, v8, v23\n" \
"v_cvt_pk_bf16_f32 v28, v32, v33\n" \
"ds_read_b128 v[20:23], v7 offset:1312\n" \
"v_mul_f32 v16, v34, v34\n" \
"v_mul_f32 v17, v35, v35\n" \
"v_fma_f32 v16, v16, v10, v9\n" \
"v_fma_f32 v17, v17, v10, v9\n" \
"v_mul_f32 v16, v16, v34\n" \
"v_mul_f32 v17, v17, v35\n" \
"v_exp_f32 v16, v16\n" \
"v_fmac_f32 v42, v8, v26\n" \
"v_fmac_f32 v43, v8, v27\n" \
"ds_read_b128 v[24:27], v7 offset:1376\n" \
"v_exp_f32 v17, v17\n" \
"v_fma_f32 v16, v16, v11, v11\n" \
"v_mul_f32 v34, v34, v42\n" \
"v_rcp_f32 v16, v16\n" \
"v_fma_f32 v17, v17, v11, v11\n" \
"v_mul_f32 v35, v35, v43\n" \
"v_rcp_f32 v17, v17\n" \
"v_mul_f32 v34, v34, v16\n" \
"v_mul_f32 v35, v35, v17\n" \
"s_waitcnt lgkmcnt(1)\n" \
"v_fmac_f32 v36, v8, v20\n" \
"v_fmac_f32 v37, v8, v21\n" \
"v_cvt_pk_bf16_f32 v29, v34, v35\n" \
"v_mul_f32 v16, v36, v36\n" \
"v_mul_f32 v17, v37, v37\n" \
"v_fma_f32 v16, v16, v10, v9\n" \
"v_fma_f32 v17, v17, v10, v9\n" \
"v_mul_f32 v16, v16, v36\n" \
"v_mul_f32 v17, v17, v37\n" \
"s_waitcnt vmcnt(15)\n" \
"v_bitop3_b32 v12, v96, v100, s24 bitop3:0x7e\n" \
"v_exp_f32 v16, v16\n" \
"s_waitcnt lgkmcnt(0)\n" \
"v_fmac_f32 v44, v8, v24\n" \
"v_fmac_f32 v45, v8, v25\n" \
"v_exp_f32 v17, v17\n" \
"v_fma_f32 v16, v16, v11, v11\n" \
"v_mul_f32 v36, v36, v44\n" \
"s_waitcnt vmcnt(14)\n" \
"v_bitop3_b32 v12, v12, v104, s24 bitop3:0xf6\n" \
"s_waitcnt vmcnt(13)\n" \
"v_bitop3_b32 v12, v12, v108, s24 bitop3:0xf6\n" \
"v_rcp_f32 v16, v16\n" \
"v_fma_f32 v17, v17, v11, v11\n" \
"v_mul_f32 v37, v37, v45\n" \
"v_rcp_f32 v17, v17\n" \
"v_mul_f32 v36, v36, v16\n" \
"s_waitcnt vmcnt(12)\n" \
"v_bitop3_b32 v12, v12, v112, s24 bitop3:0xf6\n" \
"s_waitcnt vmcnt(11)\n" \
"v_bitop3_b32 v12, v12, v116, s24 bitop3:0xf6\n" \
"v_mul_f32 v37, v37, v17\n" \
"v_fmac_f32 v38, v8, v22\n" \
"v_fmac_f32 v39, v8, v23\n" \
"v_cvt_pk_bf16_f32 v30, v36, v37\n" \
"v_mul_f32 v16, v38, v38\n" \
"v_mul_f32 v17, v39, v39\n" \
"v_fma_f32 v16, v16, v10, v9\n" \
"s_waitcnt vmcnt(10)\n" \
"v_bitop3_b32 v12, v12, v120, s24 bitop3:0xf6\n" \
"s_waitcnt vmcnt(9)\n" \
"v_bitop3_b32 v12, v12, v124, s24 bitop3:0xf6\n" \
"v_fma_f32 v17, v17, v10, v9\n" \
"v_mul_f32 v16, v16, v38\n" \
"v_mul_f32 v17, v17, v39\n" \
"v_exp_f32 v16, v16\n" \
"v_fmac_f32 v46, v8, v26\n" \
"v_fmac_f32 v47, v8, v27\n" \
"v_and_b32 v12, 1, v12\n" \
"v_cmp_ne_u32 vcc, 0, v12\n" \
"v_exp_f32 v17, v17\n" \
"v_fma_f32 v16, v16, v11, v11\n" \
"v_mul_f32 v38, v38, v46\n" \
"v_rcp_f32 v16, v16\n" \
"v_fma_f32 v17, v17, v11, v11\n" \
"s_cbranch_vccnz .Lc107_%=\n" \
".Lc108_%=:\n" \
"v_mul_f32 v39, v39, v47\n" \
"v_rcp_f32 v17, v17\n" \
"v_mul_f32 v38, v38, v16\n" \
"v_mul_f32 v39, v39, v17\n" \
"v_cvt_pk_bf16_f32 v31, v38, v39\n" \
"v_bfi_b32 v28, 1, s24, v28\n" \
"v_or_b32 v6, s64, v6\n" \
"v_mfma_f32_16x16x32_bf16 v[68:71], v[80:83], v[100:103], 0\n" \
"buffer_store_dwordx4 v[28:31], v6, %0, s52 offen sc1\n" \
"v_mfma_f32_16x16x32_bf16 v[48:51], v[80:83], v[96:99], 0\n" \
"ds_read_b128 v[80:83], v217 offset:14336\n" \
"s_add_u32 s52, s52, -24576\n" \
"v_mfma_f32_16x16x32_bf16 v[72:75], v[84:87], v[100:103], 0\n" \
"v_and_b32 v13, 0xf0, v0\n" \
"v_lshrrev_b32 v14, 9, v0\n" \
"v_mfma_f32_16x16x32_bf16 v[52:55], v[84:87], v[96:99], 0\n" \
"ds_read_b128 v[84:87], v217 offset:40960\n" \
"v_lshl_add_u32 v13, v14, 8, v13\n" \
"v_mfma_f32_16x16x32_bf16 v[76:79], v[88:91], v[100:103], 0\n" \
"v_bfe_u32 v14, v0, 8, 1\n" \
"v_lshl_add_u32 v13, v14, 3, v13\n" \
"v_mfma_f32_16x16x32_bf16 v[56:59], v[88:91], v[96:99], 0\n" \
"ds_read_b128 v[88:91], v217 offset:41984\n" \
"v_mov_b32 v10, 0x3f80\n" \
"v_mfma_f32_16x16x32_bf16 v[248:251], v[92:95], v[100:103], 0\n" \
"v_mov_b32 v11, 0\n" \
"v_and_b32 v14, 0x30, v0\n" \
"v_mfma_f32_16x16x32_bf16 v[60:63], v[92:95], v[96:99], 0\n" \
"ds_read_b128 v[92:95], v217 offset:43008\n" \
"v_lshlrev_b64 v[10:11], v14, v[10:11]\n" \
"s_waitcnt lgkmcnt(2)\n" \
"v_mfma_f32_16x16x32_bf16 v[252:255], v[80:83], v[100:103], 0\n" \
"buffer_load_dwordx4 v[160:163], v0, %0, s53 offen offset:0 sc1\n" \
"v_mfma_f32_16x16x32_bf16 v[64:67], v[80:83], v[96:99], 0\n" \
"ds_read_b128 v[80:83], v217 offset:44032\n" \
"v_lshrrev_b32 v14, 2, v0\n" \
"v_mfma_f32_16x16x32_bf16 v[68:71], v[84:87], v[108:111], v[68:71]\n" \
"v_xor_b32 v14, v14, v0\n" \
"v_and_b32 v14, 0xc0, v14\n" \
"v_mfma_f32_16x16x32_bf16 v[48:51], v[84:87], v[104:107], v[48:51]\n" \
"ds_read_b128 v[84:87], v217 offset:45056\n" \
"v_cmp_eq_u32 s[28:29], 0, v14\n" \
"s_nop 1\n" \
"v_cndmask_b32 v8, 0, v10, s[28:29]\n" \
"v_cndmask_b32 v9, 0, v11, s[28:29]\n" \
"s_waitcnt lgkmcnt(2)\n" \
"v_mfma_f32_16x16x32_bf16 v[72:75], v[88:91], v[108:111], v[72:75]\n" \
"buffer_load_dwordx4 v[164:167], v0, %0, s54 offen offset:0 sc1\n" \
"v_mfma_f32_16x16x32_bf16 v[52:55], v[88:91], v[104:107], v[52:55]\n" \
"ds_read_b128 v[88:91], v218 offset:0\n" \
"v_mov_b32 v10, v8\n" \
"v_mfma_f32_16x16x32_bf16 v[76:79], v[92:95], v[108:111], v[76:79]\n" \
"v_mov_b32 v11, v9\n" \
"v_mfma_f32_16x16x32_bf16 v[56:59], v[92:95], v[104:107], v[56:59]\n" \
"ds_read_b128 v[92:95], v218 offset:1024\n" \
"s_waitcnt lgkmcnt(2)\n" \
"v_mfma_f32_16x16x32_bf16 v[248:251], v[80:83], v[108:111], v[248:251]\n" \
"buffer_load_dwordx4 v[168:171], v0, %0, s53 offen offset:1024 sc1\n" \
"v_mfma_f32_16x16x32_bf16 v[60:63], v[80:83], v[104:107], v[60:63]\n" \
"ds_read_b128 v[80:83], v218 offset:2048\n" \
"v_mfma_f32_16x16x32_bf16 v[252:255], v[84:87], v[108:111], v[252:255]\n" \
"v_mfma_f32_16x16x32_bf16 v[64:67], v[84:87], v[104:107], v[64:67]\n" \
"ds_read_b128 v[84:87], v218 offset:3072\n" \
"s_barrier\n" \
"s_waitcnt lgkmcnt(2)\n" \
"v_mfma_f32_16x16x32_bf16 v[68:71], v[88:91], v[116:119], v[68:71]\n" \
"buffer_load_dwordx4 v[172:175], v0, %0, s54 offen offset:1024 sc1\n" \
"v_mfma_f32_16x16x32_bf16 v[48:51], v[88:91], v[112:115], v[48:51]\n" \
"ds_read_b128 v[88:91], v218 offset:4096\n" \
"v_mfma_f32_16x16x32_bf16 v[72:75], v[92:95], v[116:119], v[72:75]\n" \
"v_mfma_f32_16x16x32_bf16 v[52:55], v[92:95], v[112:115], v[52:55]\n" \
"ds_read_b128 v[92:95], v218 offset:10240\n" \
"s_waitcnt lgkmcnt(2)\n" \
"v_mfma_f32_16x16x32_bf16 v[76:79], v[80:83], v[116:119], v[76:79]\n" \
"buffer_load_dwordx4 v[176:179], v0, %0, s53 offen offset:2048 sc1\n" \
"v_mfma_f32_16x16x32_bf16 v[56:59], v[80:83], v[112:115], v[56:59]\n" \
"ds_read_b128 v[80:83], v218 offset:11264\n" \
"v_mfma_f32_16x16x32_bf16 v[248:251], v[84:87], v[116:119], v[248:251]\n" \
"v_mfma_f32_16x16x32_bf16 v[60:63], v[84:87], v[112:115], v[60:63]\n" \
"ds_read_b128 v[84:87], v218 offset:12288\n" \
"s_waitcnt lgkmcnt(2)\n" \
"v_mfma_f32_16x16x32_bf16 v[252:255], v[88:91], v[116:119], v[252:255]\n" \
"buffer_load_dwordx4 v[180:183], v0, %0, s54 offen offset:2048 sc1\n" \
"s_waitcnt vmcnt(14)\n" \
"v_bitop3_b32 v12, v128, v132, s24 bitop3:0x7e\n" \
"v_mfma_f32_16x16x32_bf16 v[64:67], v[88:91], v[112:115], v[64:67]\n" \
"ds_read_b128 v[88:91], v218 offset:13312\n" \
"v_mfma_f32_16x16x32_bf16 v[68:71], v[92:95], v[124:127], v[68:71]\n" \
"s_waitcnt vmcnt(13)\n" \
"v_bitop3_b32 v12, v12, v136, s24 bitop3:0xf6\n" \
"s_waitcnt vmcnt(12)\n" \
"v_bitop3_b32 v12, v12, v140, s24 bitop3:0xf6\n" \
"v_mfma_f32_16x16x32_bf16 v[48:51], v[92:95], v[120:123], v[48:51]\n" \
"ds_read_b128 v[92:95], v218 offset:14336\n" \
"s_waitcnt lgkmcnt(2)\n" \
"v_mfma_f32_16x16x32_bf16 v[72:75], v[80:83], v[124:127], v[72:75]\n" \
"buffer_load_dwordx4 v[184:187], v0, %0, s53 offen offset:3072 sc1\n" \
"s_waitcnt vmcnt(12)\n" \
"v_bitop3_b32 v12, v12, v144, s24 bitop3:0xf6\n" \
"s_waitcnt vmcnt(11)\n" \
"v_bitop3_b32 v12, v12, v148, s24 bitop3:0xf6\n" \
"v_mfma_f32_16x16x32_bf16 v[52:55], v[80:83], v[120:123], v[52:55]\n" \
"ds_read_b128 v[80:83], v217 offset:30720\n" \
"v_mfma_f32_16x16x32_bf16 v[76:79], v[84:87], v[124:127], v[76:79]\n" \
"s_waitcnt vmcnt(10)\n" \
"v_bitop3_b32 v12, v12, v152, s24 bitop3:0xf6\n" \
"s_waitcnt vmcnt(8)\n" \
"v_bitop3_b32 v12, v12, v156, s24 bitop3:0xf6\n" \
"v_mfma_f32_16x16x32_bf16 v[56:59], v[84:87], v[120:123], v[56:59]\n" \
"ds_read_b128 v[84:87], v217 offset:31744\n" \
"s_waitcnt lgkmcnt(2)\n" \
"v_mfma_f32_16x16x32_bf16 v[248:251], v[88:91], v[124:127], v[248:251]\n" \
"buffer_load_dwordx4 v[188:191], v0, %0, s55 offen offset:0 sc1\n" \
"s_add_u32 s53, s53, 4096\n" \
"s_add_u32 s54, s54, 4096\n" \
"v_and_b32 v12, 1, v12\n" \
"v_cmp_ne_u32 vcc, 0, v12\n" \
"v_mfma_f32_16x16x32_bf16 v[60:63], v[88:91], v[120:123], v[60:63]\n" \
"ds_read_b128 v[88:91], v217 offset:32768\n" \
"v_mfma_f32_16x16x32_bf16 v[252:255], v[92:95], v[124:127], v[252:255]\n" \
"s_cbranch_vccnz .Lc109_%=\n" \
".Lc110_%=:\n" \
"v_mfma_f32_16x16x32_bf16 v[64:67], v[92:95], v[120:123], v[64:67]\n" \
"ds_read_b128 v[92:95], v217 offset:33792\n" \
"s_barrier\n" \
"s_waitcnt lgkmcnt(2)\n" \
"v_mfma_f32_16x16x32_bf16 v[68:71], v[80:83], v[132:135], v[68:71]\n" \
"v_mfma_f32_16x16x32_bf16 v[48:51], v[80:83], v[128:131], v[48:51]\n" \
"ds_read_b128 v[80:83], v217 offset:34816\n" \
"v_mfma_f32_16x16x32_bf16 v[72:75], v[84:87], v[132:135], v[72:75]\n" \
"v_mfma_f32_16x16x32_bf16 v[52:55], v[84:87], v[128:131], v[52:55]\n" \
"ds_read_b128 v[84:87], v217 offset:51200\n" \
"s_waitcnt lgkmcnt(2)\n" \
"v_mfma_f32_16x16x32_bf16 v[76:79], v[88:91], v[132:135], v[76:79]\n" \
"buffer_load_dwordx4 v[230:233], v0, %0, s49 offen sc1\n" \
"v_mfma_f32_16x16x32_bf16 v[56:59], v[88:91], v[128:131], v[56:59]\n" \
"ds_read_b128 v[88:91], v217 offset:52224\n" \
"v_mfma_f32_16x16x32_bf16 v[248:251], v[92:95], v[132:135], v[248:251]\n" \
"v_mfma_f32_16x16x32_bf16 v[60:63], v[92:95], v[128:131], v[60:63]\n" \
"ds_read_b128 v[92:95], v217 offset:53248\n" \
"s_waitcnt lgkmcnt(2)\n" \
"v_mfma_f32_16x16x32_bf16 v[252:255], v[80:83], v[132:135], v[252:255]\n" \
"buffer_load_dwordx4 v[238:241], v0, %0, s49 offen offset:1024 sc1\n" \
"v_mfma_f32_16x16x32_bf16 v[64:67], v[80:83], v[128:131], v[64:67]\n" \
"ds_read_b128 v[80:83], v217 offset:54272\n" \
"v_mfma_f32_16x16x32_bf16 v[68:71], v[84:87], v[140:143], v[68:71]\n" \
"v_mfma_f32_16x16x32_bf16 v[48:51], v[84:87], v[136:139], v[48:51]\n" \
"ds_read_b128 v[84:87], v217 offset:55296\n" \
"s_waitcnt lgkmcnt(2)\n" \
"v_mfma_f32_16x16x32_bf16 v[72:75], v[88:91], v[140:143], v[72:75]\n" \
"buffer_load_dwordx2 v[246:247], v13, %0, s49 offen offset:2048 sc1\n" \
"v_mfma_f32_16x16x32_bf16 v[52:55], v[88:91], v[136:139], v[52:55]\n" \
"ds_read_b128 v[88:91], v218 offset:20480\n" \
"v_mfma_f32_16x16x32_bf16 v[76:79], v[92:95], v[140:143], v[76:79]\n" \
"v_mfma_f32_16x16x32_bf16 v[56:59], v[92:95], v[136:139], v[56:59]\n" \
"ds_read_b128 v[92:95], v218 offset:21504\n" \
"s_waitcnt lgkmcnt(2)\n" \
"v_mfma_f32_16x16x32_bf16 v[248:251], v[80:83], v[140:143], v[248:251]\n" \
"v_mfma_f32_16x16x32_bf16 v[60:63], v[80:83], v[136:139], v[60:63]\n" \
"ds_read_b128 v[80:83], v218 offset:22528\n" \
"v_mfma_f32_16x16x32_bf16 v[252:255], v[84:87], v[140:143], v[252:255]\n" \
"v_mfma_f32_16x16x32_bf16 v[64:67], v[84:87], v[136:139], v[64:67]\n" \
"ds_read_b128 v[84:87], v218 offset:23552\n" \
"s_barrier\n" \
"s_waitcnt lgkmcnt(2)\n" \
"v_mfma_f32_16x16x32_bf16 v[68:71], v[88:91], v[148:151], v[68:71]\n" \
"v_mfma_f32_16x16x32_bf16 v[48:51], v[88:91], v[144:147], v[48:51]\n" \
"ds_read_b128 v[88:91], v218 offset:24576\n" \
"v_mfma_f32_16x16x32_bf16 v[72:75], v[92:95], v[148:151], v[72:75]\n" \
"v_mfma_f32_16x16x32_bf16 v[52:55], v[92:95], v[144:147], v[52:55]\n" \
"ds_read_b128 v[92:95], v217 offset:20480\n" \
"s_waitcnt lgkmcnt(2)\n" \
"v_mfma_f32_16x16x32_bf16 v[76:79], v[80:83], v[148:151], v[76:79]\n" \
"v_mfma_f32_16x16x32_bf16 v[56:59], v[80:83], v[144:147], v[56:59]\n" \
"ds_read_b128 v[80:83], v217 offset:21504\n" \
"v_mfma_f32_16x16x32_bf16 v[248:251], v[84:87], v[148:151], v[248:251]\n" \
"v_mfma_f32_16x16x32_bf16 v[60:63], v[84:87], v[144:147], v[60:63]\n" \
"ds_read_b128 v[84:87], v217 offset:22528\n" \
"s_waitcnt lgkmcnt(2)\n" \
"v_mfma_f32_16x16x32_bf16 v[252:255], v[88:91], v[148:151], v[252:255]\n" \
"v_mfma_f32_16x16x32_bf16 v[64:67], v[88:91], v[144:147], v[64:67]\n" \
"ds_read_b128 v[88:91], v217 offset:23552\n" \
"v_mfma_f32_16x16x32_bf16 v[68:71], v[92:95], v[156:159], v[68:71]\n" \
"v_mfma_f32_16x16x32_bf16 v[48:51], v[92:95], v[152:155], v[48:51]\n" \
"ds_read_b128 v[92:95], v217 offset:24576\n" \
"s_waitcnt lgkmcnt(2)\n" \
"v_mfma_f32_16x16x32_bf16 v[72:75], v[80:83], v[156:159], v[72:75]\n" \
"buffer_load_dwordx4 v[192:195], v0, %0, s53 offen offset:0 sc1\n" \
"s_waitcnt vmcnt(10)\n" \
"v_bitop3_b32 v12, v160, v164, s24 bitop3:0x7e\n" \
"v_mfma_f32_16x16x32_bf16 v[52:55], v[80:83], v[152:155], v[52:55]\n" \
"ds_read_b128 v[80:83], v216 offset:40960\n" \
"v_mfma_f32_16x16x32_bf16 v[76:79], v[84:87], v[156:159], v[76:79]\n" \
"s_waitcnt vmcnt(9)\n" \
"v_bitop3_b32 v12, v12, v168, s24 bitop3:0xf6\n" \
"s_waitcnt vmcnt(8)\n" \
"v_bitop3_b32 v12, v12, v172, s24 bitop3:0xf6\n" \
"v_mfma_f32_16x16x32_bf16 v[56:59], v[84:87], v[152:155], v[56:59]\n" \
"ds_read_b128 v[84:87], v216 offset:41984\n" \
"s_waitcnt lgkmcnt(2)\n" \
"v_mfma_f32_16x16x32_bf16 v[248:251], v[88:91], v[156:159], v[248:251]\n" \
"buffer_load_dwordx4 v[196:199], v0, %0, s55 offen offset:1024 sc1\n" \
"v_and_b32 v12, 1, v12\n" \
"v_cmp_ne_u32 vcc, 0, v12\n" \
"v_mfma_f32_16x16x32_bf16 v[60:63], v[88:91], v[152:155], v[60:63]\n" \
"ds_read_b128 v[88:91], v216 offset:43008\n" \
"v_mfma_f32_16x16x32_bf16 v[252:255], v[92:95], v[156:159], v[252:255]\n" \
"s_cbranch_vccnz .Lc111_%=\n" \
".Lc112_%=:\n" \
"v_mfma_f32_16x16x32_bf16 v[64:67], v[92:95], v[152:155], v[64:67]\n" \
"ds_read_b128 v[92:95], v216 offset:44032\n" \
"s_waitcnt lgkmcnt(2)\n" \
"v_mfma_f32_16x16x32_bf16 v[68:71], v[80:83], v[164:167], v[68:71]\n" \
"buffer_load_dwordx4 v[200:203], v0, %0, s53 offen offset:1024 sc1\n" \
"v_mfma_f32_16x16x32_bf16 v[48:51], v[80:83], v[160:163], v[48:51]\n" \
"ds_read_b128 v[80:83], v216 offset:45056\n" \
"v_mfma_f32_16x16x32_bf16 v[72:75], v[84:87], v[164:167], v[72:75]\n" \
"v_mfma_f32_16x16x32_bf16 v[52:55], v[84:87], v[160:163], v[52:55]\n" \
"ds_read_b128 v[84:87], v216 offset:51200\n" \
"s_waitcnt lgkmcnt(2)\n" \
"v_mfma_f32_16x16x32_bf16 v[76:79], v[88:91], v[164:167], v[76:79]\n" \
"buffer_load_dwordx4 v[204:207], v0, %0, s55 offen offset:2048 sc1\n" \
"v_mfma_f32_16x16x32_bf16 v[56:59], v[88:91], v[160:163], v[56:59]\n" \
"ds_read_b128 v[88:91], v216 offset:52224\n" \
"v_mfma_f32_16x16x32_bf16 v[248:251], v[92:95], v[164:167], v[248:251]\n" \
"v_mfma_f32_16x16x32_bf16 v[60:63], v[92:95], v[160:163], v[60:63]\n" \
"ds_read_b128 v[92:95], v216 offset:53248\n" \
"s_waitcnt lgkmcnt(2)\n" \
"v_mfma_f32_16x16x32_bf16 v[252:255], v[80:83], v[164:167], v[252:255]\n" \
"buffer_load_dwordx4 v[208:211], v0, %0, s53 offen offset:2048 sc1\n" \
"v_mfma_f32_16x16x32_bf16 v[64:67], v[80:83], v[160:163], v[64:67]\n" \
"ds_read_b128 v[80:83], v216 offset:54272\n" \
"v_mfma_f32_16x16x32_bf16 v[68:71], v[84:87], v[172:175], v[68:71]\n" \
"v_mfma_f32_16x16x32_bf16 v[48:51], v[84:87], v[168:171], v[48:51]\n" \
"ds_read_b128 v[84:87], v216 offset:55296\n" \
"s_waitcnt lgkmcnt(2)\n" \
"v_mfma_f32_16x16x32_bf16 v[72:75], v[88:91], v[172:175], v[72:75]\n" \
"buffer_load_dwordx4 v[212:215], v0, %0, s55 offen offset:3072 sc1\n" \
"v_mfma_f32_16x16x32_bf16 v[52:55], v[88:91], v[168:171], v[52:55]\n" \
"ds_read_b128 v[88:91], v217 offset:0\n" \
"v_mfma_f32_16x16x32_bf16 v[76:79], v[92:95], v[172:175], v[76:79]\n" \
"s_waitcnt vmcnt(11)\n" \
"v_bitop3_b32 v12, v176, v180, s24 bitop3:0x7e\n" \
"v_mfma_f32_16x16x32_bf16 v[56:59], v[92:95], v[168:171], v[56:59]\n" \
"ds_read_b128 v[92:95], v217 offset:1024\n" \
"s_waitcnt lgkmcnt(2)\n" \
"v_mfma_f32_16x16x32_bf16 v[248:251], v[80:83], v[172:175], v[248:251]\n" \
"v_and_b32 v12, 1, v12\n" \
"v_cmp_ne_u32 vcc, 0, v12\n" \
"v_mfma_f32_16x16x32_bf16 v[60:63], v[80:83], v[168:171], v[60:63]\n" \
"ds_read_b128 v[80:83], v217 offset:2048\n" \
"v_mfma_f32_16x16x32_bf16 v[252:255], v[84:87], v[172:175], v[252:255]\n" \
"s_cbranch_vccnz .Lc113_%=\n" \
".Lc114_%=:\n" \
"v_mfma_f32_16x16x32_bf16 v[64:67], v[84:87], v[168:171], v[64:67]\n" \
"ds_read_b128 v[84:87], v217 offset:3072\n" \
"s_barrier\n" \
"s_waitcnt lgkmcnt(2)\n" \
"v_mfma_f32_16x16x32_bf16 v[68:71], v[88:91], v[180:183], v[68:71]\n" \
"ds_read_b128 v[96:99], v223 offset:0\n" \
"ds_read_b128 v[100:103], v223 offset:1024\n" \
"v_mfma_f32_16x16x32_bf16 v[48:51], v[88:91], v[176:179], v[48:51]\n" \
"ds_read_b128 v[88:91], v217 offset:4096\n" \
"v_mfma_f32_16x16x32_bf16 v[72:75], v[92:95], v[180:183], v[72:75]\n" \
"ds_read_b128 v[104:107], v223 offset:2048\n" \
"ds_read_b128 v[108:111], v223 offset:3072\n" \
"v_mfma_f32_16x16x32_bf16 v[52:55], v[92:95], v[176:179], v[52:55]\n" \
"ds_read_b128 v[92:95], v217 offset:14336\n" \
"ds_read_b128 v[32:35], v217 offset:11264\n" \
"s_waitcnt lgkmcnt(7)\n" \
"v_mfma_f32_16x16x32_bf16 v[76:79], v[80:83], v[180:183], v[76:79]\n" \
"s_waitcnt vmcnt(9)\n" \
"v_bitop3_b32 v12, v184, v188, s24 bitop3:0x7e\n" \
"ds_read_b128 v[112:115], v223 offset:4096\n" \
"ds_read_b128 v[116:119], v223 offset:5120\n" \
"v_mfma_f32_16x16x32_bf16 v[56:59], v[80:83], v[176:179], v[56:59]\n" \
"ds_read_b128 v[80:83], v217 offset:12288\n" \
"ds_read_b128 v[36:39], v219 offset:14336\n" \
"v_mfma_f32_16x16x32_bf16 v[248:251], v[84:87], v[180:183], v[248:251]\n" \
"v_and_b32 v12, 1, v12\n" \
"v_cmp_ne_u32 vcc, 0, v12\n" \
"ds_read_b128 v[120:123], v223 offset:6144\n" \
"ds_read_b128 v[124:127], v223 offset:7168\n" \
"v_mfma_f32_16x16x32_bf16 v[60:63], v[84:87], v[176:179], v[60:63]\n" \
"ds_read_b128 v[84:87], v217 offset:13312\n" \
"ds_read_b128 v[40:43], v219 offset:12288\n" \
"s_waitcnt lgkmcnt(9)\n" \
"v_mfma_f32_16x16x32_bf16 v[252:255], v[88:91], v[180:183], v[252:255]\n" \
"s_cbranch_vccnz .Lc115_%=\n" \
".Lc116_%=:\n" \
"ds_read_b128 v[128:131], v223 offset:8192\n" \
"ds_read_b128 v[132:135], v223 offset:9216\n" \
"v_mfma_f32_16x16x32_bf16 v[64:67], v[88:91], v[176:179], v[64:67]\n" \
"ds_read_b128 v[88:91], v217 offset:10240\n" \
"ds_read_b128 v[44:47], v219 offset:13312\n" \
"s_waitcnt lgkmcnt(9)\n" \
"v_mfma_f32_16x16x32_bf16 v[64:67], v[92:95], v[184:187], v[64:67]\n" \
"ds_read_b128 v[92:95], v219 offset:10240\n" \
"v_mfma_f32_16x16x32_bf16 v[56:59], v[80:83], v[184:187], v[56:59]\n" \
"ds_read_b128 v[80:83], v219 offset:11264\n" \
"v_cvt_pk_bf16_f32 v68, v68, v69\n" \
"v_cvt_pk_bf16_f32 v69, v70, v71\n" \
"s_waitcnt lgkmcnt(3)\n" \
"v_mfma_f32_16x16x32_bf16 v[60:63], v[84:87], v[184:187], v[60:63]\n" \
"ds_read_b128 v[84:87], v217 offset:45056\n" \
"ds_write_b64 v221, v[68:69] offset:0\n" \
"v_cvt_pk_bf16_f32 v72, v72, v73\n" \
"v_mfma_f32_16x16x32_bf16 v[48:51], v[88:91], v[184:187], v[48:51]\n" \
"ds_read_b128 v[88:91], v219 offset:45056\n" \
"v_cvt_pk_bf16_f32 v73, v74, v75\n" \
"ds_write_b64 v221, v[72:73] offset:512\n" \
"v_mfma_f32_16x16x32_bf16 v[52:55], v[32:35], v[184:187], v[52:55]\n" \
"ds_read_b128 v[32:35], v218 offset:4096\n" \
"v_cvt_pk_bf16_f32 v76, v76, v77\n" \
"v_cvt_pk_bf16_f32 v77, v78, v79\n" \
"v_mfma_f32_16x16x32_bf16 v[64:67], v[36:39], v[188:191], v[64:67]\n" \
"ds_read_b128 v[36:39], v220 offset:4096\n" \
"ds_write_b64 v221, v[76:77] offset:1024\n" \
"v_cvt_pk_bf16_f32 v248, v248, v249\n" \
"s_waitcnt lgkmcnt(9)\n" \
"v_mfma_f32_16x16x32_bf16 v[56:59], v[40:43], v[188:191], v[56:59]\n" \
"ds_read_b128 v[40:43], v218 offset:14336\n" \
"v_cvt_pk_bf16_f32 v249, v250, v251\n" \
"ds_write_b64 v221, v[248:249] offset:1536\n" \
"v_mfma_f32_16x16x32_bf16 v[60:63], v[44:47], v[188:191], v[60:63]\n" \
"ds_read_b128 v[44:47], v220 offset:14336\n" \
"v_cvt_pk_bf16_f32 v252, v252, v253\n" \
"v_cvt_pk_bf16_f32 v253, v254, v255\n" \
"s_waitcnt lgkmcnt(10)\n" \
"v_mfma_f32_16x16x32_bf16 v[48:51], v[92:95], v[188:191], v[48:51]\n" \
"ds_read_b128 v[92:95], v217 offset:43008\n" \
"ds_write_b64 v221, v[252:253] offset:2048\n" \
"v_mfma_f32_16x16x32_bf16 v[52:55], v[80:83], v[188:191], v[52:55]\n" \
"ds_read_b128 v[80:83], v219 offset:43008\n" \
"v_mfma_f32_16x16x32_bf16 v[48:51], v[8:11], v[96:99], v[48:51]\n" \
"v_mfma_f32_16x16x32_bf16 v[52:55], v[8:11], v[104:107], v[52:55]\n" \
"v_mfma_f32_16x16x32_bf16 v[56:59], v[8:11], v[112:115], v[56:59]\n" \
"v_mfma_f32_16x16x32_bf16 v[60:63], v[8:11], v[120:123], v[60:63]\n" \
"v_mfma_f32_16x16x32_bf16 v[64:67], v[8:11], v[128:131], v[64:67]\n" \
"s_waitcnt lgkmcnt(1)\n" \
"s_barrier\n" \
"v_mfma_f32_16x16x32_bf16 v[48:51], v[8:11], v[100:103], v[48:51]\n" \
"ds_read_b64 v[244:245], v222 offset:2048\n" \
"s_waitcnt vmcnt(4)\n" \
"v_bitop3_b32 v12, v192, v196, s24 bitop3:0x7e\n" \
"v_mfma_f32_16x16x32_bf16 v[52:55], v[8:11], v[108:111], v[52:55]\n" \
"ds_read_b64 v[236:237], v222 offset:1024\n" \
"s_waitcnt vmcnt(3)\n" \
"v_bitop3_b32 v12, v12, v200, s24 bitop3:0xf6\n" \
"s_waitcnt vmcnt(2)\n" \
"v_bitop3_b32 v12, v12, v204, s24 bitop3:0xf6\n" \
"v_mfma_f32_16x16x32_bf16 v[56:59], v[8:11], v[116:119], v[56:59]\n" \
"ds_read_b64 v[242:243], v222 offset:1536\n" \
"s_waitcnt vmcnt(1)\n" \
"v_bitop3_b32 v12, v12, v208, s24 bitop3:0xf6\n" \
"s_waitcnt vmcnt(0)\n" \
"v_bitop3_b32 v12, v12, v212, s24 bitop3:0xf6\n" \
"v_mfma_f32_16x16x32_bf16 v[60:63], v[8:11], v[124:127], v[60:63]\n" \
"ds_read_b64 v[228:229], v222 offset:0\n" \
"v_and_b32 v12, 1, v12\n" \
"v_cmp_ne_u32 vcc, 0, v12\n" \
"v_mfma_f32_16x16x32_bf16 v[64:67], v[8:11], v[132:135], v[64:67]\n" \
"ds_read_b64 v[234:235], v222 offset:512\n" \
"s_cbranch_vccnz .Lc117_%=\n" \
".Lc118_%=:\n" \
"v_mfma_f32_16x16x32_bf16 v[64:67], v[84:87], v[192:195], v[64:67]\n" \
"ds_read_b128 v[84:87], v218 offset:2048\n" \
"v_mfma_f32_16x16x32_bf16 v[64:67], v[88:91], v[196:199], v[64:67]\n" \
"ds_read_b128 v[88:91], v220 offset:2048\n" \
"v_mfma_f32_16x16x32_bf16 v[64:67], v[32:35], v[200:203], v[64:67]\n" \
"ds_read_b128 v[32:35], v218 offset:12288\n" \
"v_mfma_f32_16x16x32_bf16 v[64:67], v[36:39], v[204:207], v[64:67]\n" \
"ds_read_b128 v[36:39], v220 offset:12288\n" \
"v_mfma_f32_16x16x32_bf16 v[64:67], v[40:43], v[208:211], v[64:67]\n" \
"ds_read_b128 v[40:43], v217 offset:44032\n" \
"s_waitcnt lgkmcnt(9)\n" \
"v_mfma_f32_16x16x32_bf16 v[64:67], v[8:11], v[244:247], v[64:67]\n" \
"v_mfma_f32_16x16x32_bf16 v[64:67], v[44:47], v[212:215], v[64:67]\n" \
"ds_read_b128 v[44:47], v219 offset:44032\n" \
"v_mfma_f32_16x16x32_bf16 v[56:59], v[92:95], v[192:195], v[56:59]\n" \
"ds_read_b128 v[92:95], v218 offset:3072\n" \
"v_mfma_f32_16x16x32_bf16 v[56:59], v[80:83], v[196:199], v[56:59]\n" \
"ds_read_b128 v[80:83], v220 offset:3072\n" \
"s_waitcnt lgkmcnt(6)\n" \
"v_mfma_f32_16x16x32_bf16 v[56:59], v[84:87], v[200:203], v[56:59]\n" \
"ds_read_b128 v[84:87], v218 offset:13312\n" \
"v_cvt_pk_bf16_f32 v24, v64, v65\n" \
"v_cvt_pk_bf16_f32 v25, v66, v67\n" \
"v_mfma_f32_16x16x32_bf16 v[56:59], v[88:91], v[204:207], v[56:59]\n" \
"ds_read_b128 v[88:91], v220 offset:13312\n" \
"v_cvt_pk_bf16_f32 v26, v64, v65\n" \
"v_cvt_pk_bf16_f32 v27, v66, v67\n" \
"s_waitcnt lgkmcnt(6)\n" \
"v_mfma_f32_16x16x32_bf16 v[56:59], v[32:35], v[208:211], v[56:59]\n" \
"ds_read_b128 v[32:35], v217 offset:40960\n" \
"v_cvt_pk_bf16_f32 v28, v64, v65\n" \
"v_cvt_pk_bf16_f32 v29, v66, v67\n" \
"v_mfma_f32_16x16x32_bf16 v[56:59], v[8:11], v[236:239], v[56:59]\n" \
"v_and_b32 v24, -2, v24\n" \
"v_mfma_f32_16x16x32_bf16 v[56:59], v[36:39], v[212:215], v[56:59]\n" \
"ds_read_b128 v[36:39], v219 offset:40960\n" \
"s_cmp_lg_u32 s24, 0\n" \
"s_cbranch_scc1 .Lc119_%=\n" \
"v_permlane16_swap_b32 v25, v27\n" \
"v_permlane16_swap_b32 v24, v26\n" \
"buffer_store_dwordx4 v[24:27], v4, %0, s49 offen offset:2048 sc1\n" \
".Lc119_%=:\n" \
"s_cmp_lg_u32 s24, 1\n" \
"s_cbranch_scc1 .Lc120_%=\n" \
"buffer_store_dwordx2 v[28:29], v225, %1, s58 offen offset:128\n" \
".Lc120_%=:\n" \
"s_waitcnt lgkmcnt(6)\n" \
"v_mfma_f32_16x16x32_bf16 v[60:63], v[40:43], v[192:195], v[60:63]\n" \
"ds_read_b128 v[40:43], v218 offset:0\n" \
"v_mfma_f32_16x16x32_bf16 v[60:63], v[44:47], v[196:199], v[60:63]\n" \
"ds_read_b128 v[44:47], v220 offset:0\n" \
"s_waitcnt lgkmcnt(6)\n" \
"v_mfma_f32_16x16x32_bf16 v[60:63], v[92:95], v[200:203], v[60:63]\n" \
"ds_read_b128 v[92:95], v218 offset:10240\n" \
"v_cvt_pk_bf16_f32 v20, v56, v57\n" \
"v_cvt_pk_bf16_f32 v21, v58, v59\n" \
"v_mfma_f32_16x16x32_bf16 v[60:63], v[80:83], v[204:207], v[60:63]\n" \
"ds_read_b128 v[80:83], v220 offset:10240\n" \
"s_waitcnt lgkmcnt(6)\n" \
"v_mfma_f32_16x16x32_bf16 v[60:63], v[84:87], v[208:211], v[60:63]\n" \
"ds_read_b128 v[84:87], v217 offset:41984\n" \
"v_mfma_f32_16x16x32_bf16 v[60:63], v[8:11], v[240:243], v[60:63]\n" \
"v_mfma_f32_16x16x32_bf16 v[60:63], v[88:91], v[212:215], v[60:63]\n" \
"ds_read_b128 v[88:91], v219 offset:41984\n" \
"s_waitcnt lgkmcnt(6)\n" \
"v_mfma_f32_16x16x32_bf16 v[48:51], v[32:35], v[192:195], v[48:51]\n" \
"ds_read_b128 v[32:35], v218 offset:1024\n" \
"v_mfma_f32_16x16x32_bf16 v[48:51], v[36:39], v[196:199], v[48:51]\n" \
"ds_read_b128 v[36:39], v220 offset:1024\n" \
"s_waitcnt lgkmcnt(6)\n" \
"v_mfma_f32_16x16x32_bf16 v[48:51], v[40:43], v[200:203], v[48:51]\n" \
"ds_read_b128 v[40:43], v218 offset:11264\n" \
"v_cvt_pk_bf16_f32 v22, v60, v61\n" \
"v_cvt_pk_bf16_f32 v23, v62, v63\n" \
"v_mfma_f32_16x16x32_bf16 v[48:51], v[44:47], v[204:207], v[48:51]\n" \
"ds_read_b128 v[44:47], v220 offset:11264\n" \
"s_waitcnt lgkmcnt(6)\n" \
"v_mfma_f32_16x16x32_bf16 v[48:51], v[92:95], v[208:211], v[48:51]\n" \
"s_cmp_lg_u32 s24, 0\n" \
"s_cbranch_scc1 .Lc121_%=\n" \
"v_and_b32 v20, -2, v20\n" \
"buffer_store_dwordx4 v[20:23], v0, %0, s49 offen offset:1024 sc1\n" \
".Lc121_%=:\n" \
"s_cmp_lg_u32 s24, 1\n" \
"s_cbranch_scc1 .Lc122_%=\n" \
"buffer_store_dwordx4 v[20:23], v224, %1, s58 offen offset:64\n" \
".Lc122_%=:\n" \
"v_mfma_f32_16x16x32_bf16 v[48:51], v[8:11], v[228:231], v[48:51]\n" \
"v_mfma_f32_16x16x32_bf16 v[48:51], v[80:83], v[212:215], v[48:51]\n" \
"s_waitcnt lgkmcnt(4)\n" \
"v_mfma_f32_16x16x32_bf16 v[52:55], v[84:87], v[192:195], v[52:55]\n" \
"v_mfma_f32_16x16x32_bf16 v[52:55], v[88:91], v[196:199], v[52:55]\n" \
"s_waitcnt lgkmcnt(2)\n" \
"v_mfma_f32_16x16x32_bf16 v[52:55], v[32:35], v[200:203], v[52:55]\n" \
"s_nop 2\n" \
"v_cvt_pk_bf16_f32 v16, v48, v49\n" \
"v_cvt_pk_bf16_f32 v17, v50, v51\n" \
"v_mfma_f32_16x16x32_bf16 v[52:55], v[36:39], v[204:207], v[52:55]\n" \
"s_waitcnt lgkmcnt(0)\n" \
"v_mfma_f32_16x16x32_bf16 v[52:55], v[40:43], v[208:211], v[52:55]\n" \
"v_mfma_f32_16x16x32_bf16 v[52:55], v[8:11], v[232:235], v[52:55]\n" \
"v_mfma_f32_16x16x32_bf16 v[52:55], v[44:47], v[212:215], v[52:55]\n" \
"s_nop 7\n" \
"v_cvt_pk_bf16_f32 v18, v52, v53\n" \
"v_cvt_pk_bf16_f32 v19, v54, v55\n" \
"s_cmp_lg_u32 s24, 0\n" \
"s_cbranch_scc1 .Lc123_%=\n" \
"v_and_b32 v16, -2, v16\n" \
"buffer_store_dwordx4 v[16:19], v0, %0, s49 offen offset:0 sc1\n" \
"s_barrier\n" \
"s_mov_b32 s25, 0x3f803f80\n" \
"v_dot2_f32_bf16 v30, v16, s25, 0\n" \
"v_dot2_f32_bf16 v31, v16, v16, 0\n" \
"v_dot2_f32_bf16 v30, v17, s25, v30\n" \
"v_dot2_f32_bf16 v31, v17, v17, v31\n" \
"v_dot2_f32_bf16 v30, v18, s25, v30\n" \
"v_dot2_f32_bf16 v31, v18, v18, v31\n" \
"v_dot2_f32_bf16 v30, v19, s25, v30\n" \
"v_dot2_f32_bf16 v31, v19, v19, v31\n" \
"v_dot2_f32_bf16 v30, v20, s25, v30\n" \
"v_dot2_f32_bf16 v31, v20, v20, v31\n" \
"v_dot2_f32_bf16 v30, v21, s25, v30\n" \
"v_dot2_f32_bf16 v31, v21, v21, v31\n" \
"v_dot2_f32_bf16 v30, v22, s25, v30\n" \
"v_dot2_f32_bf16 v31, v22, v22, v31\n" \
"v_dot2_f32_bf16 v30, v23, s25, v30\n" \
"v_dot2_f32_bf16 v31, v23, v23, v31\n" \
"v_dot2_f32_bf16 v30, v28, s25, v30\n" \
"v_dot2_f32_bf16 v31, v28, v28, v31\n" \
"v_dot2_f32_bf16 v30, v29, s25, v30\n" \
"v_dot2_f32_bf16 v31, v29, v29, v31\n" \
"s_nop 2\n" \
"v_permlane16_swap_b32 v30, v31\n" \
"v_add_f32 v30, v30, v31\n" \
"v_mov_b32 v31, v30\n" \
"s_nop 1\n" \
"v_permlane32_swap_b32 v30, v31\n" \
"v_add_f32 v30, v30, v31\n" \
"v_and_b32 v30, -2, v30\n" \
"buffer_store_dword v30, v5, %0, s51 offen sc1\n" \
"s_sub_u32 s53, s53, 12288\n" \
"s_sub_u32 s54, s54, 12288\n" \
"s_mov_b32 s24, 1\n" \
"s_branch .Lcloop_%=\n" \
".Lc123_%=:\n" \
"buffer_store_dwordx4 v[16:19], v224, %1, s58 offen\n" \
"s_branch .Lend_%=\n" \
".Lloader_%=:\n" \
"s_lshr_b32 s26, %3, 3\n" \
"s_lshl_b32 s26, s26, 2\n" \
"s_and_b32 s28, %3, 3\n" \
"s_add_u32 s26, s26, s28\n" \
"s_mul_i32 s34, %3, 153600\n" \
"s_add_u32 s34, s34, 0\n" \
"s_mul_i32 s35, s26, 102400\n" \
"s_add_u32 s35, s35, 2457600\n" \
"s_mul_i32 s36, %3, 286720\n" \
"s_add_u32 s36, s36, 3276800\n" \
"s_mul_i32 s37, s26, 276480\n" \
"s_add_u32 s37, s37, 7864320\n" \
"s_bfe_i32 s38, s27, 0x10001\n" \
"s_and_b32 s39, s27, 1\n" \
"s_mul_i32 s39, s39, 5120\n" \
"s_mul_i32 s40, s27, 2560\n" \
"v_add_u32 v1, s39, v0\n" \
"v_add_u32 v2, 4096, v1\n" \
"s_mul_i32 s50, %3, 256\n" \
"s_mul_i32 s25, %2, 16\n" \
"s_add_u32 s50, s50, s25\n" \
"s_mul_i32 s25, s27, 4\n" \
"s_add_u32 s50, s50, s25\n" \
"s_add_u32 s50, s50, 27774976\n" \
"s_mul_i32 s28, %3, 5120\n" \
"s_add_u32 s28, s28, 12828672\n" \
"s_mul_i32 s44, %2, 327680\n" \
"s_mul_i32 s25, s27, 81920\n" \
"s_add_u32 s44, s44, s25\n" \
"s_mul_i32 s25, %3, 5120\n" \
"s_add_u32 s44, s44, s25\n" \
"s_add_u32 s44, s44, 12173312\n" \
"s_cmp_ge_u32 %2, 2\n" \
"s_cselect_b32 s44, s44, s28\n" \
"s_cselect_b32 s48, -1, 0\n" \
"s_add_u32 s46, s44, 3072\n" \
"s_mul_i32 s45, %2, 327680\n" \
"s_mul_i32 s25, s27, 81920\n" \
"s_add_u32 s45, s45, s25\n" \
"s_mul_i32 s25, %3, 5120\n" \
"s_add_u32 s45, s45, s25\n" \
"s_add_u32 s45, s45, 12500992\n" \
"s_cmp_ge_u32 %2, 1\n" \
"s_cselect_b32 s45, s45, s28\n" \
"s_cselect_b32 s49, -1, 0\n" \
"s_add_u32 s47, s45, 3072\n" \
"s_bfe_u32 s28, %3, 0x10002\n" \
"s_lshr_b32 s29, s27, 1\n" \
"s_mul_i32 s26, %2, 442368\n" \
"s_mul_i32 s25, s28, 221184\n" \
"s_add_u32 s26, s26, s25\n" \
"s_mul_i32 s25, s27, 27648\n" \
"s_add_u32 s26, s26, s25\n" \
"s_mul_i32 s25, s29, -54272\n" \
"s_add_u32 s26, s26, s25\n" \
"s_add_u32 s26, s26, 20692992\n" \
"s_mul_i32 s55, s27, 8\n" \
"s_mul_i32 s25, s29, 1008\n" \
"s_add_u32 s55, s55, s25\n" \
"s_add_u32 s55, s55, 0\n" \
"s_xor_b32 s29, s27, 0\n" \
"s_mul_i32 s57, s29, 10240\n" \
"s_add_u32 s57, s57, s55\n" \
"s_mul_i32 s51, s29, 55296\n" \
"s_add_u32 s51, s51, s26\n" \
"s_xor_b32 s29, s27, 1\n" \
"s_mul_i32 s58, s29, 10240\n" \
"s_add_u32 s58, s58, s55\n" \
"s_mul_i32 s52, s29, 55296\n" \
"s_add_u32 s52, s52, s26\n" \
"s_xor_b32 s29, s27, 2\n" \
"s_mul_i32 s59, s29, 10240\n" \
"s_add_u32 s59, s59, s55\n" \
"s_mul_i32 s53, s29, 55296\n" \
"s_add_u32 s53, s53, s26\n" \
"s_xor_b32 s29, s27, 3\n" \
"s_mul_i32 s60, s29, 10240\n" \
"s_add_u32 s60, s60, s55\n" \
"s_mul_i32 s54, s29, 55296\n" \
"s_add_u32 s54, s54, s26\n" \
"s_mul_i32 s26, %3, 128\n" \
"s_add_u32 s26, s26, 27779072\n" \
"v_and_b32 v30, 0xf0, v0\n" \
"v_lshlrev_b32 v30, 7, v30\n" \
"buffer_load_dword v29, v30, %0, s26 offen sc1\n" \
"s_waitcnt vmcnt(0)\n" \
"v_cmp_ne_u32 vcc, %5, v29\n" \
"s_cbranch_vccnz .Ll1_%=\n" \
".Ll2_%=:\n" \
"s_and_b32 s25, s38, 4294916096\n" \
"s_add_u32 s25, s25, s39\n" \
"s_add_u32 m0, s25, 143360\n" \
"s_and_b32 s25, s38, 81920\n" \
"s_add_u32 s25, s25, s34\n" \
"s_add_u32 s26, s25, 0\n" \
"buffer_load_dwordx4 v1, %0, s26 offen offset:0 sc1 lds\n" \
"buffer_load_dwordx4 v1, %0, s26 offen offset:1024 sc1 lds\n" \
"buffer_load_dwordx4 v1, %0, s26 offen offset:2048 sc1 lds\n" \
"buffer_load_dwordx4 v1, %0, s26 offen offset:3072 sc1 lds\n" \
"s_add_u32 m0, m0, 4096\n" \
"s_nop 0\n" \
"buffer_load_dwordx4 v2, %0, s26 offen sc1 lds\n" \
"s_and_b32 s25, s38, 81920\n" \
"s_add_u32 s25, s25, s39\n" \
"s_add_u32 m0, s25, 0\n" \
"s_and_b32 s25, s38, 4294864896\n" \
"s_add_u32 s25, s25, s34\n" \
"s_add_u32 s26, s25, 112640\n" \
"buffer_load_dwordx4 v1, %0, s26 offen offset:0 sc1 lds\n" \
"buffer_load_dwordx4 v1, %0, s26 offen offset:1024 sc1 lds\n" \
"buffer_load_dwordx4 v1, %0, s26 offen offset:2048 sc1 lds\n" \
"buffer_load_dwordx4 v1, %0, s26 offen offset:3072 sc1 lds\n" \
"s_add_u32 m0, m0, 4096\n" \
"s_nop 0\n" \
"buffer_load_dwordx4 v2, %0, s26 offen sc1 lds\n" \
"s_and_b32 s25, s38, 92160\n" \
"s_add_u32 s25, s25, s39\n" \
"s_add_u32 m0, s25, 10240\n" \
"s_and_b32 s25, s38, 4294936576\n" \
"s_add_u32 s25, s25, s34\n" \
"s_add_u32 s26, s25, 122880\n" \
"buffer_load_dwordx4 v1, %0, s26 offen offset:0 sc1 lds\n" \
"buffer_load_dwordx4 v1, %0, s26 offen offset:1024 sc1 lds\n" \
"buffer_load_dwordx4 v1, %0, s26 offen offset:2048 sc1 lds\n" \
"buffer_load_dwordx4 v1, %0, s26 offen offset:3072 sc1 lds\n" \
"s_add_u32 m0, m0, 4096\n" \
"s_nop 0\n" \
"buffer_load_dwordx4 v2, %0, s26 offen sc1 lds\n" \
"s_and_b32 s25, s38, 4294957056\n" \
"s_add_u32 s25, s25, s39\n" \
"s_add_u32 m0, s25, 122880\n" \
"s_and_b32 s25, s38, 81920\n" \
"s_add_u32 s25, s25, s34\n" \
"s_add_u32 s26, s25, 20480\n" \
"buffer_load_dwordx4 v1, %0, s26 offen offset:0 sc1 lds\n" \
"buffer_load_dwordx4 v1, %0, s26 offen offset:1024 sc1 lds\n" \
"buffer_load_dwordx4 v1, %0, s26 offen offset:2048 sc1 lds\n" \
"buffer_load_dwordx4 v1, %0, s26 offen offset:3072 sc1 lds\n" \
"s_add_u32 m0, m0, 4096\n" \
"s_nop 0\n" \
"buffer_load_dwordx4 v2, %0, s26 offen sc1 lds\n" \
"s_mul_i32 s26, %3, 8192\n" \
"s_mul_i32 s25, s27, 128\n" \
"s_add_u32 s26, s26, s25\n" \
"s_add_u32 s26, s26, 27975680\n" \
"v_bfe_u32 v30, v0, 2, 7\n" \
"v_lshrrev_b32 v29, 9, v0\n" \
"v_lshl_add_u32 v29, v29, 12, v30\n" \
"buffer_load_dword v56, v29, %0, s26 offen offset:0 sc1\n" \
"buffer_load_dword v57, v29, %0, s26 offen offset:512 sc1\n" \
"buffer_load_dword v58, v29, %0, s26 offen offset:1024 sc1\n" \
"buffer_load_dword v59, v29, %0, s26 offen offset:1536 sc1\n" \
"buffer_load_dword v60, v29, %0, s26 offen offset:2048 sc1\n" \
"buffer_load_dword v61, v29, %0, s26 offen offset:2560 sc1\n" \
"buffer_load_dword v62, v29, %0, s26 offen offset:3072 sc1\n" \
"buffer_load_dword v63, v29, %0, s26 offen offset:3584 sc1\n" \
"v_bfe_u32 v23, v0, 4, 1\n" \
"v_sub_u32 v23, %2, v23\n" \
"v_add_u32 v23, -1, v23\n" \
"v_max_i32 v23, 0, v23\n" \
"v_lshlrev_b32 v23, 4, v23\n" \
"v_add_lshl_u32 v23, v23, %3, 7\n" \
"v_and_b32 v22, 0xf0, v0\n" \
"s_lshl_b32 s26, %2, 11\n" \
"v_lshl_add_u32 v22, v22, 3, s26\n" \
"v_cndmask_b32 v22, v22, v23, s[30:31]\n" \
"s_mov_b32 s62, 27844608\n" \
"s_waitcnt vmcnt(18)\n" \
"s_barrier\n" \
"s_mul_i32 s26, %2, 2048\n" \
"s_mul_i32 s25, %3, 128\n" \
"s_add_u32 s26, s26, s25\n" \
"s_add_u32 s26, s26, 27844608\n" \
"s_cmp_lg_u32 s27, 1\n" \
"s_cbranch_scc1 .Lna_%=\n" \
"v_mov_b32 v29, %5\n" \
"s_mov_b64 exec, 1\n" \
"buffer_store_dword v29, off, %0, s26 sc1\n" \
"s_mov_b64 exec, -1\n" \
".Lna_%=:\n" \
"s_and_b32 s25, s38, 4294854656\n" \
"s_add_u32 s25, s25, s39\n" \
"s_add_u32 m0, s25, 133120\n" \
"s_and_b32 s25, s38, 102400\n" \
"s_add_u32 s25, s25, s34\n" \
"s_add_u32 s26, s25, 30720\n" \
"buffer_load_dwordx4 v1, %0, s26 offen offset:0 sc1 lds\n" \
"buffer_load_dwordx4 v1, %0, s26 offen offset:1024 sc1 lds\n" \
"buffer_load_dwordx4 v1, %0, s26 offen offset:2048 sc1 lds\n" \
"buffer_load_dwordx4 v1, %0, s26 offen offset:3072 sc1 lds\n" \
"s_add_u32 m0, m0, 4096\n" \
"s_nop 0\n" \
"buffer_load_dwordx4 v2, %0, s26 offen sc1 lds\n" \
"s_add_u32 m0, s40, 30720\n" \
"s_add_u32 s26, s40, s34\n" \
"s_add_u32 s26, s26, 143360\n" \
"buffer_load_dwordx4 v0, %0, s26 offen sc1 lds\n" \
"buffer_load_dwordx4 v0, %0, s26 offen offset:1024 sc1 lds\n" \
"s_mov_b32 exec_hi, 0\n" \
"buffer_load_dwordx4 v0, %0, s26 offen offset:2048 sc1 lds\n" \
"s_mov_b32 exec_hi, -1\n" \
"ds_read_b64_tr_b16 v[64:65], v17 offset:0\n" \
"ds_read_b64_tr_b16 v[66:67], v17 offset:2048\n" \
"ds_read_b64_tr_b16 v[68:69], v17 offset:4096\n" \
"ds_read_b64_tr_b16 v[70:71], v17 offset:6144\n" \
"ds_read_b64_tr_b16 v[72:73], v17 offset:8192\n" \
"ds_read_b64_tr_b16 v[74:75], v17 offset:10240\n" \
"ds_read_b64_tr_b16 v[76:77], v17 offset:12288\n" \
"ds_read_b64_tr_b16 v[78:79], v17 offset:14336\n" \
"ds_read_b64_tr_b16 v[80:81], v17 offset:16384\n" \
"ds_read_b64_tr_b16 v[82:83], v17 offset:18432\n" \
"ds_read_b64_tr_b16 v[128:129], v16 offset:0\n" \
"ds_read_b64_tr_b16 v[130:131], v16 offset:1024\n" \
"ds_read_b64_tr_b16 v[132:133], v16 offset:2048\n" \
"ds_read_b64_tr_b16 v[134:135], v16 offset:3072\n" \
"ds_read_b64_tr_b16 v[136:137], v16 offset:4096\n" \
"ds_read_b64_tr_b16 v[138:139], v16 offset:5120\n" \
"ds_read_b64_tr_b16 v[140:141], v16 offset:6144\n" \
"ds_read_b64_tr_b16 v[142:143], v16 offset:7168\n" \
"ds_read_b64_tr_b16 v[144:145], v16 offset:8192\n" \
"ds_read_b64_tr_b16 v[146:147], v16 offset:9216\n" \
"s_mov_b32 s25, 0x3f803f80\n" \
"s_waitcnt lgkmcnt(9)\n" \
"v_dot2_f32_bf16 v86, v128, s25, 0\n" \
"v_dot2_f32_bf16 v86, v129, s25, v86\n" \
"s_waitcnt lgkmcnt(8)\n" \
"v_dot2_f32_bf16 v86, v130, s25, v86\n" \
"v_dot2_f32_bf16 v86, v131, s25, v86\n" \
"s_waitcnt lgkmcnt(7)\n" \
"v_dot2_f32_bf16 v86, v132, s25, v86\n" \
"v_dot2_f32_bf16 v86, v133, s25, v86\n" \
"s_waitcnt lgkmcnt(6)\n" \
"v_dot2_f32_bf16 v86, v134, s25, v86\n" \
"v_dot2_f32_bf16 v86, v135, s25, v86\n" \
"s_waitcnt lgkmcnt(5)\n" \
"v_dot2_f32_bf16 v86, v136, s25, v86\n" \
"v_dot2_f32_bf16 v86, v137, s25, v86\n" \
"s_waitcnt lgkmcnt(4)\n" \
"v_dot2_f32_bf16 v86, v138, s25, v86\n" \
"v_dot2_f32_bf16 v86, v139, s25, v86\n" \
"s_waitcnt lgkmcnt(3)\n" \
"v_dot2_f32_bf16 v86, v140, s25, v86\n" \
"v_dot2_f32_bf16 v86, v141, s25, v86\n" \
"s_waitcnt lgkmcnt(2)\n" \
"v_dot2_f32_bf16 v86, v142, s25, v86\n" \
"v_dot2_f32_bf16 v86, v143, s25, v86\n" \
"s_waitcnt lgkmcnt(1)\n" \
"v_dot2_f32_bf16 v86, v144, s25, v86\n" \
"v_dot2_f32_bf16 v86, v145, s25, v86\n" \
"s_waitcnt lgkmcnt(0)\n" \
"v_dot2_f32_bf16 v86, v146, s25, v86\n" \
"v_dot2_f32_bf16 v86, v147, s25, v86\n" \
"buffer_store_dwordx4 v[80:83], v87, %0, 0 offen sc1\n" \
"buffer_store_dwordx2 v[76:77], v88, %0, 0 offen sc1\n" \
"buffer_store_dwordx2 v[78:79], v89, %0, 0 offen sc1\n" \
"buffer_store_dwordx4 v[128:131], v52, %0, 0 offen sc1\n" \
"buffer_store_dwordx4 v[132:135], v53, %0, 0 offen sc1\n" \
"buffer_store_dwordx4 v[136:139], v54, %0, 0 offen sc1\n" \
"buffer_store_dwordx4 v[140:143], v55, %0, 0 offen sc1\n" \
"buffer_store_dwordx4 v[144:147], v84, %0, 0 offen sc1\n" \
"buffer_store_dword v86, v85, %0, 0 offen sc1\n" \
"buffer_store_dwordx4 v[76:79], v90, %0, 0 offen sc1\n" \
"buffer_store_dwordx4 v[64:67], v91, %0, 0 offen sc1\n" \
"buffer_store_dwordx4 v[68:71], v92, %0, 0 offen sc1\n" \
"buffer_store_dwordx4 v[72:75], v93, %0, 0 offen sc1\n" \
"s_waitcnt vmcnt(34)\n" \
"s_barrier\n" \
"s_waitcnt vmcnt(27)\n" \
"v_add_f32 v56, v56, v57\n" \
"s_waitcnt vmcnt(26)\n" \
"v_add_f32 v56, v56, v58\n" \
"s_waitcnt vmcnt(25)\n" \
"v_add_f32 v56, v56, v59\n" \
"s_waitcnt vmcnt(24)\n" \
"v_add_f32 v56, v56, v60\n" \
"s_waitcnt vmcnt(23)\n" \
"v_add_f32 v56, v56, v61\n" \
"s_waitcnt vmcnt(22)\n" \
"v_add_f32 v56, v56, v62\n" \
"s_waitcnt vmcnt(21)\n" \
"v_add_f32 v56, v56, v63\n" \
"v_mov_b32 v57, v56\n" \
"s_nop 1\n" \
"v_permlane32_swap_b32 v56, v57\n" \
"v_add_f32 v56, v56, v57\n" \
"s_lshl_b32 s26, s27, 7\n" \
"s_add_u32 s26, s26, 160512\n" \
"v_add_u32 v30, s26, v30\n" \
"ds_write_b32 v30, v56\n" \
"s_waitcnt vmcnt(0)\n" \
"s_waitcnt lgkmcnt(0)\n" \
"s_barrier\n" \
"s_mul_i32 s26, %2, 2048\n" \
"s_mul_i32 s25, %3, 128\n" \
"s_add_u32 s26, s26, s25\n" \
"s_add_u32 s26, s26, 27811840\n" \
"s_cmp_lg_u32 s27, 0\n" \
"s_cbranch_scc1 .Lnp_%=\n" \
"v_mov_b32 v29, %5\n" \
"s_mov_b64 exec, 1\n" \
"buffer_store_dword v29, off, %0, s26 sc1\n" \
"s_mov_b64 exec, -1\n" \
".Lnp_%=:\n" \
"buffer_load_dword v23, v22, %0, s62 offen sc1\n" \
"s_and_b32 s25, s38, 10240\n" \
"s_add_u32 s25, s25, s39\n" \
"s_add_u32 m0, s25, 40960\n" \
"s_and_b32 s25, s38, 10240\n" \
"s_add_u32 s25, s25, s34\n" \
"s_add_u32 s26, s25, 40960\n" \
"buffer_load_dwordx4 v1, %0, s26 offen offset:0 sc1 lds\n" \
"buffer_load_dwordx4 v1, %0, s26 offen offset:1024 sc1 lds\n" \
"buffer_load_dwordx4 v1, %0, s26 offen offset:2048 sc1 lds\n" \
"buffer_load_dwordx4 v1, %0, s26 offen offset:3072 sc1 lds\n" \
"s_add_u32 m0, m0, 4096\n" \
"s_nop 0\n" \
"buffer_load_dwordx4 v2, %0, s26 offen sc1 lds\n" \
"s_and_b32 s25, s38, 10240\n" \
"s_add_u32 s25, s25, s39\n" \
"s_add_u32 m0, s25, 61440\n" \
"s_and_b32 s25, s38, 10240\n" \
"s_add_u32 s25, s25, s34\n" \
"s_add_u32 s26, s25, 61440\n" \
"buffer_load_dwordx4 v1, %0, s26 offen offset:0 sc1 lds\n" \
"buffer_load_dwordx4 v1, %0, s26 offen offset:1024 sc1 lds\n" \
"buffer_load_dwordx4 v1, %0, s26 offen offset:2048 sc1 lds\n" \
"buffer_load_dwordx4 v1, %0, s26 offen offset:3072 sc1 lds\n" \
"s_add_u32 m0, m0, 4096\n" \
"s_nop 0\n" \
"buffer_load_dwordx4 v2, %0, s26 offen sc1 lds\n" \
"v_mov_b32 v24, -1\n" \
"v_mov_b32 v25, -1\n" \
"v_mov_b32 v26, -1\n" \
"v_mov_b32 v27, -1\n" \
"s_mul_i32 s26, %2, 442368\n" \
"s_mul_i32 s25, %3, 27648\n" \
"s_add_u32 s26, s26, s25\n" \
"s_mul_i32 s25, s27, 6144\n" \
"s_add_u32 s26, s26, s25\n" \
"s_add_u32 s26, s26, 20692992\n" \
"buffer_store_dwordx4 v[24:27], v0, %0, s26 offen offset:0 sc1\n" \
"buffer_store_dwordx4 v[24:27], v0, %0, s26 offen offset:1024 sc1\n" \
"buffer_store_dwordx4 v[24:27], v0, %0, s26 offen offset:2048 sc1\n" \
"buffer_store_dwordx4 v[24:27], v0, %0, s26 offen offset:3072 sc1\n" \
"s_add_u32 s26, s26, 4096\n" \
"buffer_store_dwordx4 v[24:27], v0, %0, s26 offen offset:0 sc1\n" \
"buffer_store_dwordx4 v[24:27], v0, %0, s26 offen offset:1024 sc1\n" \
"v_bfe_u32 v28, v0, 4, 5\n" \
"v_add_u32 v8, 20, v28\n" \
"v_add_u32 v29, -16, v28\n" \
"v_cmp_gt_u32 vcc, 16, v28\n" \
"s_nop 1\n" \
"v_cndmask_b32 v8, v29, v8, vcc\n" \
"v_cmp_gt_u32 vcc, 20, v28\n" \
"s_nop 1\n" \
"v_cndmask_b32 v8, 40, v8, vcc\n" \
"v_subrev_u32 v29, 12, v29\n" \
"v_cmp_gt_u32 vcc, 28, v28\n" \
"s_nop 1\n" \
"v_cndmask_b32 v8, v29, v8, vcc\n" \
"v_lshrrev_b32 v28, 9, v0\n" \
"v_mad_u32_u24 v8, v28, 41, v8\n" \
"v_lshl_add_u32 v8, v8, 4, s23\n" \
"v_and_b32 v10, 0xf0, v0\n" \
"v_lshl_add_u32 v9, v28, 8, v10\n" \
"v_lshl_add_u32 v10, v28, 31, v10\n" \
"v_bfe_u32 v28, v0, 8, 1\n" \
"v_lshl_add_u32 v9, v28, 14, v9\n" \
"v_lshl_add_u32 v10, v28, 12, v10\n" \
"v_add_u32 v46, s67, v0\n" \
"v_add_u32 v6, 1024, v46\n" \
"v_lshrrev_b32 v7, 1, v0\n" \
"v_add_u32 v7, s67, v7\n" \
"v_add_u32 v11, s66, v0\n" \
"v_bfe_u32 v12, v0, 1, 8\n" \
"v_add_u32 v12, s66, v12\n" \
"v_add_u32 v12, 1024, v12\n" \
"v_lshrrev_b32 v29, 9, v0\n" \
"v_mul_u32_u24 v29, 144, v29\n" \
"v_add3_u32 v3, v29, v0, s23\n" \
"v_lshrrev_b32 v29, 4, v0\n" \
"v_min_u32 v29, 47, v29\n" \
"v_mul_u32_u24 v31, 656, v29\n" \
"v_add_u32 v31, 123520, v31\n" \
"s_lshl_b32 s26, %2, 4\n" \
"v_add_u32 v29, s26, v29\n" \
"v_cmp_lt_u32 vcc, 31, v29\n" \
"v_mov_b32 v29, 0x3f803f80\n" \
"s_nop 0\n" \
"v_cndmask_b32 v40, 0, v29, vcc\n" \
"v_mov_b32 v41, v40\n" \
"v_mov_b32 v42, v40\n" \
"v_mov_b32 v43, v40\n" \
"v_bfe_u32 v47, v0, 8, 1\n" \
"v_bfe_u32 v29, v0, 6, 1\n" \
"v_lshl_add_u32 v47, v47, 1, v29\n" \
"v_mul_u32_u24 v47, 656, v47\n" \
"v_bfe_u32 v29, v0, 3, 3\n" \
"v_bfe_u32 v30, v0, 7, 1\n" \
"v_lshl_add_u32 v29, v30, 3, v29\n" \
"v_add3_u32 v47, v47, v29, s23\n" \
"v_add_u32 v29, 576, v47\n" \
"v_cndmask_b32 v47, v47, v29, s[30:31]\n" \
"s_or_b32 s25, s48, 0x200\n" \
"s_andn2_b32 s26, 2147483648, s48\n" \
"v_and_b32 v44, s25, v0\n" \
"v_or_b32 v13, s26, v0\n" \
"s_or_b32 s25, s49, 0x200\n" \
"s_andn2_b32 s26, 2147483648, s49\n" \
"v_and_b32 v45, s25, v0\n" \
"v_or_b32 v14, s26, v0\n" \
"s_barrier\n" \
"s_waitcnt vmcnt(11)\n" \
"s_barrier\n" \
"ds_write_b128 v31, v[40:43]\n" \
"v_cmp_ne_u32 vcc, %5, v23\n" \
"s_cbranch_vccnz .Ll3_%=\n" \
".Ll4_%=:\n" \
"v_bfe_u32 v29, v0, 8, 1\n" \
"v_lshlrev_b32 v29, 2, v29\n" \
"v_xor_b32 v29, %3, v29\n" \
"v_and_b32 v30, 0xf0, v0\n" \
"v_add_lshl_u32 v30, v29, v30, 7\n" \
"s_mov_b32 s61, 27811840\n" \
"s_sleep 12\n" \
"buffer_load_dword v31, v30, %0, s61 offen sc1\n" \
"s_waitcnt vmcnt(7) lgkmcnt(0)\n" \
"s_barrier\n" \
"s_waitcnt vmcnt(0)\n" \
"v_cmp_ne_u32 vcc, %5, v31\n" \
"s_cbranch_vccnz .Ll5_%=\n" \
".Ll6_%=:\n" \
"s_and_b32 s25, s38, 4294885376\n" \
"s_add_u32 s25, s25, s39\n" \
"s_add_u32 m0, s25, 81920\n" \
"s_and_b32 s25, s38, 10240\n" \
"s_add_u32 s25, s25, s35\n" \
"s_add_u32 s26, s25, 0\n" \
"buffer_load_dwordx4 v1, %0, s26 offen offset:0 sc1 lds\n" \
"buffer_load_dwordx4 v1, %0, s26 offen offset:1024 sc1 lds\n" \
"buffer_load_dwordx4 v1, %0, s26 offen offset:2048 sc1 lds\n" \
"buffer_load_dwordx4 v1, %0, s26 offen offset:3072 sc1 lds\n" \
"s_add_u32 m0, m0, 4096\n" \
"s_nop 0\n" \
"buffer_load_dwordx4 v2, %0, s26 offen sc1 lds\n" \
"s_and_b32 s25, s38, 10240\n" \
"s_add_u32 s25, s25, s39\n" \
"s_add_u32 m0, s25, 10240\n" \
"s_and_b32 s25, s38, 10240\n" \
"s_add_u32 s25, s25, s35\n" \
"s_add_u32 s26, s25, 20480\n" \
"buffer_load_dwordx4 v1, %0, s26 offen offset:0 sc1 lds\n" \
"buffer_load_dwordx4 v1, %0, s26 offen offset:1024 sc1 lds\n" \
"buffer_load_dwordx4 v1, %0, s26 offen offset:2048 sc1 lds\n" \
"buffer_load_dwordx4 v1, %0, s26 offen offset:3072 sc1 lds\n" \
"s_add_u32 m0, m0, 4096\n" \
"s_nop 0\n" \
"buffer_load_dwordx4 v2, %0, s26 offen sc1 lds\n" \
"s_add_u32 m0, s40, 30720\n" \
"s_add_u32 s26, s40, s35\n" \
"s_add_u32 s26, s26, 40960\n" \
"buffer_load_dwordx4 v0, %0, s26 offen sc1 lds\n" \
"buffer_load_dwordx4 v0, %0, s26 offen offset:1024 sc1 lds\n" \
"s_mov_b32 exec_hi, 0\n" \
"buffer_load_dwordx4 v0, %0, s26 offen offset:2048 sc1 lds\n" \
"s_mov_b32 exec_hi, -1\n" \
"s_sleep 4\n" \
".p2align 6\n" \
".Llloop_%=:\n" \
"s_nop 1\n" \
"buffer_load_dwordx4 v[96:99], v44, %0, s44 offen offset:0 sc1\n" \
"buffer_load_dwordx4 v[100:103], v44, %0, s44 offen offset:1024 sc1\n" \
"buffer_load_dwordx4 v[104:107], v44, %0, s44 offen offset:2048 sc1\n" \
"buffer_load_dwordx4 v[108:111], v45, %0, s45 offen offset:0 sc1\n" \
"buffer_load_dwordx4 v[112:115], v45, %0, s45 offen offset:1024 sc1\n" \
"buffer_load_dwordx4 v[116:119], v45, %0, s45 offen offset:2048 sc1\n" \
"v_mov_b32 v27, 0xffff\n" \
"v_cndmask_b32 v88, 0, v27, s[68:69]\n" \
"v_cndmask_b32 v89, 0, v27, s[72:73]\n" \
"v_cndmask_b32 v90, 0, v27, s[76:77]\n" \
"v_cndmask_b32 v91, 0, v27, s[80:81]\n" \
"v_cndmask_b32 v92, 0, v27, s[84:85]\n" \
"v_cndmask_b32 v93, 0, v27, s[88:89]\n" \
"v_cndmask_b32 v94, 0, v27, s[92:93]\n" \
"v_cndmask_b32 v95, 0, v27, s[96:97]\n" \
"v_cndmask_b32 v88, v88, -1, s[70:71]\n" \
"v_cndmask_b32 v89, v89, -1, s[74:75]\n" \
"v_cndmask_b32 v90, v90, -1, s[78:79]\n" \
"v_cndmask_b32 v91, v91, -1, s[82:83]\n" \
"v_cndmask_b32 v92, v92, -1, s[86:87]\n" \
"v_cndmask_b32 v93, v93, -1, s[90:91]\n" \
"v_cndmask_b32 v94, v94, -1, s[94:95]\n" \
"v_cndmask_b32 v95, v95, -1, s[98:99]\n" \
"s_waitcnt vmcnt(4)\n" \
"v_bitop3_b32 v29, v96, v100, s24 bitop3:0x7e\n" \
"s_waitcnt vmcnt(3)\n" \
"v_bitop3_b32 v29, v29, v104, s24 bitop3:0xf6\n" \
"s_waitcnt vmcnt(2)\n" \
"v_bitop3_b32 v29, v29, v108, s24 bitop3:0xf6\n" \
"s_waitcnt vmcnt(1)\n" \
"v_bitop3_b32 v29, v29, v112, s24 bitop3:0xf6\n" \
"s_waitcnt vmcnt(0)\n" \
"v_bitop3_b32 v29, v29, v116, s24 bitop3:0xf6\n" \
"v_and_b32 v29, 1, v29\n" \
"v_cmp_ne_u32 vcc, 0, v29\n" \
"s_cbranch_vccnz .Ll501_%=\n" \
".Ll502_%=:\n" \
"ds_write_b128 v46, v[96:99] offset:0\n" \
"ds_write_b128 v46, v[100:103] offset:1024\n" \
"ds_write_b64 v7, v[104:105] offset:2048\n" \
"v_and_b32 v106, s48, v106\n" \
"v_and_b32 v107, s48, v107\n" \
"ds_write_b128 v46, v[108:111] offset:10240\n" \
"ds_write_b128 v46, v[112:115] offset:11264\n" \
"ds_write_b64 v7, v[116:117] offset:12288\n" \
"v_and_b32 v118, s49, v118\n" \
"v_and_b32 v119, s49, v119\n" \
"ds_write_b16 v47, v106 offset:0\n" \
"ds_write_b16_d16_hi v47, v106 offset:16\n" \
"ds_write_b16 v47, v107 offset:32\n" \
"ds_write_b16_d16_hi v47, v107 offset:48\n" \
"ds_write_b16 v47, v118 offset:10496\n" \
"ds_write_b16_d16_hi v47, v118 offset:10512\n" \
"ds_write_b16 v47, v119 offset:10528\n" \
"ds_write_b16_d16_hi v47, v119 offset:10544\n" \
"s_waitcnt lgkmcnt(0)\n" \
"s_barrier\n" \
"s_setprio 1\n" \
"ds_read_b128 v[16:19], v11\n" \
"ds_read_b64 v[20:21], v12\n" \
"ds_read_b128 v[56:59], v6 offset:20480\n" \
"ds_read_b64 v[60:61], v7 offset:22528\n" \
"ds_read_b128 v[62:65], v6 offset:0\n" \
"ds_read_b64 v[66:67], v7 offset:2048\n" \
"ds_read_b128 v[68:71], v6 offset:2560\n" \
"ds_read_b64 v[72:73], v7 offset:4608\n" \
"s_waitcnt lgkmcnt(6)\n" \
"v_cndmask_b32 v20, v20, 0, s[30:31]\n" \
"v_cndmask_b32 v21, v21, 0, s[30:31]\n" \
"v_mov_b32 v22, 0xbf00bf00\n" \
"v_mov_b32 v23, 0xbf00bf00\n" \
"s_waitcnt lgkmcnt(5)\n" \
"v_mfma_f32_32x32x16_bf16 v[240:255], v[56:59], v[16:19], 0\n" \
"s_waitcnt lgkmcnt(4)\n" \
"v_mfma_f32_32x32x8_bf16 v[240:255], v[60:61], v[20:21], v[240:255]\n" \
"ds_read_b128 v[56:59], v6 offset:5120\n" \
"ds_read_b64 v[60:61], v7 offset:7168\n" \
"s_waitcnt lgkmcnt(5)\n" \
"v_mfma_f32_32x32x16_bf16 v[112:127], v[62:65], v[16:19], 0\n" \
"s_waitcnt lgkmcnt(4)\n" \
"v_mfma_f32_32x32x8_bf16 v[112:127], v[66:67], v[20:21], v[112:127]\n" \
"ds_read_b128 v[62:65], v6 offset:7680\n" \
"ds_read_b64 v[66:67], v7 offset:9728\n" \
"s_waitcnt lgkmcnt(5)\n" \
"v_mfma_f32_32x32x16_bf16 v[128:143], v[68:71], v[16:19], 0\n" \
"s_waitcnt lgkmcnt(4)\n" \
"v_mfma_f32_32x32x8_bf16 v[128:143], v[72:73], v[20:21], v[128:143]\n" \
"ds_read_b128 v[68:71], v6 offset:10240\n" \
"ds_read_b64 v[72:73], v7 offset:12288\n" \
"s_waitcnt lgkmcnt(5)\n" \
"v_mfma_f32_32x32x16_bf16 v[144:159], v[56:59], v[16:19], 0\n" \
"s_nop 1\n" \
"v_cndmask_b32 v112, v112, v240, s[68:69]\n" \
"v_cndmask_b32 v113, v113, v241, s[70:71]\n" \
"v_cndmask_b32 v114, v114, v242, s[72:73]\n" \
"v_cndmask_b32 v115, v115, v243, s[74:75]\n" \
"v_cndmask_b32 v116, v116, v244, s[76:77]\n" \
"v_cndmask_b32 v117, v117, v245, s[78:79]\n" \
"s_waitcnt lgkmcnt(4)\n" \
"v_mfma_f32_32x32x8_bf16 v[144:159], v[60:61], v[20:21], v[144:159]\n" \
"ds_read_b128 v[56:59], v6 offset:12800\n" \
"ds_read_b64 v[60:61], v7 offset:14848\n" \
"v_cndmask_b32 v118, v118, v246, s[80:81]\n" \
"v_cndmask_b32 v119, v119, v247, s[82:83]\n" \
"v_cndmask_b32 v120, v120, v248, s[84:85]\n" \
"v_cndmask_b32 v121, v121, v249, s[86:87]\n" \
"v_cndmask_b32 v122, v122, v250, s[88:89]\n" \
"v_cndmask_b32 v123, v123, v251, s[90:91]\n" \
"s_waitcnt lgkmcnt(5)\n" \
"v_mfma_f32_32x32x16_bf16 v[160:175], v[62:65], v[16:19], 0\n" \
"buffer_load_dwordx4 v[96:99], v13, %0, s46 offen offset:0 sc1\n" \
"v_cndmask_b32 v124, v124, v252, s[92:93]\n" \
"v_cndmask_b32 v125, v125, v253, s[94:95]\n" \
"v_cndmask_b32 v126, v126, v254, s[96:97]\n" \
"v_cndmask_b32 v127, v127, v255, s[98:99]\n" \
"s_waitcnt lgkmcnt(4)\n" \
"v_mfma_f32_32x32x8_bf16 v[160:175], v[66:67], v[20:21], v[160:175]\n" \
"ds_read_b128 v[62:65], v6 offset:15360\n" \
"ds_read_b64 v[66:67], v7 offset:17408\n" \
"buffer_load_dwordx4 v[100:103], v13, %0, s46 offen offset:1024 sc1\n" \
"v_max_f32 v26, v112, v113\n" \
"v_max_f32 v27, v114, v115\n" \
"v_max3_f32 v26, v116, v117, v26\n" \
"v_max3_f32 v27, v118, v119, v27\n" \
"s_waitcnt lgkmcnt(5)\n" \
"v_mfma_f32_32x32x16_bf16 v[176:191], v[68:71], v[16:19], 0\n" \
"buffer_load_dwordx4 v[104:107], v14, %0, s47 offen offset:0 sc1\n" \
"v_max3_f32 v26, v120, v121, v26\n" \
"v_max3_f32 v27, v122, v123, v27\n" \
"v_max3_f32 v26, v124, v125, v26\n" \
"v_max3_f32 v27, v126, v127, v27\n" \
"s_waitcnt lgkmcnt(4)\n" \
"v_mfma_f32_32x32x8_bf16 v[176:191], v[72:73], v[20:21], v[176:191]\n" \
"ds_read_b128 v[68:71], v6 offset:17920\n" \
"ds_read_b64 v[72:73], v7 offset:19968\n" \
"buffer_load_dwordx4 v[108:111], v14, %0, s47 offen offset:1024 sc1\n" \
"v_max3_f32 v26, v128, v129, v26\n" \
"v_max3_f32 v27, v130, v131, v27\n" \
"v_max3_f32 v26, v132, v133, v26\n" \
"s_waitcnt lgkmcnt(5)\n" \
"v_mfma_f32_32x32x16_bf16 v[192:207], v[56:59], v[16:19], 0\n" \
"v_max3_f32 v27, v134, v135, v27\n" \
"v_max3_f32 v26, v136, v137, v26\n" \
"v_max3_f32 v27, v138, v139, v27\n" \
"v_max3_f32 v26, v140, v141, v26\n" \
"v_max3_f32 v27, v142, v143, v27\n" \
"s_waitcnt lgkmcnt(4)\n" \
"v_mfma_f32_32x32x8_bf16 v[192:207], v[60:61], v[20:21], v[192:207]\n" \
"v_max3_f32 v26, v144, v145, v26\n" \
"v_max3_f32 v27, v146, v147, v27\n" \
"v_max3_f32 v26, v148, v149, v26\n" \
"v_max3_f32 v27, v150, v151, v27\n" \
"v_max3_f32 v26, v152, v153, v26\n" \
"s_waitcnt lgkmcnt(3)\n" \
"v_mfma_f32_32x32x16_bf16 v[208:223], v[62:65], v[16:19], 0\n" \
"v_max3_f32 v27, v154, v155, v27\n" \
"v_max3_f32 v26, v156, v157, v26\n" \
"v_max3_f32 v27, v158, v159, v27\n" \
"v_max3_f32 v26, v160, v161, v26\n" \
"v_max3_f32 v27, v162, v163, v27\n" \
"s_waitcnt lgkmcnt(2)\n" \
"v_mfma_f32_32x32x8_bf16 v[208:223], v[66:67], v[20:21], v[208:223]\n" \
"v_max3_f32 v26, v164, v165, v26\n" \
"v_max3_f32 v27, v166, v167, v27\n" \
"v_max3_f32 v26, v168, v169, v26\n" \
"v_max3_f32 v27, v170, v171, v27\n" \
"v_max3_f32 v26, v172, v173, v26\n" \
"s_waitcnt lgkmcnt(1)\n" \
"v_mfma_f32_32x32x16_bf16 v[224:239], v[68:71], v[16:19], 0\n" \
"v_max3_f32 v27, v174, v175, v27\n" \
"v_max3_f32 v26, v176, v177, v26\n" \
"v_max3_f32 v27, v178, v179, v27\n" \
"v_max3_f32 v26, v180, v181, v26\n" \
"v_max3_f32 v27, v182, v183, v27\n" \
"s_waitcnt lgkmcnt(0)\n" \
"v_mfma_f32_32x32x8_bf16 v[224:239], v[72:73], v[20:21], v[224:239]\n" \
"v_max3_f32 v26, v184, v185, v26\n" \
"v_max3_f32 v27, v186, v187, v27\n" \
"v_max3_f32 v26, v188, v189, v26\n" \
"v_max3_f32 v27, v190, v191, v27\n" \
"v_max3_f32 v26, v192, v193, v26\n" \
"v_max3_f32 v27, v194, v195, v27\n" \
"v_max3_f32 v26, v196, v197, v26\n" \
"v_max3_f32 v27, v198, v199, v27\n" \
"v_max3_f32 v26, v200, v201, v26\n" \
"v_max3_f32 v27, v202, v203, v27\n" \
"v_max3_f32 v26, v204, v205, v26\n" \
"v_max3_f32 v27, v206, v207, v27\n" \
"v_max3_f32 v26, v208, v209, v26\n" \
"v_max3_f32 v27, v210, v211, v27\n" \
"v_max3_f32 v26, v212, v213, v26\n" \
"v_max3_f32 v27, v214, v215, v27\n" \
"v_max3_f32 v26, v216, v217, v26\n" \
"v_max3_f32 v27, v218, v219, v27\n" \
"v_max3_f32 v26, v220, v221, v26\n" \
"v_max3_f32 v27, v222, v223, v27\n" \
"v_max3_f32 v26, v224, v225, v26\n" \
"v_max3_f32 v27, v226, v227, v27\n" \
"v_max3_f32 v26, v228, v229, v26\n" \
"v_max3_f32 v27, v230, v231, v27\n" \
"v_max3_f32 v26, v232, v233, v26\n" \
"v_max3_f32 v27, v234, v235, v27\n" \
"v_max3_f32 v26, v236, v237, v26\n" \
"v_max3_f32 v27, v238, v239, v27\n" \
"v_max_f32 v26, v26, v27\n" \
"v_mov_b32 v27, v26\n" \
"s_nop 1\n" \
"v_permlane32_swap_b32 v26, v27\n" \
"v_max_f32 v26, v26, v27\n" \
"v_and_b32 v27, 0xffff0000, v26\n" \
"v_sub_f32 v28, v26, v27\n" \
"v_and_b32 v27, 0xffff0000, v28\n" \
"v_sub_f32 v25, v28, v27\n" \
"v_perm_b32 v24, v26, v28, s22\n" \
"s_nop 1\n" \
"v_mfma_f32_32x32x8_bf16 v[112:127], v[22:23], v[24:25], v[112:127]\n" \
"v_mfma_f32_32x32x8_bf16 v[128:143], v[22:23], v[24:25], v[128:143]\n" \
"s_nop 10\n" \
"v_exp_f32 v112, v112\n" \
"v_exp_f32 v113, v113\n" \
"v_mfma_f32_32x32x8_bf16 v[144:159], v[22:23], v[24:25], v[144:159]\n" \
"v_exp_f32 v114, v114\n" \
"v_cvt_pk_bf16_f32 v112, v112, v113\n" \
"v_exp_f32 v115, v115\n" \
"v_exp_f32 v116, v116\n" \
"v_cvt_pk_bf16_f32 v113, v114, v115\n" \
"v_exp_f32 v117, v117\n" \
"v_exp_f32 v118, v118\n" \
"v_cvt_pk_bf16_f32 v114, v116, v117\n" \
"v_exp_f32 v119, v119\n" \
"v_exp_f32 v120, v120\n" \
"v_cvt_pk_bf16_f32 v115, v118, v119\n" \
"v_exp_f32 v121, v121\n" \
"v_exp_f32 v122, v122\n" \
"v_cvt_pk_bf16_f32 v116, v120, v121\n" \
"v_exp_f32 v123, v123\n" \
"v_exp_f32 v124, v124\n" \
"v_cvt_pk_bf16_f32 v117, v122, v123\n" \
"v_exp_f32 v125, v125\n" \
"v_exp_f32 v126, v126\n" \
"v_cvt_pk_bf16_f32 v118, v124, v125\n" \
"v_exp_f32 v127, v127\n" \
"s_nop 0\n" \
"v_cvt_pk_bf16_f32 v119, v126, v127\n" \
"v_and_b32 v240, v112, v88\n" \
"v_and_b32 v241, v113, v89\n" \
"v_and_b32 v242, v114, v90\n" \
"v_and_b32 v243, v115, v91\n" \
"v_and_b32 v244, v116, v92\n" \
"v_and_b32 v245, v117, v93\n" \
"v_and_b32 v246, v118, v94\n" \
"v_and_b32 v247, v119, v95\n" \
"v_xor_b32 v112, v112, v240\n" \
"v_xor_b32 v113, v113, v241\n" \
"v_xor_b32 v114, v114, v242\n" \
"v_xor_b32 v115, v115, v243\n" \
"v_xor_b32 v116, v116, v244\n" \
"v_xor_b32 v117, v117, v245\n" \
"v_xor_b32 v118, v118, v246\n" \
"v_xor_b32 v119, v119, v247\n" \
"v_exp_f32 v128, v128\n" \
"v_exp_f32 v129, v129\n" \
"v_mfma_f32_32x32x8_bf16 v[160:175], v[22:23], v[24:25], v[160:175]\n" \
"v_exp_f32 v130, v130\n" \
"v_cvt_pk_bf16_f32 v128, v128, v129\n" \
"v_exp_f32 v131, v131\n" \
"v_exp_f32 v132, v132\n" \
"v_cvt_pk_bf16_f32 v129, v130, v131\n" \
"v_exp_f32 v133, v133\n" \
"v_exp_f32 v134, v134\n" \
"v_cvt_pk_bf16_f32 v130, v132, v133\n" \
"v_exp_f32 v135, v135\n" \
"v_exp_f32 v136, v136\n" \
"v_cvt_pk_bf16_f32 v131, v134, v135\n" \
"v_exp_f32 v137, v137\n" \
"v_exp_f32 v138, v138\n" \
"v_cvt_pk_bf16_f32 v132, v136, v137\n" \
"v_exp_f32 v139, v139\n" \
"v_exp_f32 v140, v140\n" \
"v_cvt_pk_bf16_f32 v133, v138, v139\n" \
"v_exp_f32 v141, v141\n" \
"v_exp_f32 v142, v142\n" \
"v_cvt_pk_bf16_f32 v134, v140, v141\n" \
"v_exp_f32 v143, v143\n" \
"v_exp_f32 v144, v144\n" \
"v_cvt_pk_bf16_f32 v135, v142, v143\n" \
"v_exp_f32 v145, v145\n" \
"v_mfma_f32_32x32x8_bf16 v[176:191], v[22:23], v[24:25], v[176:191]\n" \
"v_exp_f32 v146, v146\n" \
"v_cvt_pk_bf16_f32 v144, v144, v145\n" \
"v_exp_f32 v147, v147\n" \
"v_exp_f32 v148, v148\n" \
"v_cvt_pk_bf16_f32 v145, v146, v147\n" \
"v_exp_f32 v149, v149\n" \
"v_exp_f32 v150, v150\n" \
"v_cvt_pk_bf16_f32 v146, v148, v149\n" \
"v_exp_f32 v151, v151\n" \
"v_exp_f32 v152, v152\n" \
"v_cvt_pk_bf16_f32 v147, v150, v151\n" \
"v_exp_f32 v153, v153\n" \
"v_exp_f32 v154, v154\n" \
"v_cvt_pk_bf16_f32 v148, v152, v153\n" \
"v_exp_f32 v155, v155\n" \
"v_exp_f32 v156, v156\n" \
"v_cvt_pk_bf16_f32 v149, v154, v155\n" \
"v_exp_f32 v157, v157\n" \
"v_exp_f32 v158, v158\n" \
"v_cvt_pk_bf16_f32 v150, v156, v157\n" \
"v_exp_f32 v159, v159\n" \
"s_waitcnt vmcnt(2)\n" \
"v_bitop3_b32 v28, v96, v100, s24 bitop3:0x7e\n" \
"v_and_b32 v28, s48, v28\n" \
"s_waitcnt vmcnt(0)\n" \
"v_bitop3_b32 v29, v104, v108, s24 bitop3:0x7e\n" \
"v_and_b32 v29, s49, v29\n" \
"v_bitop3_b32 v29, v28, v29, 1 bitop3:168\n" \
"v_cmp_ne_u32 vcc, 0, v29\n" \
"s_cbranch_vccnz .Ll503_%=\n" \
".Ll504_%=:\n" \
"ds_write_b128 v3, v[96:99] offset:64\n" \
"ds_write_b128 v3, v[100:103] offset:1376\n" \
"ds_write_b128 v3, v[104:107] offset:10560\n" \
"ds_write_b128 v3, v[108:111] offset:11872\n" \
"s_waitcnt lgkmcnt(0)\n" \
"s_barrier\n" \
"ds_read_b128 v[56:59], v8 offset:0\n" \
"ds_read_b128 v[60:63], v8 offset:1312\n" \
"ds_read_b128 v[64:67], v8 offset:20992\n" \
"ds_read_b128 v[68:71], v8 offset:22304\n" \
"v_exp_f32 v160, v160\n" \
"v_cvt_pk_bf16_f32 v151, v158, v159\n" \
"v_exp_f32 v161, v161\n" \
"v_mfma_f32_32x32x8_bf16 v[192:207], v[22:23], v[24:25], v[192:207]\n" \
"v_exp_f32 v162, v162\n" \
"v_cvt_pk_bf16_f32 v160, v160, v161\n" \
"s_add_u32 m0, s40, 92160\n" \
"s_add_u32 s26, s40, s35\n" \
"s_add_u32 s26, s26, 92160\n" \
"buffer_load_dwordx4 v0, %0, s26 offen sc1 lds\n" \
"buffer_load_dwordx4 v0, %0, s26 offen offset:1024 sc1 lds\n" \
"s_mov_b32 exec_hi, 0\n" \
"buffer_load_dwordx4 v0, %0, s26 offen offset:2048 sc1 lds\n" \
"s_mov_b32 exec_hi, -1\n" \
"v_exp_f32 v163, v163\n" \
"s_waitcnt lgkmcnt(3)\n" \
"v_mfma_f32_32x32x16_bf16 v[72:87], v[56:59], v[112:115], 0\n" \
"ds_read_b128 v[56:59], v8 offset:2624\n" \
"v_exp_f32 v164, v164\n" \
"v_cvt_pk_bf16_f32 v161, v162, v163\n" \
"v_exp_f32 v165, v165\n" \
"v_exp_f32 v166, v166\n" \
"v_cvt_pk_bf16_f32 v162, v164, v165\n" \
"s_waitcnt lgkmcnt(3)\n" \
"v_mfma_f32_32x32x16_bf16 v[72:87], v[60:63], v[116:119], v[72:87]\n" \
"ds_read_b128 v[60:63], v8 offset:3936\n" \
"v_exp_f32 v167, v167\n" \
"v_exp_f32 v168, v168\n" \
"v_cvt_pk_bf16_f32 v163, v166, v167\n" \
"s_and_b32 s25, s38, 10240\n" \
"s_add_u32 s25, s25, s39\n" \
"s_add_u32 m0, s25, 102400\n" \
"s_and_b32 s25, s38, 40960\n" \
"s_add_u32 s25, s25, s36\n" \
"s_add_u32 s26, s25, 0\n" \
"buffer_load_dwordx4 v1, %0, s26 offen offset:0 sc1 lds\n" \
"v_exp_f32 v169, v169\n" \
"s_waitcnt lgkmcnt(3)\n" \
"v_mfma_f32_32x32x16_bf16 v[72:87], v[64:67], v[240:243], v[72:87]\n" \
"ds_read_b128 v[64:67], v8 offset:5248\n" \
"v_exp_f32 v170, v170\n" \
"v_cvt_pk_bf16_f32 v164, v168, v169\n" \
"v_exp_f32 v171, v171\n" \
"buffer_load_dwordx4 v1, %0, s26 offen offset:1024 sc1 lds\n" \
"v_exp_f32 v172, v172\n" \
"v_cvt_pk_bf16_f32 v165, v170, v171\n" \
"s_waitcnt lgkmcnt(3)\n" \
"v_mfma_f32_32x32x16_bf16 v[72:87], v[68:71], v[244:247], v[72:87]\n" \
"ds_read_b128 v[68:71], v8 offset:6560\n" \
"v_exp_f32 v173, v173\n" \
"v_exp_f32 v174, v174\n" \
"v_cvt_pk_bf16_f32 v166, v172, v173\n" \
"buffer_load_dwordx4 v1, %0, s26 offen offset:2048 sc1 lds\n" \
"v_exp_f32 v175, v175\n" \
"s_waitcnt lgkmcnt(3)\n" \
"v_mfma_f32_32x32x16_bf16 v[72:87], v[56:59], v[128:131], v[72:87]\n" \
"ds_read_b128 v[56:59], v8 offset:7872\n" \
"v_exp_f32 v176, v176\n" \
"v_cvt_pk_bf16_f32 v167, v174, v175\n" \
"v_exp_f32 v177, v177\n" \
"v_mfma_f32_32x32x8_bf16 v[208:223], v[22:23], v[24:25], v[208:223]\n" \
"buffer_load_dwordx4 v1, %0, s26 offen offset:3072 sc1 lds\n" \
"v_exp_f32 v178, v178\n" \
"v_cvt_pk_bf16_f32 v176, v176, v177\n" \
"v_exp_f32 v179, v179\n" \
"s_waitcnt lgkmcnt(3)\n" \
"v_mfma_f32_32x32x16_bf16 v[72:87], v[60:63], v[132:135], v[72:87]\n" \
"ds_read_b128 v[60:63], v8 offset:9184\n" \
"v_exp_f32 v180, v180\n" \
"v_cvt_pk_bf16_f32 v177, v178, v179\n" \
"s_add_u32 m0, m0, 4096\n" \
"s_nop 0\n" \
"buffer_load_dwordx4 v2, %0, s26 offen sc1 lds\n" \
"v_exp_f32 v181, v181\n" \
"v_exp_f32 v182, v182\n" \
"v_cvt_pk_bf16_f32 v178, v180, v181\n" \
"s_waitcnt lgkmcnt(3)\n" \
"v_mfma_f32_32x32x16_bf16 v[72:87], v[64:67], v[144:147], v[72:87]\n" \
"ds_read_b128 v[64:67], v8 offset:10496\n" \
"v_exp_f32 v183, v183\n" \
"v_exp_f32 v184, v184\n" \
"v_cvt_pk_bf16_f32 v179, v182, v183\n" \
"v_exp_f32 v185, v185\n" \
"s_waitcnt lgkmcnt(3)\n" \
"v_mfma_f32_32x32x16_bf16 v[72:87], v[68:71], v[148:151], v[72:87]\n" \
"ds_read_b128 v[68:71], v8 offset:11808\n" \
"v_exp_f32 v186, v186\n" \
"v_cvt_pk_bf16_f32 v180, v184, v185\n" \
"v_exp_f32 v187, v187\n" \
"v_exp_f32 v188, v188\n" \
"v_cvt_pk_bf16_f32 v181, v186, v187\n" \
"s_waitcnt lgkmcnt(3)\n" \
"v_mfma_f32_32x32x16_bf16 v[72:87], v[56:59], v[160:163], v[72:87]\n" \
"ds_read_b128 v[56:59], v8 offset:13120\n" \
"v_exp_f32 v189, v189\n" \
"v_exp_f32 v190, v190\n" \
"v_cvt_pk_bf16_f32 v182, v188, v189\n" \
"v_exp_f32 v191, v191\n" \
"s_waitcnt lgkmcnt(3)\n" \
"v_mfma_f32_32x32x16_bf16 v[72:87], v[60:63], v[164:167], v[72:87]\n" \
"ds_read_b128 v[60:63], v8 offset:14432\n" \
"v_exp_f32 v192, v192\n" \
"v_cvt_pk_bf16_f32 v183, v190, v191\n" \
"v_exp_f32 v193, v193\n" \
"v_mfma_f32_32x32x8_bf16 v[224:239], v[22:23], v[24:25], v[224:239]\n" \
"v_exp_f32 v194, v194\n" \
"v_cvt_pk_bf16_f32 v192, v192, v193\n" \
"v_exp_f32 v195, v195\n" \
"s_waitcnt lgkmcnt(3)\n" \
"v_mfma_f32_32x32x16_bf16 v[72:87], v[64:67], v[176:179], v[72:87]\n" \
"ds_read_b128 v[64:67], v8 offset:15744\n" \
"v_exp_f32 v196, v196\n" \
"v_cvt_pk_bf16_f32 v193, v194, v195\n" \
"v_exp_f32 v197, v197\n" \
"v_exp_f32 v198, v198\n" \
"v_cvt_pk_bf16_f32 v194, v196, v197\n" \
"s_waitcnt lgkmcnt(3)\n" \
"v_mfma_f32_32x32x16_bf16 v[72:87], v[68:71], v[180:183], v[72:87]\n" \
"ds_read_b128 v[68:71], v8 offset:17056\n" \
"v_exp_f32 v199, v199\n" \
"v_exp_f32 v200, v200\n" \
"v_cvt_pk_bf16_f32 v195, v198, v199\n" \
"v_exp_f32 v201, v201\n" \
"v_exp_f32 v202, v202\n" \
"v_cvt_pk_bf16_f32 v196, v200, v201\n" \
"v_exp_f32 v203, v203\n" \
"v_exp_f32 v204, v204\n" \
"v_cvt_pk_bf16_f32 v197, v202, v203\n" \
"v_exp_f32 v205, v205\n" \
"v_exp_f32 v206, v206\n" \
"v_cvt_pk_bf16_f32 v198, v204, v205\n" \
"v_exp_f32 v207, v207\n" \
"v_exp_f32 v208, v208\n" \
"v_cvt_pk_bf16_f32 v199, v206, v207\n" \
"v_exp_f32 v209, v209\n" \
"v_exp_f32 v210, v210\n" \
"v_cvt_pk_bf16_f32 v208, v208, v209\n" \
"v_exp_f32 v211, v211\n" \
"s_waitcnt lgkmcnt(3)\n" \
"v_mfma_f32_32x32x16_bf16 v[72:87], v[56:59], v[192:195], v[72:87]\n" \
"ds_read_b128 v[56:59], v8 offset:18368\n" \
"v_exp_f32 v212, v212\n" \
"v_cvt_pk_bf16_f32 v209, v210, v211\n" \
"v_exp_f32 v213, v213\n" \
"v_exp_f32 v214, v214\n" \
"v_cvt_pk_bf16_f32 v210, v212, v213\n" \
"s_waitcnt lgkmcnt(3)\n" \
"v_mfma_f32_32x32x16_bf16 v[72:87], v[60:63], v[196:199], v[72:87]\n" \
"ds_read_b128 v[60:63], v8 offset:19680\n" \
"v_exp_f32 v215, v215\n" \
"v_exp_f32 v216, v216\n" \
"v_cvt_pk_bf16_f32 v211, v214, v215\n" \
"v_exp_f32 v217, v217\n" \
"v_exp_f32 v218, v218\n" \
"v_cvt_pk_bf16_f32 v212, v216, v217\n" \
"v_exp_f32 v219, v219\n" \
"v_exp_f32 v220, v220\n" \
"v_cvt_pk_bf16_f32 v213, v218, v219\n" \
"v_exp_f32 v221, v221\n" \
"v_exp_f32 v222, v222\n" \
"v_cvt_pk_bf16_f32 v214, v220, v221\n" \
"v_exp_f32 v223, v223\n" \
"v_exp_f32 v224, v224\n" \
"v_cvt_pk_bf16_f32 v215, v222, v223\n" \
"v_exp_f32 v225, v225\n" \
"v_exp_f32 v226, v226\n" \
"v_cvt_pk_bf16_f32 v224, v224, v225\n" \
"v_exp_f32 v227, v227\n" \
"s_waitcnt lgkmcnt(3)\n" \
"v_mfma_f32_32x32x16_bf16 v[72:87], v[64:67], v[208:211], v[72:87]\n" \
"v_exp_f32 v228, v228\n" \
"v_cvt_pk_bf16_f32 v225, v226, v227\n" \
"v_exp_f32 v229, v229\n" \
"v_exp_f32 v230, v230\n" \
"v_cvt_pk_bf16_f32 v226, v228, v229\n" \
"s_waitcnt lgkmcnt(2)\n" \
"v_mfma_f32_32x32x16_bf16 v[72:87], v[68:71], v[212:215], v[72:87]\n" \
"v_exp_f32 v231, v231\n" \
"v_exp_f32 v232, v232\n" \
"v_cvt_pk_bf16_f32 v227, v230, v231\n" \
"v_exp_f32 v233, v233\n" \
"v_exp_f32 v234, v234\n" \
"v_cvt_pk_bf16_f32 v228, v232, v233\n" \
"v_exp_f32 v235, v235\n" \
"v_exp_f32 v236, v236\n" \
"v_cvt_pk_bf16_f32 v229, v234, v235\n" \
"s_waitcnt lgkmcnt(1)\n" \
"v_mfma_f32_32x32x16_bf16 v[72:87], v[56:59], v[224:227], v[72:87]\n" \
"v_exp_f32 v237, v237\n" \
"v_exp_f32 v238, v238\n" \
"v_cvt_pk_bf16_f32 v230, v236, v237\n" \
"v_exp_f32 v239, v239\n" \
"s_nop 0\n" \
"v_cvt_pk_bf16_f32 v231, v238, v239\n" \
"s_waitcnt lgkmcnt(0)\n" \
"s_nop 0\n" \
"v_mfma_f32_32x32x16_bf16 v[72:87], v[60:63], v[228:231], v[72:87]\n" \
"s_nop 11\n" \
"v_cndmask_b32 v26, v80, v84, s[30:31]\n" \
"v_rcp_f32 v26, v26\n" \
"s_nop 0\n" \
"v_mul_f32 v72, v72, v26\n" \
"v_mul_f32 v73, v73, v26\n" \
"v_mul_f32 v74, v74, v26\n" \
"v_mul_f32 v75, v75, v26\n" \
"v_mul_f32 v76, v76, v26\n" \
"v_mul_f32 v77, v77, v26\n" \
"v_mul_f32 v78, v78, v26\n" \
"v_mul_f32 v79, v79, v26\n" \
"v_mul_f32 v80, v80, v26\n" \
"v_mul_f32 v81, v81, v26\n" \
"v_mul_f32 v82, v82, v26\n" \
"v_mul_f32 v83, v83, v26\n" \
"v_cvt_pk_bf16_f32 v88, v72, v73\n" \
"v_cvt_pk_bf16_f32 v89, v74, v75\n" \
"v_cvt_pk_bf16_f32 v90, v76, v77\n" \
"v_cvt_pk_bf16_f32 v91, v78, v79\n" \
"v_bfi_b32 v88, 1, s24, v88\n" \
"buffer_store_dwordx4 v[88:91], v9, %0, s42 offen sc1\n" \
"v_cvt_pk_bf16_f32 v92, v80, v81\n" \
"v_cvt_pk_bf16_f32 v93, v82, v83\n" \
"v_bfi_b32 v92, 1, s24, v92\n" \
"buffer_store_dwordx2 v[92:93], v10, %0, s43 offen sc1\n" \
"s_setprio 0\n" \
"s_waitcnt vmcnt(7)\n" \
"s_barrier\n" \
"s_and_b32 s25, s38, 10240\n" \
"s_add_u32 s25, s25, s39\n" \
"s_add_u32 m0, s25, 122880\n" \
"s_and_b32 s25, s38, 4294895616\n" \
"s_add_u32 s25, s25, s36\n" \
"s_add_u32 s26, s25, 81920\n" \
"buffer_load_dwordx4 v1, %0, s26 offen offset:0 sc1 lds\n" \
"buffer_load_dwordx4 v1, %0, s26 offen offset:1024 sc1 lds\n" \
"buffer_load_dwordx4 v1, %0, s26 offen offset:2048 sc1 lds\n" \
"buffer_load_dwordx4 v1, %0, s26 offen offset:3072 sc1 lds\n" \
"s_add_u32 m0, m0, 4096\n" \
"s_nop 0\n" \
"buffer_load_dwordx4 v2, %0, s26 offen sc1 lds\n" \
"s_add_u32 m0, s40, 143360\n" \
"s_add_u32 s26, s40, s36\n" \
"s_add_u32 s26, s26, 51200\n" \
"buffer_load_dwordx4 v0, %0, s26 offen sc1 lds\n" \
"buffer_load_dwordx4 v0, %0, s26 offen offset:1024 sc1 lds\n" \
"s_mov_b32 exec_hi, 0\n" \
"buffer_load_dwordx4 v0, %0, s26 offen offset:2048 sc1 lds\n" \
"s_mov_b32 exec_hi, -1\n" \
"s_and_b32 s25, s38, 10240\n" \
"s_add_u32 s25, s25, s37\n" \
"s_add_u32 s26, s25, 0\n" \
"buffer_load_dwordx4 v[96:99], v1, %0, s26 offen offset:0 sc1\n" \
"buffer_load_dwordx4 v[100:103], v1, %0, s26 offen offset:1024 sc1\n" \
"buffer_load_dwordx4 v[104:107], v1, %0, s26 offen offset:2048 sc1\n" \
"buffer_load_dwordx4 v[108:111], v1, %0, s26 offen offset:3072 sc1\n" \
"buffer_load_dwordx4 v[112:115], v2, %0, s26 offen offset:0 sc1\n" \
"s_barrier\n" \
"s_sleep 6\n" \
"s_and_b32 s25, s38, 4294885376\n" \
"s_add_u32 s25, s25, s39\n" \
"s_add_u32 m0, s25, 81920\n" \
"s_and_b32 s25, s38, 4294895616\n" \
"s_add_u32 s25, s25, s36\n" \
"s_add_u32 s26, s25, 92160\n" \
"buffer_load_dwordx4 v1, %0, s26 offen offset:0 sc1 lds\n" \
"buffer_load_dwordx4 v1, %0, s26 offen offset:1024 sc1 lds\n" \
"buffer_load_dwordx4 v1, %0, s26 offen offset:2048 sc1 lds\n" \
"buffer_load_dwordx4 v1, %0, s26 offen offset:3072 sc1 lds\n" \
"s_add_u32 m0, m0, 4096\n" \
"s_nop 0\n" \
"buffer_load_dwordx4 v2, %0, s26 offen sc1 lds\n" \
"s_and_b32 s25, s38, 10240\n" \
"s_add_u32 s25, s25, s39\n" \
"s_add_u32 m0, s25, 10240\n" \
"s_and_b32 s25, s38, 4294936576\n" \
"s_add_u32 s25, s25, s36\n" \
"s_add_u32 s26, s25, 61440\n" \
"buffer_load_dwordx4 v1, %0, s26 offen offset:0 sc1 lds\n" \
"buffer_load_dwordx4 v1, %0, s26 offen offset:1024 sc1 lds\n" \
"buffer_load_dwordx4 v1, %0, s26 offen offset:2048 sc1 lds\n" \
"buffer_load_dwordx4 v1, %0, s26 offen offset:3072 sc1 lds\n" \
"s_add_u32 m0, m0, 4096\n" \
"s_nop 0\n" \
"buffer_load_dwordx4 v2, %0, s26 offen sc1 lds\n" \
"s_add_u32 m0, s40, 30720\n" \
"s_add_u32 s26, s40, s36\n" \
"s_add_u32 s26, s26, 71680\n" \
"buffer_load_dwordx4 v0, %0, s26 offen sc1 lds\n" \
"buffer_load_dwordx4 v0, %0, s26 offen offset:1024 sc1 lds\n" \
"s_mov_b32 exec_hi, 0\n" \
"buffer_load_dwordx4 v0, %0, s26 offen offset:2048 sc1 lds\n" \
"s_mov_b32 exec_hi, -1\n" \
"s_waitcnt vmcnt(3)\n" \
"s_barrier\n" \
"s_and_b32 s25, s38, 10240\n" \
"s_add_u32 s25, s25, s39\n" \
"s_add_u32 m0, s25, 40960\n" \
"s_and_b32 s25, s38, 10240\n" \
"s_add_u32 s25, s25, s36\n" \
"s_add_u32 s26, s25, 102400\n" \
"buffer_load_dwordx4 v1, %0, s26 offen offset:0 sc1 lds\n" \
"buffer_load_dwordx4 v1, %0, s26 offen offset:1024 sc1 lds\n" \
"buffer_load_dwordx4 v1, %0, s26 offen offset:2048 sc1 lds\n" \
"buffer_load_dwordx4 v1, %0, s26 offen offset:3072 sc1 lds\n" \
"s_add_u32 m0, m0, 4096\n" \
"s_nop 0\n" \
"buffer_load_dwordx4 v2, %0, s26 offen sc1 lds\n" \
"s_and_b32 s25, s38, 10240\n" \
"s_add_u32 s25, s25, s39\n" \
"s_add_u32 m0, s25, 61440\n" \
"s_and_b32 s25, s38, 10240\n" \
"s_add_u32 s25, s25, s36\n" \
"s_add_u32 s26, s25, 122880\n" \
"buffer_load_dwordx4 v1, %0, s26 offen offset:0 sc1 lds\n" \
"buffer_load_dwordx4 v1, %0, s26 offen offset:1024 sc1 lds\n" \
"buffer_load_dwordx4 v1, %0, s26 offen offset:2048 sc1 lds\n" \
"buffer_load_dwordx4 v1, %0, s26 offen offset:3072 sc1 lds\n" \
"s_add_u32 m0, m0, 4096\n" \
"s_nop 0\n" \
"buffer_load_dwordx4 v2, %0, s26 offen sc1 lds\n" \
"s_cmp_lg_u32 s24, 0\n" \
"s_cbranch_scc1 .Ll505_%=\n" \
"s_mul_i32 s26, %3, 16384\n" \
"s_mul_i32 s25, s27, 256\n" \
"s_add_u32 s26, s26, s25\n" \
"s_add_u32 s26, s26, 28106752\n" \
"v_lshrrev_b32 v30, 2, v0\n" \
"buffer_load_dword v56, v30, %0, s26 offen offset:0 sc1\n" \
"buffer_load_dword v57, v30, %0, s26 offen offset:1024 sc1\n" \
"buffer_load_dword v58, v30, %0, s26 offen offset:2048 sc1\n" \
"buffer_load_dword v59, v30, %0, s26 offen offset:3072 sc1\n" \
"s_add_u32 s26, s26, 4096\n" \
"buffer_load_dword v60, v30, %0, s26 offen offset:0 sc1\n" \
"buffer_load_dword v61, v30, %0, s26 offen offset:1024 sc1\n" \
"buffer_load_dword v62, v30, %0, s26 offen offset:2048 sc1\n" \
"buffer_load_dword v63, v30, %0, s26 offen offset:3072 sc1\n" \
"s_add_u32 s26, s26, 4096\n" \
"buffer_load_dword v64, v30, %0, s26 offen offset:0 sc1\n" \
"buffer_load_dword v65, v30, %0, s26 offen offset:1024 sc1\n" \
"buffer_load_dword v66, v30, %0, s26 offen offset:2048 sc1\n" \
"buffer_load_dword v67, v30, %0, s26 offen offset:3072 sc1\n" \
"s_add_u32 s26, s26, 4096\n" \
"buffer_load_dword v68, v30, %0, s26 offen offset:0 sc1\n" \
"buffer_load_dword v69, v30, %0, s26 offen offset:1024 sc1\n" \
"buffer_load_dword v70, v30, %0, s26 offen offset:2048 sc1\n" \
"buffer_load_dword v71, v30, %0, s26 offen offset:3072 sc1\n" \
".Ll505_%=:\n" \
"s_cmp_lg_u32 s24, 0\n" \
"s_cbranch_scc1 .Ll506_%=\n" \
"s_waitcnt vmcnt(14)\n" \
"v_add_f32 v56, v56, v57\n" \
"s_waitcnt vmcnt(13)\n" \
"v_add_f32 v56, v56, v58\n" \
"s_waitcnt vmcnt(12)\n" \
"v_add_f32 v56, v56, v59\n" \
"s_waitcnt vmcnt(11)\n" \
"v_add_f32 v56, v56, v60\n" \
"s_waitcnt vmcnt(10)\n" \
"v_add_f32 v56, v56, v61\n" \
"s_waitcnt vmcnt(9)\n" \
"v_add_f32 v56, v56, v62\n" \
"s_waitcnt vmcnt(8)\n" \
"v_add_f32 v56, v56, v63\n" \
"s_waitcnt vmcnt(7)\n" \
"v_add_f32 v56, v56, v64\n" \
"s_waitcnt vmcnt(6)\n" \
"v_add_f32 v56, v56, v65\n" \
"s_waitcnt vmcnt(5)\n" \
"v_add_f32 v56, v56, v66\n" \
"s_waitcnt vmcnt(4)\n" \
"v_add_f32 v56, v56, v67\n" \
"s_waitcnt vmcnt(3)\n" \
"v_add_f32 v56, v56, v68\n" \
"s_waitcnt vmcnt(2)\n" \
"v_add_f32 v56, v56, v69\n" \
"s_waitcnt vmcnt(1)\n" \
"v_add_f32 v56, v56, v70\n" \
"s_waitcnt vmcnt(0)\n" \
"v_add_f32 v56, v56, v71\n" \
"s_lshl_b32 s26, s27, 8\n" \
"s_add_u32 s26, s26, 161024\n" \
"v_add_u32 v30, s26, v30\n" \
"ds_write_b32 v30, v56\n" \
".Ll506_%=:\n" \
"s_waitcnt vmcnt(10) lgkmcnt(0)\n" \
"s_barrier\n" \
"s_and_b32 s25, s38, 10240\n" \
"s_add_u32 s25, s25, s39\n" \
"s_add_u32 m0, s25, 92160\n" \
"s_and_b32 s25, s38, 10240\n" \
"s_add_u32 s25, s25, s36\n" \
"s_add_u32 s26, s25, 143360\n" \
"buffer_load_dwordx4 v1, %0, s26 offen offset:0 sc1 lds\n" \
"buffer_load_dwordx4 v1, %0, s26 offen offset:1024 sc1 lds\n" \
"buffer_load_dwordx4 v1, %0, s26 offen offset:2048 sc1 lds\n" \
"buffer_load_dwordx4 v1, %0, s26 offen offset:3072 sc1 lds\n" \
"s_add_u32 m0, m0, 4096\n" \
"s_nop 0\n" \
"buffer_load_dwordx4 v2, %0, s26 offen sc1 lds\n" \
"s_and_b32 s25, s38, 10240\n" \
"s_add_u32 s25, s25, s39\n" \
"s_add_u32 m0, s25, 112640\n" \
"s_and_b32 s25, s38, 10240\n" \
"s_add_u32 s25, s25, s36\n" \
"s_add_u32 s26, s25, 163840\n" \
"buffer_load_dwordx4 v1, %0, s26 offen offset:0 sc1 lds\n" \
"buffer_load_dwordx4 v1, %0, s26 offen offset:1024 sc1 lds\n" \
"buffer_load_dwordx4 v1, %0, s26 offen offset:2048 sc1 lds\n" \
"buffer_load_dwordx4 v1, %0, s26 offen offset:3072 sc1 lds\n" \
"s_add_u32 m0, m0, 4096\n" \
"s_nop 0\n" \
"buffer_load_dwordx4 v2, %0, s26 offen sc1 lds\n" \
"s_and_b32 s25, s38, 10240\n" \
"s_add_u32 s25, s25, s39\n" \
"s_add_u32 m0, s25, 133120\n" \
"s_and_b32 s25, s38, 10240\n" \
"s_add_u32 s25, s25, s36\n" \
"s_add_u32 s26, s25, 184320\n" \
"buffer_load_dwordx4 v1, %0, s26 offen offset:0 sc1 lds\n" \
"buffer_load_dwordx4 v1, %0, s26 offen offset:1024 sc1 lds\n" \
"buffer_load_dwordx4 v1, %0, s26 offen offset:2048 sc1 lds\n" \
"buffer_load_dwordx4 v1, %0, s26 offen offset:3072 sc1 lds\n" \
"s_add_u32 m0, m0, 4096\n" \
"s_nop 0\n" \
"buffer_load_dwordx4 v2, %0, s26 offen sc1 lds\n" \
"s_and_b32 s25, s38, 10240\n" \
"s_add_u32 s25, s25, s37\n" \
"s_add_u32 s26, s25, 20480\n" \
"buffer_load_dwordx4 v[116:119], v1, %0, s26 offen offset:0 sc1\n" \
"buffer_load_dwordx4 v[120:123], v1, %0, s26 offen offset:1024 sc1\n" \
"buffer_load_dwordx4 v[124:127], v1, %0, s26 offen offset:2048 sc1\n" \
"buffer_load_dwordx4 v[128:131], v1, %0, s26 offen offset:3072 sc1\n" \
"buffer_load_dwordx4 v[132:135], v2, %0, s26 offen offset:0 sc1\n" \
"s_waitcnt vmcnt(20)\n" \
"s_barrier\n" \
"s_and_b32 s25, s38, 10240\n" \
"s_add_u32 s25, s25, s39\n" \
"s_add_u32 m0, s25, 0\n" \
"s_and_b32 s25, s38, 10240\n" \
"s_add_u32 s25, s25, s36\n" \
"s_add_u32 s26, s25, 204800\n" \
"buffer_load_dwordx4 v1, %0, s26 offen offset:0 sc1 lds\n" \
"buffer_load_dwordx4 v1, %0, s26 offen offset:1024 sc1 lds\n" \
"buffer_load_dwordx4 v1, %0, s26 offen offset:2048 sc1 lds\n" \
"buffer_load_dwordx4 v1, %0, s26 offen offset:3072 sc1 lds\n" \
"s_add_u32 m0, m0, 4096\n" \
"s_nop 0\n" \
"buffer_load_dwordx4 v2, %0, s26 offen sc1 lds\n" \
"s_and_b32 s25, s38, 10240\n" \
"s_add_u32 s25, s25, s39\n" \
"s_add_u32 m0, s25, 20480\n" \
"s_and_b32 s25, s38, 10240\n" \
"s_add_u32 s25, s25, s36\n" \
"s_add_u32 s26, s25, 225280\n" \
"buffer_load_dwordx4 v1, %0, s26 offen offset:0 sc1 lds\n" \
"buffer_load_dwordx4 v1, %0, s26 offen offset:1024 sc1 lds\n" \
"buffer_load_dwordx4 v1, %0, s26 offen offset:2048 sc1 lds\n" \
"buffer_load_dwordx4 v1, %0, s26 offen offset:3072 sc1 lds\n" \
"s_add_u32 m0, m0, 4096\n" \
"s_nop 0\n" \
"buffer_load_dwordx4 v2, %0, s26 offen sc1 lds\n" \
"s_and_b32 s25, s38, 10240\n" \
"s_add_u32 s25, s25, s37\n" \
"s_add_u32 s26, s25, 40960\n" \
"buffer_load_dwordx4 v[136:139], v1, %0, s26 offen offset:0 sc1\n" \
"buffer_load_dwordx4 v[140:143], v1, %0, s26 offen offset:1024 sc1\n" \
"buffer_load_dwordx4 v[144:147], v1, %0, s26 offen offset:2048 sc1\n" \
"buffer_load_dwordx4 v[148:151], v1, %0, s26 offen offset:3072 sc1\n" \
"buffer_load_dwordx4 v[152:155], v2, %0, s26 offen offset:0 sc1\n" \
"s_waitcnt vmcnt(25)\n" \
"s_barrier\n" \
"s_and_b32 s25, s38, 4294926336\n" \
"s_add_u32 s25, s25, s39\n" \
"s_add_u32 m0, s25, 81920\n" \
"s_and_b32 s25, s38, 10240\n" \
"s_add_u32 s25, s25, s36\n" \
"s_add_u32 s26, s25, 245760\n" \
"buffer_load_dwordx4 v1, %0, s26 offen offset:0 sc1 lds\n" \
"buffer_load_dwordx4 v1, %0, s26 offen offset:1024 sc1 lds\n" \
"buffer_load_dwordx4 v1, %0, s26 offen offset:2048 sc1 lds\n" \
"buffer_load_dwordx4 v1, %0, s26 offen offset:3072 sc1 lds\n" \
"s_add_u32 m0, m0, 4096\n" \
"s_nop 0\n" \
"buffer_load_dwordx4 v2, %0, s26 offen sc1 lds\n" \
"s_add_u32 m0, s40, 51200\n" \
"s_add_u32 s26, s40, s36\n" \
"s_add_u32 s26, s26, 266240\n" \
"buffer_load_dwordx4 v0, %0, s26 offen sc1 lds\n" \
"buffer_load_dwordx4 v0, %0, s26 offen offset:1024 sc1 lds\n" \
"s_mov_b32 exec_hi, 0\n" \
"buffer_load_dwordx4 v0, %0, s26 offen offset:2048 sc1 lds\n" \
"s_mov_b32 exec_hi, -1\n" \
"s_and_b32 s25, s38, 10240\n" \
"s_add_u32 s25, s25, s37\n" \
"s_add_u32 s26, s25, 61440\n" \
"buffer_load_dwordx4 v[156:159], v1, %0, s26 offen offset:0 sc1\n" \
"buffer_load_dwordx4 v[160:163], v1, %0, s26 offen offset:1024 sc1\n" \
"buffer_load_dwordx4 v[164:167], v1, %0, s26 offen offset:2048 sc1\n" \
"buffer_load_dwordx4 v[168:171], v1, %0, s26 offen offset:3072 sc1\n" \
"buffer_load_dwordx4 v[172:175], v2, %0, s26 offen offset:0 sc1\n" \
"s_and_b32 s25, s38, 10240\n" \
"s_add_u32 s25, s25, s37\n" \
"s_add_u32 s26, s25, 81920\n" \
"buffer_load_dwordx4 v[176:179], v1, %0, s26 offen offset:0 sc1\n" \
"buffer_load_dwordx4 v[180:183], v1, %0, s26 offen offset:1024 sc1\n" \
"buffer_load_dwordx4 v[184:187], v1, %0, s26 offen offset:2048 sc1\n" \
"buffer_load_dwordx4 v[188:191], v1, %0, s26 offen offset:3072 sc1\n" \
"buffer_load_dwordx4 v[192:195], v2, %0, s26 offen offset:0 sc1\n" \
"s_cmp_lg_u32 s24, 0\n" \
"s_cbranch_scc1 .Ll507_%=\n" \
"v_mov_b32 v6, %5\n" \
"s_mov_b64 exec, 1\n" \
"buffer_store_dword v6, off, %0, s50 sc1\n" \
"s_mov_b64 exec, -1\n" \
".Ll507_%=:\n" \
"s_waitcnt vmcnt(28)\n" \
"s_barrier\n" \
"s_add_u32 m0, s40, 61440\n" \
"s_add_u32 s26, s40, s36\n" \
"s_add_u32 s26, s26, 276480\n" \
"buffer_load_dwordx4 v0, %0, s26 offen sc1 lds\n" \
"buffer_load_dwordx4 v0, %0, s26 offen offset:1024 sc1 lds\n" \
"s_mov_b32 exec_hi, 0\n" \
"buffer_load_dwordx4 v0, %0, s26 offen offset:2048 sc1 lds\n" \
"s_mov_b32 exec_hi, -1\n" \
"s_and_b32 s25, s38, 30720\n" \
"s_add_u32 s25, s25, s39\n" \
"s_add_u32 m0, s25, 71680\n" \
"s_and_b32 s25, s38, 10240\n" \
"s_add_u32 s25, s25, s37\n" \
"s_add_u32 s26, s25, 122880\n" \
"buffer_load_dwordx4 v1, %0, s26 offen offset:0 sc1 lds\n" \
"buffer_load_dwordx4 v1, %0, s26 offen offset:1024 sc1 lds\n" \
"buffer_load_dwordx4 v1, %0, s26 offen offset:2048 sc1 lds\n" \
"buffer_load_dwordx4 v1, %0, s26 offen offset:3072 sc1 lds\n" \
"s_add_u32 m0, m0, 4096\n" \
"s_nop 0\n" \
"buffer_load_dwordx4 v2, %0, s26 offen sc1 lds\n" \
"s_add_u32 m0, s40, 92160\n" \
"s_add_u32 s26, s40, s37\n" \
"s_add_u32 s26, s26, 163840\n" \
"buffer_load_dwordx4 v0, %0, s26 offen sc1 lds\n" \
"buffer_load_dwordx4 v0, %0, s26 offen offset:1024 sc1 lds\n" \
"s_mov_b32 exec_hi, 0\n" \
"buffer_load_dwordx4 v0, %0, s26 offen offset:2048 sc1 lds\n" \
"s_mov_b32 exec_hi, -1\n" \
"s_and_b32 s25, s38, 10240\n" \
"s_add_u32 s25, s25, s37\n" \
"s_add_u32 s26, s25, 102400\n" \
"buffer_load_dwordx4 v[196:199], v1, %0, s26 offen offset:0 sc1\n" \
"buffer_load_dwordx4 v[200:203], v1, %0, s26 offen offset:1024 sc1\n" \
"buffer_load_dwordx4 v[204:207], v1, %0, s26 offen offset:2048 sc1\n" \
"buffer_load_dwordx4 v[208:211], v1, %0, s26 offen offset:3072 sc1\n" \
"buffer_load_dwordx4 v[212:215], v2, %0, s26 offen offset:0 sc1\n" \
"s_sleep 0\n" \
"buffer_load_dwordx4 v[216:219], v0, %0, s51 offen offset:0 sc1\n" \
"buffer_load_dwordx4 v[220:223], v0, %0, s51 offen offset:2048 sc1\n" \
"s_add_u32 s51, s51, 4096\n" \
"buffer_load_dwordx4 v[224:227], v0, %0, s52 offen offset:0 sc1\n" \
"buffer_load_dwordx4 v[228:231], v0, %0, s52 offen offset:2048 sc1\n" \
"s_add_u32 s52, s52, 4096\n" \
"buffer_load_dwordx4 v[232:235], v0, %0, s53 offen offset:0 sc1\n" \
"s_waitcnt vmcnt(34)\n" \
"s_barrier\n" \
"buffer_load_dwordx4 v[236:239], v0, %0, s53 offen offset:2048 sc1\n" \
"s_add_u32 s53, s53, 4096\n" \
"buffer_load_dwordx4 v[240:243], v0, %0, s54 offen offset:0 sc1\n" \
"buffer_load_dwordx4 v[244:247], v0, %0, s54 offen offset:2048 sc1\n" \
"s_add_u32 s54, s54, 4096\n" \
"buffer_load_dwordx4 v[248:251], v0, %0, s51 offen offset:0 sc1\n" \
"buffer_load_dwordx4 v[252:255], v0, %0, s51 offen offset:2048 sc1\n" \
"s_add_u32 s51, s51, 4096\n" \
"v_add_u32 v6, s57, v0\n" \
"v_add_u32 v7, s58, v0\n" \
"v_add_u32 v8, s59, v0\n" \
"v_add_u32 v9, s60, v0\n" \
"s_setprio 1\n" \
"s_and_b32 s25, s38, 10240\n" \
"s_add_u32 s25, s25, s39\n" \
"s_add_u32 m0, s25, 122880\n" \
"s_and_b32 s25, s38, 10240\n" \
"s_add_u32 s25, s25, s37\n" \
"s_add_u32 s26, s25, 143360\n" \
"buffer_load_dwordx4 v1, %0, s26 offen offset:0 sc1 lds\n" \
"s_waitcnt vmcnt(10)\n" \
"v_bitop3_b32 v12, v216, s24, 1 bitop3:0x28\n" \
"v_cmp_ne_u32 vcc, 0, v12\n" \
"s_cbranch_vccnz .Ll508_%=\n" \
".Ll509_%=:\n" \
"v_mfma_f32_16x16x32_bf16 v[16:19], v[96:99], v[216:219], 0\n" \
"v_mfma_f32_16x16x32_bf16 v[20:23], v[100:103], v[216:219], 0\n" \
"v_mfma_f32_16x16x32_bf16 v[24:27], v[104:107], v[216:219], 0\n" \
"v_mfma_f32_16x16x32_bf16 v[28:31], v[108:111], v[216:219], 0\n" \
"s_waitcnt vmcnt(9)\n" \
"v_bitop3_b32 v12, v220, s24, 1 bitop3:0x28\n" \
"v_cmp_ne_u32 vcc, 0, v12\n" \
"buffer_load_dwordx4 v1, %0, s26 offen offset:1024 sc1 lds\n" \
"v_mfma_f32_16x16x32_bf16 v[32:35], v[112:115], v[216:219], 0\n" \
"s_cbranch_vccnz .Ll510_%=\n" \
".Ll511_%=:\n" \
"buffer_load_dwordx4 v[216:219], v0, %0, s52 offen offset:0 sc1\n" \
"v_mfma_f32_16x16x32_bf16 v[16:19], v[116:119], v[220:223], v[16:19]\n" \
"v_mfma_f32_16x16x32_bf16 v[20:23], v[120:123], v[220:223], v[20:23]\n" \
"v_mfma_f32_16x16x32_bf16 v[24:27], v[124:127], v[220:223], v[24:27]\n" \
"buffer_load_dwordx4 v1, %0, s26 offen offset:2048 sc1 lds\n" \
"v_mfma_f32_16x16x32_bf16 v[28:31], v[128:131], v[220:223], v[28:31]\n" \
"s_waitcnt vmcnt(11)\n" \
"v_bitop3_b32 v12, v224, s24, 1 bitop3:0x28\n" \
"v_cmp_ne_u32 vcc, 0, v12\n" \
"v_mfma_f32_16x16x32_bf16 v[32:35], v[132:135], v[220:223], v[32:35]\n" \
"s_cbranch_vccnz .Ll512_%=\n" \
".Ll513_%=:\n" \
"buffer_load_dwordx4 v[220:223], v0, %0, s52 offen offset:2048 sc1\n" \
"s_add_u32 s52, s52, 4096\n" \
"v_mfma_f32_16x16x32_bf16 v[36:39], v[96:99], v[224:227], 0\n" \
"v_mfma_f32_16x16x32_bf16 v[40:43], v[100:103], v[224:227], 0\n" \
"buffer_load_dwordx4 v1, %0, s26 offen offset:3072 sc1 lds\n" \
"v_mfma_f32_16x16x32_bf16 v[44:47], v[104:107], v[224:227], 0\n" \
"v_mfma_f32_16x16x32_bf16 v[48:51], v[108:111], v[224:227], 0\n" \
"s_waitcnt vmcnt(12)\n" \
"v_bitop3_b32 v12, v228, s24, 1 bitop3:0x28\n" \
"v_cmp_ne_u32 vcc, 0, v12\n" \
"v_mfma_f32_16x16x32_bf16 v[52:55], v[112:115], v[224:227], 0\n" \
"s_cbranch_vccnz .Ll514_%=\n" \
".Ll515_%=:\n" \
"buffer_load_dwordx4 v[224:227], v0, %0, s53 offen offset:0 sc1\n" \
"v_mfma_f32_16x16x32_bf16 v[36:39], v[116:119], v[228:231], v[36:39]\n" \
"s_add_u32 m0, m0, 4096\n" \
"s_nop 0\n" \
"buffer_load_dwordx4 v2, %0, s26 offen sc1 lds\n" \
"v_mfma_f32_16x16x32_bf16 v[40:43], v[120:123], v[228:231], v[40:43]\n" \
"v_mfma_f32_16x16x32_bf16 v[44:47], v[124:127], v[228:231], v[44:47]\n" \
"v_mfma_f32_16x16x32_bf16 v[48:51], v[128:131], v[228:231], v[48:51]\n" \
"s_waitcnt vmcnt(13)\n" \
"v_bitop3_b32 v12, v232, s24, 1 bitop3:0x28\n" \
"v_cmp_ne_u32 vcc, 0, v12\n" \
"v_mfma_f32_16x16x32_bf16 v[52:55], v[132:135], v[228:231], v[52:55]\n" \
"s_cbranch_vccnz .Ll516_%=\n" \
".Ll517_%=:\n" \
"buffer_load_dwordx4 v[228:231], v0, %0, s53 offen offset:2048 sc1\n" \
"s_add_u32 s53, s53, 4096\n" \
"v_mfma_f32_16x16x32_bf16 v[56:59], v[96:99], v[232:235], 0\n" \
"v_mfma_f32_16x16x32_bf16 v[60:63], v[100:103], v[232:235], 0\n" \
"v_mfma_f32_16x16x32_bf16 v[64:67], v[104:107], v[232:235], 0\n" \
"v_mfma_f32_16x16x32_bf16 v[68:71], v[108:111], v[232:235], 0\n" \
"s_waitcnt vmcnt(13)\n" \
"v_bitop3_b32 v12, v236, s24, 1 bitop3:0x28\n" \
"v_cmp_ne_u32 vcc, 0, v12\n" \
"v_mfma_f32_16x16x32_bf16 v[72:75], v[112:115], v[232:235], 0\n" \
"s_cbranch_vccnz .Ll518_%=\n" \
".Ll519_%=:\n" \
"buffer_load_dwordx4 v[232:235], v0, %0, s54 offen offset:0 sc1\n" \
"v_mfma_f32_16x16x32_bf16 v[56:59], v[116:119], v[236:239], v[56:59]\n" \
"v_mfma_f32_16x16x32_bf16 v[60:63], v[120:123], v[236:239], v[60:63]\n" \
"v_mfma_f32_16x16x32_bf16 v[64:67], v[124:127], v[236:239], v[64:67]\n" \
"v_mfma_f32_16x16x32_bf16 v[68:71], v[128:131], v[236:239], v[68:71]\n" \
"s_waitcnt vmcnt(13)\n" \
"v_bitop3_b32 v12, v240, s24, 1 bitop3:0x28\n" \
"v_cmp_ne_u32 vcc, 0, v12\n" \
"v_mfma_f32_16x16x32_bf16 v[72:75], v[132:135], v[236:239], v[72:75]\n" \
"s_cbranch_vccnz .Ll520_%=\n" \
".Ll521_%=:\n" \
"buffer_load_dwordx4 v[236:239], v0, %0, s54 offen offset:2048 sc1\n" \
"s_add_u32 s54, s54, 4096\n" \
"v_mfma_f32_16x16x32_bf16 v[76:79], v[96:99], v[240:243], 0\n" \
"v_mfma_f32_16x16x32_bf16 v[80:83], v[100:103], v[240:243], 0\n" \
"v_mfma_f32_16x16x32_bf16 v[84:87], v[104:107], v[240:243], 0\n" \
"v_mfma_f32_16x16x32_bf16 v[88:91], v[108:111], v[240:243], 0\n" \
"s_waitcnt vmcnt(13)\n" \
"v_bitop3_b32 v12, v244, s24, 1 bitop3:0x28\n" \
"v_cmp_ne_u32 vcc, 0, v12\n" \
"v_mfma_f32_16x16x32_bf16 v[92:95], v[112:115], v[240:243], 0\n" \
"s_cbranch_vccnz .Ll522_%=\n" \
".Ll523_%=:\n" \
"buffer_load_dwordx4 v[240:243], v0, %0, s51 offen offset:0 sc1\n" \
"s_barrier\n" \
"s_and_b32 s25, s38, 30720\n" \
"s_add_u32 s25, s25, s39\n" \
"s_add_u32 m0, s25, 112640\n" \
"s_and_b32 s25, s38, 10240\n" \
"s_add_u32 s25, s25, s37\n" \
"s_add_u32 s26, s25, 174080\n" \
"buffer_load_dwordx4 v1, %0, s26 offen offset:0 sc1 lds\n" \
"v_mfma_f32_16x16x32_bf16 v[76:79], v[116:119], v[244:247], v[76:79]\n" \
"v_mfma_f32_16x16x32_bf16 v[80:83], v[120:123], v[244:247], v[80:83]\n" \
"v_mfma_f32_16x16x32_bf16 v[84:87], v[124:127], v[244:247], v[84:87]\n" \
"v_mfma_f32_16x16x32_bf16 v[88:91], v[128:131], v[244:247], v[88:91]\n" \
"s_waitcnt vmcnt(14)\n" \
"v_bitop3_b32 v12, v248, s24, 1 bitop3:0x28\n" \
"v_cmp_ne_u32 vcc, 0, v12\n" \
"buffer_load_dwordx4 v1, %0, s26 offen offset:1024 sc1 lds\n" \
"v_mfma_f32_16x16x32_bf16 v[92:95], v[132:135], v[244:247], v[92:95]\n" \
"s_cbranch_vccnz .Ll524_%=\n" \
".Ll525_%=:\n" \
"buffer_load_dwordx4 v[244:247], v0, %0, s51 offen offset:2048 sc1\n" \
"s_add_u32 s51, s51, -8192\n" \
"v_mfma_f32_16x16x32_bf16 v[16:19], v[136:139], v[248:251], v[16:19]\n" \
"v_mfma_f32_16x16x32_bf16 v[20:23], v[140:143], v[248:251], v[20:23]\n" \
"v_mfma_f32_16x16x32_bf16 v[24:27], v[144:147], v[248:251], v[24:27]\n" \
"buffer_load_dwordx4 v1, %0, s26 offen offset:2048 sc1 lds\n" \
"v_mfma_f32_16x16x32_bf16 v[28:31], v[148:151], v[248:251], v[28:31]\n" \
"s_waitcnt vmcnt(16)\n" \
"v_bitop3_b32 v12, v252, s24, 1 bitop3:0x28\n" \
"v_cmp_ne_u32 vcc, 0, v12\n" \
"v_mfma_f32_16x16x32_bf16 v[32:35], v[152:155], v[248:251], v[32:35]\n" \
"s_cbranch_vccnz .Ll526_%=\n" \
".Ll527_%=:\n" \
"buffer_load_dwordx4 v[248:251], v0, %0, s52 offen offset:0 sc1\n" \
"v_mfma_f32_16x16x32_bf16 v[16:19], v[156:159], v[252:255], v[16:19]\n" \
"v_mfma_f32_16x16x32_bf16 v[20:23], v[160:163], v[252:255], v[20:23]\n" \
"buffer_load_dwordx4 v1, %0, s26 offen offset:3072 sc1 lds\n" \
"v_mfma_f32_16x16x32_bf16 v[24:27], v[164:167], v[252:255], v[24:27]\n" \
"v_mfma_f32_16x16x32_bf16 v[28:31], v[168:171], v[252:255], v[28:31]\n" \
"s_waitcnt vmcnt(15)\n" \
"v_bitop3_b32 v12, v216, s24, 1 bitop3:0x28\n" \
"v_cmp_ne_u32 vcc, 0, v12\n" \
"v_mfma_f32_16x16x32_bf16 v[32:35], v[172:175], v[252:255], v[32:35]\n" \
"s_cbranch_vccnz .Ll528_%=\n" \
".Ll529_%=:\n" \
"buffer_load_dwordx4 v[252:255], v0, %0, s52 offen offset:2048 sc1\n" \
"s_add_u32 s52, s52, -8192\n" \
"v_mfma_f32_16x16x32_bf16 v[36:39], v[136:139], v[216:219], v[36:39]\n" \
"s_add_u32 m0, m0, 4096\n" \
"s_nop 0\n" \
"buffer_load_dwordx4 v2, %0, s26 offen sc1 lds\n" \
"v_mfma_f32_16x16x32_bf16 v[40:43], v[140:143], v[216:219], v[40:43]\n" \
"v_mfma_f32_16x16x32_bf16 v[44:47], v[144:147], v[216:219], v[44:47]\n" \
"v_mfma_f32_16x16x32_bf16 v[48:51], v[148:151], v[216:219], v[48:51]\n" \
"s_waitcnt vmcnt(15)\n" \
"v_bitop3_b32 v12, v220, s24, 1 bitop3:0x28\n" \
"v_cmp_ne_u32 vcc, 0, v12\n" \
"v_mfma_f32_16x16x32_bf16 v[52:55], v[152:155], v[216:219], v[52:55]\n" \
"s_cbranch_vccnz .Ll530_%=\n" \
".Ll531_%=:\n" \
"buffer_load_dwordx4 v[216:219], v0, %0, s53 offen offset:0 sc1\n" \
"v_mfma_f32_16x16x32_bf16 v[36:39], v[156:159], v[220:223], v[36:39]\n" \
"v_mfma_f32_16x16x32_bf16 v[40:43], v[160:163], v[220:223], v[40:43]\n" \
"v_mfma_f32_16x16x32_bf16 v[44:47], v[164:167], v[220:223], v[44:47]\n" \
"v_mfma_f32_16x16x32_bf16 v[48:51], v[168:171], v[220:223], v[48:51]\n" \
"s_waitcnt vmcnt(14)\n" \
"v_bitop3_b32 v12, v224, s24, 1 bitop3:0x28\n" \
"v_cmp_ne_u32 vcc, 0, v12\n" \
"v_mfma_f32_16x16x32_bf16 v[52:55], v[172:175], v[220:223], v[52:55]\n" \
"s_cbranch_vccnz .Ll532_%=\n" \
".Ll533_%=:\n" \
"buffer_load_dwordx4 v[220:223], v0, %0, s53 offen offset:2048 sc1\n" \
"s_add_u32 s53, s53, -8192\n" \
"v_mfma_f32_16x16x32_bf16 v[56:59], v[136:139], v[224:227], v[56:59]\n" \
"v_mfma_f32_16x16x32_bf16 v[60:63], v[140:143], v[224:227], v[60:63]\n" \
"v_mfma_f32_16x16x32_bf16 v[64:67], v[144:147], v[224:227], v[64:67]\n" \
"v_mfma_f32_16x16x32_bf16 v[68:71], v[148:151], v[224:227], v[68:71]\n" \
"s_waitcnt vmcnt(13)\n" \
"v_bitop3_b32 v12, v228, s24, 1 bitop3:0x28\n" \
"v_cmp_ne_u32 vcc, 0, v12\n" \
"v_mfma_f32_16x16x32_bf16 v[72:75], v[152:155], v[224:227], v[72:75]\n" \
"s_cbranch_vccnz .Ll534_%=\n" \
".Ll535_%=:\n" \
"buffer_load_dwordx4 v[224:227], v0, %0, s54 offen offset:0 sc1\n" \
"v_mfma_f32_16x16x32_bf16 v[56:59], v[156:159], v[228:231], v[56:59]\n" \
"v_mfma_f32_16x16x32_bf16 v[60:63], v[160:163], v[228:231], v[60:63]\n" \
"v_mfma_f32_16x16x32_bf16 v[64:67], v[164:167], v[228:231], v[64:67]\n" \
"v_mfma_f32_16x16x32_bf16 v[68:71], v[168:171], v[228:231], v[68:71]\n" \
"s_waitcnt vmcnt(13)\n" \
"v_bitop3_b32 v12, v232, s24, 1 bitop3:0x28\n" \
"v_cmp_ne_u32 vcc, 0, v12\n" \
"v_mfma_f32_16x16x32_bf16 v[72:75], v[172:175], v[228:231], v[72:75]\n" \
"s_cbranch_vccnz .Ll536_%=\n" \
".Ll537_%=:\n" \
"buffer_load_dwordx4 v[228:231], v0, %0, s54 offen offset:2048 sc1\n" \
"s_add_u32 s54, s54, -8192\n" \
"v_mfma_f32_16x16x32_bf16 v[76:79], v[136:139], v[232:235], v[76:79]\n" \
"v_mfma_f32_16x16x32_bf16 v[80:83], v[140:143], v[232:235], v[80:83]\n" \
"v_mfma_f32_16x16x32_bf16 v[84:87], v[144:147], v[232:235], v[84:87]\n" \
"v_mfma_f32_16x16x32_bf16 v[88:91], v[148:151], v[232:235], v[88:91]\n" \
"s_waitcnt vmcnt(13)\n" \
"v_bitop3_b32 v12, v236, s24, 1 bitop3:0x28\n" \
"v_cmp_ne_u32 vcc, 0, v12\n" \
"v_mfma_f32_16x16x32_bf16 v[92:95], v[152:155], v[232:235], v[92:95]\n" \
"s_cbranch_vccnz .Ll538_%=\n" \
".Ll539_%=:\n" \
"v_mfma_f32_16x16x32_bf16 v[76:79], v[156:159], v[236:239], v[76:79]\n" \
"v_mfma_f32_16x16x32_bf16 v[80:83], v[160:163], v[236:239], v[80:83]\n" \
"v_mfma_f32_16x16x32_bf16 v[84:87], v[164:167], v[236:239], v[84:87]\n" \
"v_mfma_f32_16x16x32_bf16 v[88:91], v[168:171], v[236:239], v[88:91]\n" \
"s_waitcnt vmcnt(12)\n" \
"v_bitop3_b32 v12, v240, s24, 1 bitop3:0x28\n" \
"v_cmp_ne_u32 vcc, 0, v12\n" \
"v_mfma_f32_16x16x32_bf16 v[92:95], v[172:175], v[236:239], v[92:95]\n" \
"s_cbranch_vccnz .Ll540_%=\n" \
".Ll541_%=:\n" \
"v_mfma_f32_16x16x32_bf16 v[16:19], v[176:179], v[240:243], v[16:19]\n" \
"v_mfma_f32_16x16x32_bf16 v[20:23], v[180:183], v[240:243], v[20:23]\n" \
"v_mfma_f32_16x16x32_bf16 v[24:27], v[184:187], v[240:243], v[24:27]\n" \
"v_mfma_f32_16x16x32_bf16 v[28:31], v[188:191], v[240:243], v[28:31]\n" \
"s_waitcnt vmcnt(9)\n" \
"v_bitop3_b32 v12, v244, s24, 1 bitop3:0x28\n" \
"v_cmp_ne_u32 vcc, 0, v12\n" \
"v_mfma_f32_16x16x32_bf16 v[32:35], v[192:195], v[240:243], v[32:35]\n" \
"s_cbranch_vccnz .Ll542_%=\n" \
".Ll543_%=:\n" \
"v_mfma_f32_16x16x32_bf16 v[16:19], v[196:199], v[244:247], v[16:19]\n" \
"v_mfma_f32_16x16x32_bf16 v[20:23], v[200:203], v[244:247], v[20:23]\n" \
"v_mfma_f32_16x16x32_bf16 v[24:27], v[204:207], v[244:247], v[24:27]\n" \
"v_mfma_f32_16x16x32_bf16 v[28:31], v[208:211], v[244:247], v[28:31]\n" \
"s_waitcnt vmcnt(7)\n" \
"v_bitop3_b32 v12, v248, s24, 1 bitop3:0x28\n" \
"v_cmp_ne_u32 vcc, 0, v12\n" \
"s_nop 1\n" \
"v_cvt_pk_bf16_f32 v16, v16, v17\n" \
"v_cvt_pk_bf16_f32 v17, v18, v19\n" \
"ds_write_b64 v6, v[16:17] offset:0\n" \
"v_mfma_f32_16x16x32_bf16 v[32:35], v[212:215], v[244:247], v[32:35]\n" \
"s_cbranch_vccnz .Ll544_%=\n" \
".Ll545_%=:\n" \
"s_waitcnt lgkmcnt(0)\n" \
"s_barrier\n" \
"s_setprio 1\n" \
"s_and_b32 s25, s38, 4294926336\n" \
"s_add_u32 s25, s25, s39\n" \
"s_add_u32 m0, s25, 81920\n" \
"s_and_b32 s25, s38, 10240\n" \
"s_add_u32 s25, s25, s37\n" \
"s_add_u32 s26, s25, 194560\n" \
"buffer_load_dwordx4 v1, %0, s26 offen offset:0 sc1 lds\n" \
"v_mfma_f32_16x16x32_bf16 v[36:39], v[176:179], v[248:251], v[36:39]\n" \
"v_cvt_pk_bf16_f32 v20, v20, v21\n" \
"v_cvt_pk_bf16_f32 v21, v22, v23\n" \
"ds_write_b64 v6, v[20:21] offset:2048\n" \
"v_mfma_f32_16x16x32_bf16 v[40:43], v[180:183], v[248:251], v[40:43]\n" \
"v_mfma_f32_16x16x32_bf16 v[44:47], v[184:187], v[248:251], v[44:47]\n" \
"v_cvt_pk_bf16_f32 v24, v24, v25\n" \
"v_cvt_pk_bf16_f32 v25, v26, v27\n" \
"ds_write_b64 v6, v[24:25] offset:4096\n" \
"v_mfma_f32_16x16x32_bf16 v[48:51], v[188:191], v[248:251], v[48:51]\n" \
"s_waitcnt vmcnt(6)\n" \
"v_bitop3_b32 v12, v252, s24, 1 bitop3:0x28\n" \
"v_cmp_ne_u32 vcc, 0, v12\n" \
"buffer_load_dwordx4 v1, %0, s26 offen offset:1024 sc1 lds\n" \
"v_mfma_f32_16x16x32_bf16 v[52:55], v[192:195], v[248:251], v[52:55]\n" \
"s_cbranch_vccnz .Ll546_%=\n" \
".Ll547_%=:\n" \
"v_cvt_pk_bf16_f32 v28, v28, v29\n" \
"v_cvt_pk_bf16_f32 v29, v30, v31\n" \
"ds_write_b64 v6, v[28:29] offset:6144\n" \
"v_mfma_f32_16x16x32_bf16 v[36:39], v[196:199], v[252:255], v[36:39]\n" \
"v_mfma_f32_16x16x32_bf16 v[40:43], v[200:203], v[252:255], v[40:43]\n" \
"v_cvt_pk_bf16_f32 v32, v32, v33\n" \
"v_cvt_pk_bf16_f32 v33, v34, v35\n" \
"ds_write_b64 v6, v[32:33] offset:8192\n" \
"v_mfma_f32_16x16x32_bf16 v[44:47], v[204:207], v[252:255], v[44:47]\n" \
"buffer_load_dwordx4 v1, %0, s26 offen offset:2048 sc1 lds\n" \
"v_mfma_f32_16x16x32_bf16 v[48:51], v[208:211], v[252:255], v[48:51]\n" \
"s_waitcnt vmcnt(6)\n" \
"v_bitop3_b32 v12, v216, s24, 1 bitop3:0x28\n" \
"v_cmp_ne_u32 vcc, 0, v12\n" \
"v_cvt_pk_bf16_f32 v36, v36, v37\n" \
"v_cvt_pk_bf16_f32 v37, v38, v39\n" \
"ds_write_b64 v7, v[36:37] offset:0\n" \
"v_mfma_f32_16x16x32_bf16 v[52:55], v[212:215], v[252:255], v[52:55]\n" \
"s_cbranch_vccnz .Ll548_%=\n" \
".Ll549_%=:\n" \
"v_mfma_f32_16x16x32_bf16 v[56:59], v[176:179], v[216:219], v[56:59]\n" \
"v_cvt_pk_bf16_f32 v40, v40, v41\n" \
"v_cvt_pk_bf16_f32 v41, v42, v43\n" \
"ds_write_b64 v7, v[40:41] offset:2048\n" \
"v_mfma_f32_16x16x32_bf16 v[60:63], v[180:183], v[216:219], v[60:63]\n" \
"buffer_load_dwordx4 v1, %0, s26 offen offset:3072 sc1 lds\n" \
"v_mfma_f32_16x16x32_bf16 v[64:67], v[184:187], v[216:219], v[64:67]\n" \
"v_cvt_pk_bf16_f32 v44, v44, v45\n" \
"v_cvt_pk_bf16_f32 v45, v46, v47\n" \
"ds_write_b64 v7, v[44:45] offset:4096\n" \
"v_mfma_f32_16x16x32_bf16 v[68:71], v[188:191], v[216:219], v[68:71]\n" \
"s_waitcnt vmcnt(6)\n" \
"v_bitop3_b32 v12, v220, s24, 1 bitop3:0x28\n" \
"v_cmp_ne_u32 vcc, 0, v12\n" \
"v_mfma_f32_16x16x32_bf16 v[72:75], v[192:195], v[216:219], v[72:75]\n" \
"s_cbranch_vccnz .Ll550_%=\n" \
".Ll551_%=:\n" \
"v_cvt_pk_bf16_f32 v48, v48, v49\n" \
"v_cvt_pk_bf16_f32 v49, v50, v51\n" \
"ds_write_b64 v7, v[48:49] offset:6144\n" \
"v_mfma_f32_16x16x32_bf16 v[56:59], v[196:199], v[220:223], v[56:59]\n" \
"s_add_u32 m0, m0, 4096\n" \
"s_nop 0\n" \
"buffer_load_dwordx4 v2, %0, s26 offen sc1 lds\n" \
"v_mfma_f32_16x16x32_bf16 v[60:63], v[200:203], v[220:223], v[60:63]\n" \
"v_cvt_pk_bf16_f32 v52, v52, v53\n" \
"v_cvt_pk_bf16_f32 v53, v54, v55\n" \
"ds_write_b64 v7, v[52:53] offset:8192\n" \
"v_mfma_f32_16x16x32_bf16 v[64:67], v[204:207], v[220:223], v[64:67]\n" \
"v_mfma_f32_16x16x32_bf16 v[68:71], v[208:211], v[220:223], v[68:71]\n" \
"s_waitcnt vmcnt(6)\n" \
"v_bitop3_b32 v12, v224, s24, 1 bitop3:0x28\n" \
"v_cmp_ne_u32 vcc, 0, v12\n" \
"v_cvt_pk_bf16_f32 v56, v56, v57\n" \
"v_cvt_pk_bf16_f32 v57, v58, v59\n" \
"ds_write_b64 v8, v[56:57] offset:0\n" \
"v_mfma_f32_16x16x32_bf16 v[72:75], v[212:215], v[220:223], v[72:75]\n" \
"s_cbranch_vccnz .Ll552_%=\n" \
".Ll553_%=:\n" \
"s_and_b32 s25, s38, 10240\n" \
"s_add_u32 s25, s25, s39\n" \
"s_add_u32 m0, s25, 51200\n" \
"s_and_b32 s25, s38, 10240\n" \
"s_add_u32 s25, s25, s37\n" \
"s_add_u32 s26, s25, 215040\n" \
"buffer_load_dwordx4 v1, %0, s26 offen offset:0 sc1 lds\n" \
"v_mfma_f32_16x16x32_bf16 v[76:79], v[176:179], v[224:227], v[76:79]\n" \
"v_cvt_pk_bf16_f32 v60, v60, v61\n" \
"v_cvt_pk_bf16_f32 v61, v62, v63\n" \
"ds_write_b64 v8, v[60:61] offset:2048\n" \
"v_mfma_f32_16x16x32_bf16 v[80:83], v[180:183], v[224:227], v[80:83]\n" \
"v_mfma_f32_16x16x32_bf16 v[84:87], v[184:187], v[224:227], v[84:87]\n" \
"v_cvt_pk_bf16_f32 v64, v64, v65\n" \
"v_cvt_pk_bf16_f32 v65, v66, v67\n" \
"ds_write_b64 v8, v[64:65] offset:4096\n" \
"v_mfma_f32_16x16x32_bf16 v[88:91], v[188:191], v[224:227], v[88:91]\n" \
"s_waitcnt vmcnt(6)\n" \
"v_bitop3_b32 v12, v228, s24, 1 bitop3:0x28\n" \
"v_cmp_ne_u32 vcc, 0, v12\n" \
"buffer_load_dwordx4 v1, %0, s26 offen offset:1024 sc1 lds\n" \
"v_mfma_f32_16x16x32_bf16 v[92:95], v[192:195], v[224:227], v[92:95]\n" \
"s_cbranch_vccnz .Ll554_%=\n" \
".Ll555_%=:\n" \
"v_cvt_pk_bf16_f32 v68, v68, v69\n" \
"v_cvt_pk_bf16_f32 v69, v70, v71\n" \
"ds_write_b64 v8, v[68:69] offset:6144\n" \
"v_mfma_f32_16x16x32_bf16 v[76:79], v[196:199], v[228:231], v[76:79]\n" \
"v_mfma_f32_16x16x32_bf16 v[80:83], v[200:203], v[228:231], v[80:83]\n" \
"v_cvt_pk_bf16_f32 v72, v72, v73\n" \
"v_cvt_pk_bf16_f32 v73, v74, v75\n" \
"ds_write_b64 v8, v[72:73] offset:8192\n" \
"v_mfma_f32_16x16x32_bf16 v[84:87], v[204:207], v[228:231], v[84:87]\n" \
"buffer_load_dwordx4 v1, %0, s26 offen offset:2048 sc1 lds\n" \
"v_mfma_f32_16x16x32_bf16 v[88:91], v[208:211], v[228:231], v[88:91]\n" \
"s_nop 0\n" \
"v_cvt_pk_bf16_f32 v76, v76, v77\n" \
"v_cvt_pk_bf16_f32 v77, v78, v79\n" \
"ds_write_b64 v9, v[76:77] offset:0\n" \
"v_mfma_f32_16x16x32_bf16 v[92:95], v[212:215], v[228:231], v[92:95]\n" \
"buffer_load_dwordx4 v1, %0, s26 offen offset:3072 sc1 lds\n" \
"s_add_u32 m0, m0, 4096\n" \
"s_nop 0\n" \
"buffer_load_dwordx4 v2, %0, s26 offen sc1 lds\n" \
"s_waitcnt lgkmcnt(0)\n" \
"s_barrier\n" \
"s_and_b32 s25, s38, 30720\n" \
"s_add_u32 s25, s25, s39\n" \
"s_add_u32 m0, s25, 71680\n" \
"s_and_b32 s25, s38, 10240\n" \
"s_add_u32 s25, s25, s37\n" \
"s_add_u32 s26, s25, 235520\n" \
"buffer_load_dwordx4 v1, %0, s26 offen offset:0 sc1 lds\n" \
"v_cvt_pk_bf16_f32 v80, v80, v81\n" \
"v_cvt_pk_bf16_f32 v81, v82, v83\n" \
"ds_write_b64 v9, v[80:81] offset:2048\n" \
"v_cvt_pk_bf16_f32 v84, v84, v85\n" \
"v_cvt_pk_bf16_f32 v85, v86, v87\n" \
"ds_write_b64 v9, v[84:85] offset:4096\n" \
"buffer_load_dwordx4 v1, %0, s26 offen offset:1024 sc1 lds\n" \
"v_cvt_pk_bf16_f32 v88, v88, v89\n" \
"v_cvt_pk_bf16_f32 v89, v90, v91\n" \
"ds_write_b64 v9, v[88:89] offset:6144\n" \
"v_cvt_pk_bf16_f32 v92, v92, v93\n" \
"v_cvt_pk_bf16_f32 v93, v94, v95\n" \
"ds_write_b64 v9, v[92:93] offset:8192\n" \
"s_setprio 0\n" \
"buffer_load_dwordx4 v1, %0, s26 offen offset:2048 sc1 lds\n" \
"buffer_load_dwordx4 v1, %0, s26 offen offset:3072 sc1 lds\n" \
"s_add_u32 m0, m0, 4096\n" \
"s_nop 0\n" \
"buffer_load_dwordx4 v2, %0, s26 offen sc1 lds\n" \
"s_waitcnt lgkmcnt(0)\n" \
"s_barrier\n" \
"s_and_b32 s25, s38, 10240\n" \
"s_add_u32 s25, s25, s39\n" \
"s_add_u32 m0, s25, 122880\n" \
"s_and_b32 s25, s38, 10240\n" \
"s_add_u32 s25, s25, s37\n" \
"s_add_u32 s26, s25, 256000\n" \
"buffer_load_dwordx4 v1, %0, s26 offen offset:0 sc1 lds\n" \
"buffer_load_dwordx4 v1, %0, s26 offen offset:1024 sc1 lds\n" \
"buffer_load_dwordx4 v1, %0, s26 offen offset:2048 sc1 lds\n" \
"buffer_load_dwordx4 v1, %0, s26 offen offset:3072 sc1 lds\n" \
"s_add_u32 m0, m0, 4096\n" \
"s_nop 0\n" \
"buffer_load_dwordx4 v2, %0, s26 offen sc1 lds\n" \
"s_waitcnt vmcnt(10)\n" \
"s_barrier\n" \
"s_waitcnt vmcnt(0)\n" \
"s_barrier\n" \
"s_cmp_lg_u32 s24, 0\n" \
"s_cbranch_scc1 .Ll556_%=\n" \
"s_and_b32 s25, s38, 4294916096\n" \
"s_add_u32 s25, s25, s39\n" \
"s_add_u32 m0, s25, 143360\n" \
"s_and_b32 s25, s38, 81920\n" \
"s_add_u32 s25, s25, s34\n" \
"s_add_u32 s26, s25, 0\n" \
"buffer_load_dwordx4 v1, %0, s26 offen offset:0 sc1 lds\n" \
"buffer_load_dwordx4 v1, %0, s26 offen offset:1024 sc1 lds\n" \
"buffer_load_dwordx4 v1, %0, s26 offen offset:2048 sc1 lds\n" \
"buffer_load_dwordx4 v1, %0, s26 offen offset:3072 sc1 lds\n" \
"s_add_u32 m0, m0, 4096\n" \
"s_nop 0\n" \
"buffer_load_dwordx4 v2, %0, s26 offen sc1 lds\n" \
"s_add_u32 m0, s40, 81920\n" \
"s_add_u32 s26, s40, s34\n" \
"s_add_u32 s26, s26, 10240\n" \
"buffer_load_dwordx4 v0, %0, s26 offen sc1 lds\n" \
"buffer_load_dwordx4 v0, %0, s26 offen offset:1024 sc1 lds\n" \
"s_mov_b32 exec_hi, 0\n" \
"buffer_load_dwordx4 v0, %0, s26 offen offset:2048 sc1 lds\n" \
"s_mov_b32 exec_hi, -1\n" \
".Ll556_%=:\n" \
"s_barrier\n" \
"s_cmp_lg_u32 s24, 0\n" \
"s_cbranch_scc1 .Ll557_%=\n" \
"s_and_b32 s25, s38, 10240\n" \
"s_add_u32 s25, s25, s39\n" \
"s_add_u32 m0, s25, 0\n" \
"s_and_b32 s25, s38, 10240\n" \
"s_add_u32 s25, s25, s34\n" \
"s_add_u32 s26, s25, 112640\n" \
"buffer_load_dwordx4 v1, %0, s26 offen offset:0 sc1 lds\n" \
"buffer_load_dwordx4 v1, %0, s26 offen offset:1024 sc1 lds\n" \
"buffer_load_dwordx4 v1, %0, s26 offen offset:2048 sc1 lds\n" \
"buffer_load_dwordx4 v1, %0, s26 offen offset:3072 sc1 lds\n" \
"s_add_u32 m0, m0, 4096\n" \
"s_nop 0\n" \
"buffer_load_dwordx4 v2, %0, s26 offen sc1 lds\n" \
"s_and_b32 s25, s38, 10240\n" \
"s_add_u32 s25, s25, s39\n" \
"s_add_u32 m0, s25, 20480\n" \
"s_and_b32 s25, s38, 10240\n" \
"s_add_u32 s25, s25, s34\n" \
"s_add_u32 s26, s25, 133120\n" \
"buffer_load_dwordx4 v1, %0, s26 offen offset:0 sc1 lds\n" \
"buffer_load_dwordx4 v1, %0, s26 offen offset:1024 sc1 lds\n" \
"buffer_load_dwordx4 v1, %0, s26 offen offset:2048 sc1 lds\n" \
"buffer_load_dwordx4 v1, %0, s26 offen offset:3072 sc1 lds\n" \
"s_add_u32 m0, m0, 4096\n" \
"s_nop 0\n" \
"buffer_load_dwordx4 v2, %0, s26 offen sc1 lds\n" \
"s_waitcnt vmcnt(5)\n" \
"s_barrier\n" \
"s_and_b32 s25, s38, 10240\n" \
"s_add_u32 s25, s25, s39\n" \
"s_add_u32 m0, s25, 102400\n" \
"s_and_b32 s25, s38, 10240\n" \
"s_add_u32 s25, s25, s34\n" \
"s_add_u32 s26, s25, 92160\n" \
"buffer_load_dwordx4 v1, %0, s26 offen offset:0 sc1 lds\n" \
"buffer_load_dwordx4 v1, %0, s26 offen offset:1024 sc1 lds\n" \
"buffer_load_dwordx4 v1, %0, s26 offen offset:2048 sc1 lds\n" \
"buffer_load_dwordx4 v1, %0, s26 offen offset:3072 sc1 lds\n" \
"s_add_u32 m0, m0, 4096\n" \
"s_nop 0\n" \
"buffer_load_dwordx4 v2, %0, s26 offen sc1 lds\n" \
"s_and_b32 s25, s38, 10240\n" \
"s_add_u32 s25, s25, s39\n" \
"s_add_u32 m0, s25, 122880\n" \
"s_and_b32 s25, s38, 10240\n" \
"s_add_u32 s25, s25, s34\n" \
"s_add_u32 s26, s25, 20480\n" \
"buffer_load_dwordx4 v1, %0, s26 offen offset:0 sc1 lds\n" \
"buffer_load_dwordx4 v1, %0, s26 offen offset:1024 sc1 lds\n" \
"buffer_load_dwordx4 v1, %0, s26 offen offset:2048 sc1 lds\n" \
"buffer_load_dwordx4 v1, %0, s26 offen offset:3072 sc1 lds\n" \
"s_add_u32 m0, m0, 4096\n" \
"s_nop 0\n" \
"buffer_load_dwordx4 v2, %0, s26 offen sc1 lds\n" \
"v_bfe_u32 v28, v0, 4, 5\n" \
"v_add_u32 v8, 20, v28\n" \
"v_add_u32 v29, -16, v28\n" \
"v_cmp_gt_u32 vcc, 16, v28\n" \
"s_nop 1\n" \
"v_cndmask_b32 v8, v29, v8, vcc\n" \
"v_cmp_gt_u32 vcc, 20, v28\n" \
"s_nop 1\n" \
"v_cndmask_b32 v8, 40, v8, vcc\n" \
"v_subrev_u32 v29, 12, v29\n" \
"v_cmp_gt_u32 vcc, 28, v28\n" \
"s_nop 1\n" \
"v_cndmask_b32 v8, v29, v8, vcc\n" \
"v_lshrrev_b32 v28, 9, v0\n" \
"v_mad_u32_u24 v8, v28, 41, v8\n" \
"v_lshl_add_u32 v8, v8, 4, s23\n" \
"v_and_b32 v10, 0xf0, v0\n" \
"v_lshl_add_u32 v9, v28, 8, v10\n" \
"v_lshl_add_u32 v10, v28, 31, v10\n" \
"v_bfe_u32 v28, v0, 8, 1\n" \
"v_lshl_add_u32 v9, v28, 14, v9\n" \
"v_lshl_add_u32 v10, v28, 12, v10\n" \
"v_add_u32 v46, s67, v0\n" \
"v_add_u32 v6, 1024, v46\n" \
"v_lshrrev_b32 v7, 1, v0\n" \
"v_add_u32 v7, s67, v7\n" \
"v_add_u32 v11, s66, v0\n" \
"v_bfe_u32 v12, v0, 1, 8\n" \
"v_add_u32 v12, s66, v12\n" \
"v_add_u32 v12, 1024, v12\n" \
"v_lshrrev_b32 v29, 9, v0\n" \
"v_mul_u32_u24 v29, 144, v29\n" \
"v_add3_u32 v3, v29, v0, s23\n" \
"v_lshrrev_b32 v29, 4, v0\n" \
"v_min_u32 v29, 47, v29\n" \
"v_mul_u32_u24 v31, 656, v29\n" \
"v_add_u32 v31, 123520, v31\n" \
"s_lshl_b32 s26, %2, 4\n" \
"v_add_u32 v29, s26, v29\n" \
"v_cmp_lt_u32 vcc, 31, v29\n" \
"v_mov_b32 v29, 0x3f803f80\n" \
"s_nop 0\n" \
"v_cndmask_b32 v40, 0, v29, vcc\n" \
"v_mov_b32 v41, v40\n" \
"v_mov_b32 v42, v40\n" \
"v_mov_b32 v43, v40\n" \
"v_bfe_u32 v47, v0, 8, 1\n" \
"v_bfe_u32 v29, v0, 6, 1\n" \
"v_lshl_add_u32 v47, v47, 1, v29\n" \
"v_mul_u32_u24 v47, 656, v47\n" \
"v_bfe_u32 v29, v0, 3, 3\n" \
"v_bfe_u32 v30, v0, 7, 1\n" \
"v_lshl_add_u32 v29, v30, 3, v29\n" \
"v_add3_u32 v47, v47, v29, s23\n" \
"v_add_u32 v29, 576, v47\n" \
"v_cndmask_b32 v47, v47, v29, s[30:31]\n" \
"s_or_b32 s25, s48, 0x200\n" \
"s_andn2_b32 s26, 2147483648, s48\n" \
"v_and_b32 v44, s25, v0\n" \
"v_or_b32 v13, s26, v0\n" \
"s_or_b32 s25, s49, 0x200\n" \
"s_andn2_b32 s26, 2147483648, s49\n" \
"v_and_b32 v45, s25, v0\n" \
"v_or_b32 v14, s26, v0\n" \
"s_waitcnt vmcnt(5)\n" \
"s_barrier\n" \
"s_waitcnt vmcnt(0)\n" \
"s_barrier\n" \
"v_lshrrev_b32 v30, 9, v0\n" \
"v_add3_u32 v30, %2, 1, v30\n" \
"v_min_u32 v30, 15, v30\n" \
"v_lshlrev_b32 v30, 4, v30\n" \
"s_mul_i32 s26, %3, 256\n" \
"s_mul_i32 s25, s27, 4\n" \
"s_add_u32 s26, s26, s25\n" \
"s_add_u32 s26, s26, 27774976\n" \
"buffer_load_dword v29, v30, %0, s26 offen sc1\n" \
"s_waitcnt vmcnt(0)\n" \
"v_cmp_ne_u32 vcc, %5, v29\n" \
"s_cbranch_vccnz .Ll558_%=\n" \
".Ll559_%=:\n" \
"s_barrier\n" \
"s_and_b32 s25, s38, 10240\n" \
"s_add_u32 s25, s25, s39\n" \
"s_add_u32 m0, s25, 40960\n" \
"s_and_b32 s25, s38, 10240\n" \
"s_add_u32 s25, s25, s34\n" \
"s_add_u32 s26, s25, 40960\n" \
"buffer_load_dwordx4 v1, %0, s26 offen offset:0 sc1 lds\n" \
"buffer_load_dwordx4 v1, %0, s26 offen offset:1024 sc1 lds\n" \
"buffer_load_dwordx4 v1, %0, s26 offen offset:2048 sc1 lds\n" \
"buffer_load_dwordx4 v1, %0, s26 offen offset:3072 sc1 lds\n" \
"s_add_u32 m0, m0, 4096\n" \
"s_nop 0\n" \
"buffer_load_dwordx4 v2, %0, s26 offen sc1 lds\n" \
"s_and_b32 s25, s38, 10240\n" \
"s_add_u32 s25, s25, s39\n" \
"s_add_u32 m0, s25, 61440\n" \
"s_and_b32 s25, s38, 10240\n" \
"s_add_u32 s25, s25, s34\n" \
"s_add_u32 s26, s25, 61440\n" \
"buffer_load_dwordx4 v1, %0, s26 offen offset:0 sc1 lds\n" \
"buffer_load_dwordx4 v1, %0, s26 offen offset:1024 sc1 lds\n" \
"buffer_load_dwordx4 v1, %0, s26 offen offset:2048 sc1 lds\n" \
"buffer_load_dwordx4 v1, %0, s26 offen offset:3072 sc1 lds\n" \
"s_add_u32 m0, m0, 4096\n" \
"s_nop 0\n" \
"buffer_load_dwordx4 v2, %0, s26 offen sc1 lds\n" \
"s_waitcnt vmcnt(5)\n" \
"s_barrier\n" \
"ds_write_b128 v31, v[40:43]\n" \
"s_and_b32 s25, s38, 4294885376\n" \
"s_add_u32 s25, s25, s39\n" \
"s_add_u32 m0, s25, 81920\n" \
"s_and_b32 s25, s38, 10240\n" \
"s_add_u32 s25, s25, s35\n" \
"s_add_u32 s26, s25, 0\n" \
"buffer_load_dwordx4 v1, %0, s26 offen offset:0 sc1 lds\n" \
"buffer_load_dwordx4 v1, %0, s26 offen offset:1024 sc1 lds\n" \
"buffer_load_dwordx4 v1, %0, s26 offen offset:2048 sc1 lds\n" \
"buffer_load_dwordx4 v1, %0, s26 offen offset:3072 sc1 lds\n" \
"s_add_u32 m0, m0, 4096\n" \
"s_nop 0\n" \
"buffer_load_dwordx4 v2, %0, s26 offen sc1 lds\n" \
"s_waitcnt vmcnt(5) lgkmcnt(0)\n" \
"s_barrier\n" \
"s_and_b32 s25, s38, 10240\n" \
"s_add_u32 s25, s25, s39\n" \
"s_add_u32 m0, s25, 10240\n" \
"s_and_b32 s25, s38, 10240\n" \
"s_add_u32 s25, s25, s35\n" \
"s_add_u32 s26, s25, 20480\n" \
"buffer_load_dwordx4 v1, %0, s26 offen offset:0 sc1 lds\n" \
"buffer_load_dwordx4 v1, %0, s26 offen offset:1024 sc1 lds\n" \
"buffer_load_dwordx4 v1, %0, s26 offen offset:2048 sc1 lds\n" \
"buffer_load_dwordx4 v1, %0, s26 offen offset:3072 sc1 lds\n" \
"s_add_u32 m0, m0, 4096\n" \
"s_nop 0\n" \
"buffer_load_dwordx4 v2, %0, s26 offen sc1 lds\n" \
"s_add_u32 m0, s40, 30720\n" \
"s_add_u32 s26, s40, s35\n" \
"s_add_u32 s26, s26, 40960\n" \
"buffer_load_dwordx4 v0, %0, s26 offen sc1 lds\n" \
"buffer_load_dwordx4 v0, %0, s26 offen offset:1024 sc1 lds\n" \
"s_mov_b32 exec_hi, 0\n" \
"buffer_load_dwordx4 v0, %0, s26 offen offset:2048 sc1 lds\n" \
"s_mov_b32 exec_hi, -1\n" \
"s_sleep 4\n" \
"s_mov_b32 s24, 1\n" \
"s_branch .Llloop_%=\n" \
".Ll557_%=:\n" \
"s_branch .Lend_%=\n" \
".Lk_last:\n" \
".Ld1_%=:\n" \
"buffer_load_dword v13, v12, %0, s26 offen sc1\n" \
"s_memrealtime s[28:29]\n" \
"s_waitcnt vmcnt(0) lgkmcnt(0)\n" \
"s_sub_u32 s28, s28, s20\n" \
"s_cmp_gt_u32 s28, 20000000\n" \
"s_cbranch_scc1 .Ld2_%=\n" \
"v_cmp_ne_u32 vcc, %5, v13\n" \
"s_cbranch_vccnz .Ld1_%=\n" \
"s_branch .Ld2_%=\n" \
".Lc3_%=:\n" \
"buffer_load_dwordx4 v[96:99], v3, %0, s34 offen offset:2048 sc1\n" \
"buffer_load_dwordx4 v[100:103], v3, %0, s35 offen offset:512 sc1\n" \
"buffer_load_dwordx4 v[104:107], v3, %0, s35 offen offset:3072 sc1\n" \
"buffer_load_dwordx4 v[108:111], v3, %0, s36 offen offset:1536 sc1\n" \
"buffer_load_dwordx4 v[112:115], v3, %0, s37 offen offset:0 sc1\n" \
"buffer_load_dwordx4 v[116:119], v3, %0, s37 offen offset:2560 sc1\n" \
"buffer_load_dwordx4 v[120:123], v3, %0, s38 offen offset:1024 sc1\n" \
"buffer_load_dwordx4 v[124:127], v3, %0, s38 offen offset:3584 sc1\n" \
"buffer_load_dwordx4 v[128:131], v3, %0, s34 offen offset:1536 sc1\n" \
"buffer_load_dwordx4 v[132:135], v3, %0, s35 offen offset:0 sc1\n" \
"s_memrealtime s[28:29]\n" \
"s_waitcnt vmcnt(0) lgkmcnt(0)\n" \
"s_sub_u32 s28, s28, s20\n" \
"s_cmp_gt_u32 s28, 20000000\n" \
"s_cbranch_scc1 .Lc4_%=\n" \
"v_bitop3_b32 v12, v96, v100, 1 bitop3:168\n" \
"v_cmp_ne_u32 vcc, 0, v12\n" \
"s_cbranch_vccnz .Lc3_%=\n" \
"s_branch .Lc4_%=\n" \
".Lc5_%=:\n" \
"buffer_load_dwordx4 v[104:107], v3, %0, s35 offen offset:3072 sc1\n" \
"buffer_load_dwordx4 v[108:111], v3, %0, s36 offen offset:1536 sc1\n" \
"buffer_load_dwordx4 v[112:115], v3, %0, s37 offen offset:0 sc1\n" \
"buffer_load_dwordx4 v[116:119], v3, %0, s37 offen offset:2560 sc1\n" \
"buffer_load_dwordx4 v[120:123], v3, %0, s38 offen offset:1024 sc1\n" \
"buffer_load_dwordx4 v[124:127], v3, %0, s38 offen offset:3584 sc1\n" \
"buffer_load_dwordx4 v[128:131], v3, %0, s34 offen offset:1536 sc1\n" \
"buffer_load_dwordx4 v[132:135], v3, %0, s35 offen offset:0 sc1\n" \
"buffer_load_dwordx4 v[136:139], v3, %0, s35 offen offset:2560 sc1\n" \
"buffer_load_dwordx4 v[140:143], v3, %0, s36 offen offset:1024 sc1\n" \
"s_memrealtime s[28:29]\n" \
"s_waitcnt vmcnt(0) lgkmcnt(0)\n" \
"s_sub_u32 s28, s28, s20\n" \
"s_cmp_gt_u32 s28, 20000000\n" \
"s_cbranch_scc1 .Lc6_%=\n" \
"v_bitop3_b32 v12, v104, v108, 1 bitop3:168\n" \
"v_cmp_ne_u32 vcc, 0, v12\n" \
"s_cbranch_vccnz .Lc5_%=\n" \
"s_branch .Lc6_%=\n" \
".Lc7_%=:\n" \
"buffer_load_dwordx4 v[112:115], v3, %0, s37 offen offset:0 sc1\n" \
"buffer_load_dwordx4 v[116:119], v3, %0, s37 offen offset:2560 sc1\n" \
"buffer_load_dwordx4 v[120:123], v3, %0, s38 offen offset:1024 sc1\n" \
"buffer_load_dwordx4 v[124:127], v3, %0, s38 offen offset:3584 sc1\n" \
"buffer_load_dwordx4 v[128:131], v3, %0, s34 offen offset:1536 sc1\n" \
"buffer_load_dwordx4 v[132:135], v3, %0, s35 offen offset:0 sc1\n" \
"buffer_load_dwordx4 v[136:139], v3, %0, s35 offen offset:2560 sc1\n" \
"buffer_load_dwordx4 v[140:143], v3, %0, s36 offen offset:1024 sc1\n" \
"buffer_load_dwordx4 v[144:147], v3, %0, s36 offen offset:3584 sc1\n" \
"buffer_load_dwordx4 v[148:151], v3, %0, s37 offen offset:2048 sc1\n" \
"buffer_load_dwordx4 v[152:155], v3, %0, s38 offen offset:512 sc1\n" \
"s_memrealtime s[28:29]\n" \
"s_waitcnt vmcnt(0) lgkmcnt(0)\n" \
"s_sub_u32 s28, s28, s20\n" \
"s_cmp_gt_u32 s28, 20000000\n" \
"s_cbranch_scc1 .Lc8_%=\n" \
"v_bitop3_b32 v12, v112, v116, 1 bitop3:168\n" \
"v_cmp_ne_u32 vcc, 0, v12\n" \
"s_cbranch_vccnz .Lc7_%=\n" \
"s_branch .Lc8_%=\n" \
".Lc9_%=:\n" \
"buffer_load_dwordx4 v[120:123], v3, %0, s38 offen offset:1024 sc1\n" \
"buffer_load_dwordx4 v[124:127], v3, %0, s38 offen offset:3584 sc1\n" \
"buffer_load_dwordx4 v[128:131], v3, %0, s34 offen offset:1536 sc1\n" \
"buffer_load_dwordx4 v[132:135], v3, %0, s35 offen offset:0 sc1\n" \
"buffer_load_dwordx4 v[136:139], v3, %0, s35 offen offset:2560 sc1\n" \
"buffer_load_dwordx4 v[140:143], v3, %0, s36 offen offset:1024 sc1\n" \
"buffer_load_dwordx4 v[144:147], v3, %0, s36 offen offset:3584 sc1\n" \
"buffer_load_dwordx4 v[148:151], v3, %0, s37 offen offset:2048 sc1\n" \
"buffer_load_dwordx4 v[152:155], v3, %0, s38 offen offset:512 sc1\n" \
"buffer_load_dwordx4 v[156:159], v3, %0, s38 offen offset:3072 sc1\n" \
"buffer_load_dwordx4 v[160:163], v3, %0, s34 offen offset:1024 sc1\n" \
"buffer_load_dwordx4 v[164:167], v3, %0, s34 offen offset:3584 sc1\n" \
"s_memrealtime s[28:29]\n" \
"s_waitcnt vmcnt(0) lgkmcnt(0)\n" \
"s_sub_u32 s28, s28, s20\n" \
"s_cmp_gt_u32 s28, 20000000\n" \
"s_cbranch_scc1 .Lc10_%=\n" \
"v_bitop3_b32 v12, v120, v124, 1 bitop3:168\n" \
"v_cmp_ne_u32 vcc, 0, v12\n" \
"s_cbranch_vccnz .Lc9_%=\n" \
"s_branch .Lc10_%=\n" \
".Lc11_%=:\n" \
"buffer_load_dwordx4 v[128:131], v3, %0, s34 offen offset:1536 sc1\n" \
"buffer_load_dwordx4 v[132:135], v3, %0, s35 offen offset:0 sc1\n" \
"buffer_load_dwordx4 v[136:139], v3, %0, s35 offen offset:2560 sc1\n" \
"buffer_load_dwordx4 v[140:143], v3, %0, s36 offen offset:1024 sc1\n" \
"buffer_load_dwordx4 v[144:147], v3, %0, s36 offen offset:3584 sc1\n" \
"buffer_load_dwordx4 v[148:151], v3, %0, s37 offen offset:2048 sc1\n" \
"buffer_load_dwordx4 v[152:155], v3, %0, s38 offen offset:512 sc1\n" \
"buffer_load_dwordx4 v[156:159], v3, %0, s38 offen offset:3072 sc1\n" \
"buffer_load_dwordx4 v[160:163], v3, %0, s34 offen offset:1024 sc1\n" \
"buffer_load_dwordx4 v[164:167], v3, %0, s34 offen offset:3584 sc1\n" \
"buffer_load_dwordx4 v[168:171], v3, %0, s35 offen offset:2048 sc1\n" \
"buffer_load_dwordx4 v[172:175], v3, %0, s36 offen offset:512 sc1\n" \
"buffer_load_dwordx4 v[176:179], v3, %0, s36 offen offset:3072 sc1\n" \
"s_memrealtime s[28:29]\n" \
"s_waitcnt vmcnt(0) lgkmcnt(0)\n" \
"s_sub_u32 s28, s28, s20\n" \
"s_cmp_gt_u32 s28, 20000000\n" \
"s_cbranch_scc1 .Lc12_%=\n" \
"v_bitop3_b32 v12, v128, v132, 1 bitop3:168\n" \
"v_cmp_ne_u32 vcc, 0, v12\n" \
"s_cbranch_vccnz .Lc11_%=\n" \
"s_branch .Lc12_%=\n" \
".Lc13_%=:\n" \
"buffer_load_dwordx4 v[136:139], v3, %0, s35 offen offset:2560 sc1\n" \
"buffer_load_dwordx4 v[140:143], v3, %0, s36 offen offset:1024 sc1\n" \
"buffer_load_dwordx4 v[144:147], v3, %0, s36 offen offset:3584 sc1\n" \
"buffer_load_dwordx4 v[148:151], v3, %0, s37 offen offset:2048 sc1\n" \
"buffer_load_dwordx4 v[152:155], v3, %0, s38 offen offset:512 sc1\n" \
"buffer_load_dwordx4 v[156:159], v3, %0, s38 offen offset:3072 sc1\n" \
"buffer_load_dwordx4 v[160:163], v3, %0, s34 offen offset:1024 sc1\n" \
"buffer_load_dwordx4 v[164:167], v3, %0, s34 offen offset:3584 sc1\n" \
"buffer_load_dwordx4 v[168:171], v3, %0, s35 offen offset:2048 sc1\n" \
"buffer_load_dwordx4 v[172:175], v3, %0, s36 offen offset:512 sc1\n" \
"buffer_load_dwordx4 v[176:179], v3, %0, s36 offen offset:3072 sc1\n" \
"buffer_load_dwordx4 v[180:183], v3, %0, s37 offen offset:1536 sc1\n" \
"buffer_load_dwordx4 v[184:187], v3, %0, s38 offen offset:0 sc1\n" \
"buffer_load_dwordx4 v[188:191], v3, %0, s38 offen offset:2560 sc1\n" \
"s_memrealtime s[28:29]\n" \
"s_waitcnt vmcnt(0) lgkmcnt(0)\n" \
"s_sub_u32 s28, s28, s20\n" \
"s_cmp_gt_u32 s28, 20000000\n" \
"s_cbranch_scc1 .Lc14_%=\n" \
"v_bitop3_b32 v12, v136, v140, 1 bitop3:168\n" \
"v_cmp_ne_u32 vcc, 0, v12\n" \
"s_cbranch_vccnz .Lc13_%=\n" \
"s_branch .Lc14_%=\n" \
".Lc15_%=:\n" \
"buffer_load_dwordx4 v[144:147], v3, %0, s36 offen offset:3584 sc1\n" \
"buffer_load_dwordx4 v[148:151], v3, %0, s37 offen offset:2048 sc1\n" \
"buffer_load_dwordx4 v[152:155], v3, %0, s38 offen offset:512 sc1\n" \
"buffer_load_dwordx4 v[156:159], v3, %0, s38 offen offset:3072 sc1\n" \
"buffer_load_dwordx4 v[160:163], v3, %0, s34 offen offset:1024 sc1\n" \
"buffer_load_dwordx4 v[164:167], v3, %0, s34 offen offset:3584 sc1\n" \
"buffer_load_dwordx4 v[168:171], v3, %0, s35 offen offset:2048 sc1\n" \
"buffer_load_dwordx4 v[172:175], v3, %0, s36 offen offset:512 sc1\n" \
"buffer_load_dwordx4 v[176:179], v3, %0, s36 offen offset:3072 sc1\n" \
"buffer_load_dwordx4 v[180:183], v3, %0, s37 offen offset:1536 sc1\n" \
"buffer_load_dwordx4 v[184:187], v3, %0, s38 offen offset:0 sc1\n" \
"buffer_load_dwordx4 v[188:191], v3, %0, s38 offen offset:2560 sc1\n" \
"buffer_load_dwordx4 v[192:195], v3, %0, s34 offen offset:512 sc1\n" \
"buffer_load_dwordx4 v[196:199], v3, %0, s34 offen offset:3072 sc1\n" \
"buffer_load_dwordx4 v[200:203], v3, %0, s35 offen offset:1536 sc1\n" \
"s_memrealtime s[28:29]\n" \
"s_waitcnt vmcnt(0) lgkmcnt(0)\n" \
"s_sub_u32 s28, s28, s20\n" \
"s_cmp_gt_u32 s28, 20000000\n" \
"s_cbranch_scc1 .Lc16_%=\n" \
"v_bitop3_b32 v12, v144, v148, 1 bitop3:168\n" \
"v_cmp_ne_u32 vcc, 0, v12\n" \
"s_cbranch_vccnz .Lc15_%=\n" \
"s_branch .Lc16_%=\n" \
".Lc17_%=:\n" \
"buffer_load_dwordx4 v[152:155], v3, %0, s38 offen offset:512 sc1\n" \
"buffer_load_dwordx4 v[156:159], v3, %0, s38 offen offset:3072 sc1\n" \
"buffer_load_dwordx4 v[160:163], v3, %0, s34 offen offset:1024 sc1\n" \
"buffer_load_dwordx4 v[164:167], v3, %0, s34 offen offset:3584 sc1\n" \
"buffer_load_dwordx4 v[168:171], v3, %0, s35 offen offset:2048 sc1\n" \
"buffer_load_dwordx4 v[172:175], v3, %0, s36 offen offset:512 sc1\n" \
"buffer_load_dwordx4 v[176:179], v3, %0, s36 offen offset:3072 sc1\n" \
"buffer_load_dwordx4 v[180:183], v3, %0, s37 offen offset:1536 sc1\n" \
"buffer_load_dwordx4 v[184:187], v3, %0, s38 offen offset:0 sc1\n" \
"buffer_load_dwordx4 v[188:191], v3, %0, s38 offen offset:2560 sc1\n" \
"buffer_load_dwordx4 v[192:195], v3, %0, s34 offen offset:512 sc1\n" \
"buffer_load_dwordx4 v[196:199], v3, %0, s34 offen offset:3072 sc1\n" \
"buffer_load_dwordx4 v[200:203], v3, %0, s35 offen offset:1536 sc1\n" \
"buffer_load_dwordx4 v[204:207], v3, %0, s36 offen offset:0 sc1\n" \
"buffer_load_dwordx4 v[208:211], v3, %0, s36 offen offset:2560 sc1\n" \
"buffer_load_dwordx4 v[212:215], v3, %0, s37 offen offset:1024 sc1\n" \
"s_memrealtime s[28:29]\n" \
"s_waitcnt vmcnt(0) lgkmcnt(0)\n" \
"s_sub_u32 s28, s28, s20\n" \
"s_cmp_gt_u32 s28, 20000000\n" \
"s_cbranch_scc1 .Lc18_%=\n" \
"v_bitop3_b32 v12, v152, v156, 1 bitop3:168\n" \
"v_cmp_ne_u32 vcc, 0, v12\n" \
"s_cbranch_vccnz .Lc17_%=\n" \
"s_branch .Lc18_%=\n" \
".Lc19_%=:\n" \
"buffer_load_dwordx4 v[160:163], v3, %0, s34 offen offset:1024 sc1\n" \
"buffer_load_dwordx4 v[164:167], v3, %0, s34 offen offset:3584 sc1\n" \
"buffer_load_dwordx4 v[168:171], v3, %0, s35 offen offset:2048 sc1\n" \
"buffer_load_dwordx4 v[172:175], v3, %0, s36 offen offset:512 sc1\n" \
"buffer_load_dwordx4 v[176:179], v3, %0, s36 offen offset:3072 sc1\n" \
"buffer_load_dwordx4 v[180:183], v3, %0, s37 offen offset:1536 sc1\n" \
"buffer_load_dwordx4 v[184:187], v3, %0, s38 offen offset:0 sc1\n" \
"buffer_load_dwordx4 v[188:191], v3, %0, s38 offen offset:2560 sc1\n" \
"buffer_load_dwordx4 v[192:195], v3, %0, s34 offen offset:512 sc1\n" \
"buffer_load_dwordx4 v[196:199], v3, %0, s34 offen offset:3072 sc1\n" \
"buffer_load_dwordx4 v[200:203], v3, %0, s35 offen offset:1536 sc1\n" \
"buffer_load_dwordx4 v[204:207], v3, %0, s36 offen offset:0 sc1\n" \
"buffer_load_dwordx4 v[208:211], v3, %0, s36 offen offset:2560 sc1\n" \
"buffer_load_dwordx4 v[212:215], v3, %0, s37 offen offset:1024 sc1\n" \
"buffer_load_dwordx4 v[216:219], v3, %0, s37 offen offset:3584 sc1\n" \
"buffer_load_dwordx4 v[220:223], v3, %0, s38 offen offset:2048 sc1\n" \
"s_memrealtime s[28:29]\n" \
"s_waitcnt vmcnt(0) lgkmcnt(0)\n" \
"s_sub_u32 s28, s28, s20\n" \
"s_cmp_gt_u32 s28, 20000000\n" \
"s_cbranch_scc1 .Lc20_%=\n" \
"v_bitop3_b32 v12, v160, v164, 1 bitop3:168\n" \
"v_cmp_ne_u32 vcc, 0, v12\n" \
"s_cbranch_vccnz .Lc19_%=\n" \
"s_branch .Lc20_%=\n" \
".Lc21_%=:\n" \
"buffer_load_dwordx4 v[168:171], v3, %0, s35 offen offset:2048 sc1\n" \
"buffer_load_dwordx4 v[172:175], v3, %0, s36 offen offset:512 sc1\n" \
"buffer_load_dwordx4 v[176:179], v3, %0, s36 offen offset:3072 sc1\n" \
"buffer_load_dwordx4 v[180:183], v3, %0, s37 offen offset:1536 sc1\n" \
"buffer_load_dwordx4 v[184:187], v3, %0, s38 offen offset:0 sc1\n" \
"buffer_load_dwordx4 v[188:191], v3, %0, s38 offen offset:2560 sc1\n" \
"buffer_load_dwordx4 v[192:195], v3, %0, s34 offen offset:512 sc1\n" \
"buffer_load_dwordx4 v[196:199], v3, %0, s34 offen offset:3072 sc1\n" \
"buffer_load_dwordx4 v[200:203], v3, %0, s35 offen offset:1536 sc1\n" \
"buffer_load_dwordx4 v[204:207], v3, %0, s36 offen offset:0 sc1\n" \
"buffer_load_dwordx4 v[208:211], v3, %0, s36 offen offset:2560 sc1\n" \
"buffer_load_dwordx4 v[212:215], v3, %0, s37 offen offset:1024 sc1\n" \
"buffer_load_dwordx4 v[216:219], v3, %0, s37 offen offset:3584 sc1\n" \
"buffer_load_dwordx4 v[220:223], v3, %0, s38 offen offset:2048 sc1\n" \
"buffer_load_dwordx4 v[224:227], v3, %0, s34 offen offset:0 sc1\n" \
"buffer_load_dwordx4 v[228:231], v3, %0, s34 offen offset:2560 sc1\n" \
"s_memrealtime s[28:29]\n" \
"s_waitcnt vmcnt(0) lgkmcnt(0)\n" \
"s_sub_u32 s28, s28, s20\n" \
"s_cmp_gt_u32 s28, 20000000\n" \
"s_cbranch_scc1 .Lc22_%=\n" \
"v_bitop3_b32 v12, v168, v172, 1 bitop3:168\n" \
"v_cmp_ne_u32 vcc, 0, v12\n" \
"s_cbranch_vccnz .Lc21_%=\n" \
"s_branch .Lc22_%=\n" \
".Lc23_%=:\n" \
"buffer_load_dwordx4 v[176:179], v3, %0, s36 offen offset:3072 sc1\n" \
"buffer_load_dwordx4 v[180:183], v3, %0, s37 offen offset:1536 sc1\n" \
"buffer_load_dwordx4 v[184:187], v3, %0, s38 offen offset:0 sc1\n" \
"buffer_load_dwordx4 v[188:191], v3, %0, s38 offen offset:2560 sc1\n" \
"buffer_load_dwordx4 v[192:195], v3, %0, s34 offen offset:512 sc1\n" \
"buffer_load_dwordx4 v[196:199], v3, %0, s34 offen offset:3072 sc1\n" \
"buffer_load_dwordx4 v[200:203], v3, %0, s35 offen offset:1536 sc1\n" \
"buffer_load_dwordx4 v[204:207], v3, %0, s36 offen offset:0 sc1\n" \
"buffer_load_dwordx4 v[208:211], v3, %0, s36 offen offset:2560 sc1\n" \
"buffer_load_dwordx4 v[212:215], v3, %0, s37 offen offset:1024 sc1\n" \
"buffer_load_dwordx4 v[216:219], v3, %0, s37 offen offset:3584 sc1\n" \
"buffer_load_dwordx4 v[220:223], v3, %0, s38 offen offset:2048 sc1\n" \
"buffer_load_dwordx4 v[224:227], v3, %0, s34 offen offset:0 sc1\n" \
"buffer_load_dwordx4 v[228:231], v3, %0, s34 offen offset:2560 sc1\n" \
"buffer_load_dwordx4 v[232:235], v3, %0, s35 offen offset:1024 sc1\n" \
"buffer_load_dwordx4 v[236:239], v3, %0, s35 offen offset:3584 sc1\n" \
"s_memrealtime s[28:29]\n" \
"s_waitcnt vmcnt(0) lgkmcnt(0)\n" \
"s_sub_u32 s28, s28, s20\n" \
"s_cmp_gt_u32 s28, 20000000\n" \
"s_cbranch_scc1 .Lc24_%=\n" \
"v_bitop3_b32 v12, v176, v180, 1 bitop3:168\n" \
"v_cmp_ne_u32 vcc, 0, v12\n" \
"s_cbranch_vccnz .Lc23_%=\n" \
"s_branch .Lc24_%=\n" \
".Lc25_%=:\n" \
"buffer_load_dwordx4 v[184:187], v3, %0, s38 offen offset:0 sc1\n" \
"buffer_load_dwordx4 v[188:191], v3, %0, s38 offen offset:2560 sc1\n" \
"buffer_load_dwordx4 v[192:195], v3, %0, s34 offen offset:512 sc1\n" \
"buffer_load_dwordx4 v[196:199], v3, %0, s34 offen offset:3072 sc1\n" \
"buffer_load_dwordx4 v[200:203], v3, %0, s35 offen offset:1536 sc1\n" \
"buffer_load_dwordx4 v[204:207], v3, %0, s36 offen offset:0 sc1\n" \
"buffer_load_dwordx4 v[208:211], v3, %0, s36 offen offset:2560 sc1\n" \
"buffer_load_dwordx4 v[212:215], v3, %0, s37 offen offset:1024 sc1\n" \
"buffer_load_dwordx4 v[216:219], v3, %0, s37 offen offset:3584 sc1\n" \
"buffer_load_dwordx4 v[220:223], v3, %0, s38 offen offset:2048 sc1\n" \
"buffer_load_dwordx4 v[224:227], v3, %0, s34 offen offset:0 sc1\n" \
"buffer_load_dwordx4 v[228:231], v3, %0, s34 offen offset:2560 sc1\n" \
"buffer_load_dwordx4 v[232:235], v3, %0, s35 offen offset:1024 sc1\n" \
"buffer_load_dwordx4 v[236:239], v3, %0, s35 offen offset:3584 sc1\n" \
"buffer_load_dwordx4 v[240:243], v3, %0, s36 offen offset:2048 sc1\n" \
"buffer_load_dwordx4 v[244:247], v3, %0, s37 offen offset:512 sc1\n" \
"s_memrealtime s[28:29]\n" \
"s_waitcnt vmcnt(0) lgkmcnt(0)\n" \
"s_sub_u32 s28, s28, s20\n" \
"s_cmp_gt_u32 s28, 20000000\n" \
"s_cbranch_scc1 .Lc26_%=\n" \
"v_bitop3_b32 v12, v184, v188, 1 bitop3:168\n" \
"v_cmp_ne_u32 vcc, 0, v12\n" \
"s_cbranch_vccnz .Lc25_%=\n" \
"s_branch .Lc26_%=\n" \
".Lc27_%=:\n" \
"buffer_load_dwordx2 v[16:17], v13, %0, s39 offen offset:0 sc1\n" \
"buffer_load_dwordx2 v[18:19], v13, %0, s39 offen offset:128 sc1\n" \
"buffer_load_dwordx2 v[20:21], v13, %0, s39 offen offset:256 sc1\n" \
"buffer_load_dwordx2 v[22:23], v13, %0, s39 offen offset:384 sc1\n" \
"s_memrealtime s[28:29]\n" \
"s_waitcnt vmcnt(0) lgkmcnt(0)\n" \
"s_sub_u32 s28, s28, s20\n" \
"s_cmp_gt_u32 s28, 20000000\n" \
"s_cbranch_scc1 .Lc28_%=\n" \
"v_bitop3_b32 v14, v16, v17, v18 bitop3:254\n" \
"v_bitop3_b32 v14, v14, v19, v20 bitop3:254\n" \
"v_bitop3_b32 v14, v14, v21, v22 bitop3:254\n" \
"v_bitop3_b32 v14, v14, v23, 1 bitop3:168\n" \
"v_cmp_ne_u32 s[28:29], 0, v14\n" \
"s_nop 0\n" \
"s_cmp_lg_u64 s[28:29], 0\n" \
"s_cbranch_scc1 .Lc27_%=\n" \
"s_branch .Lc28_%=\n" \
".Lc29_%=:\n" \
"buffer_load_dwordx4 v[192:195], v3, %0, s34 offen offset:512 sc1\n" \
"buffer_load_dwordx4 v[196:199], v3, %0, s34 offen offset:3072 sc1\n" \
"buffer_load_dwordx4 v[200:203], v3, %0, s35 offen offset:1536 sc1\n" \
"buffer_load_dwordx4 v[204:207], v3, %0, s36 offen offset:0 sc1\n" \
"buffer_load_dwordx4 v[208:211], v3, %0, s36 offen offset:2560 sc1\n" \
"buffer_load_dwordx4 v[212:215], v3, %0, s37 offen offset:1024 sc1\n" \
"buffer_load_dwordx4 v[216:219], v3, %0, s37 offen offset:3584 sc1\n" \
"buffer_load_dwordx4 v[220:223], v3, %0, s38 offen offset:2048 sc1\n" \
"buffer_load_dwordx4 v[224:227], v3, %0, s34 offen offset:0 sc1\n" \
"buffer_load_dwordx4 v[228:231], v3, %0, s34 offen offset:2560 sc1\n" \
"buffer_load_dwordx4 v[232:235], v3, %0, s35 offen offset:1024 sc1\n" \
"buffer_load_dwordx4 v[236:239], v3, %0, s35 offen offset:3584 sc1\n" \
"buffer_load_dwordx4 v[240:243], v3, %0, s36 offen offset:2048 sc1\n" \
"buffer_load_dwordx4 v[244:247], v3, %0, s37 offen offset:512 sc1\n" \
"buffer_load_dwordx4 v[248:251], v3, %0, s37 offen offset:3072 sc1\n" \
"buffer_load_dwordx4 v[252:255], v3, %0, s38 offen offset:1536 sc1\n" \
"s_memrealtime s[28:29]\n" \
"s_waitcnt vmcnt(0) lgkmcnt(0)\n" \
"s_sub_u32 s28, s28, s20\n" \
"s_cmp_gt_u32 s28, 20000000\n" \
"s_cbranch_scc1 .Lc30_%=\n" \
"v_bitop3_b32 v12, v192, v196, 1 bitop3:168\n" \
"v_cmp_ne_u32 vcc, 0, v12\n" \
"s_cbranch_vccnz .Lc29_%=\n" \
"s_branch .Lc30_%=\n" \
".Lc31_%=:\n" \
"buffer_load_dwordx4 v[200:203], v3, %0, s35 offen offset:1536 sc1\n" \
"buffer_load_dwordx4 v[204:207], v3, %0, s36 offen offset:0 sc1\n" \
"buffer_load_dwordx4 v[208:211], v3, %0, s36 offen offset:2560 sc1\n" \
"buffer_load_dwordx4 v[212:215], v3, %0, s37 offen offset:1024 sc1\n" \
"buffer_load_dwordx4 v[216:219], v3, %0, s37 offen offset:3584 sc1\n" \
"buffer_load_dwordx4 v[220:223], v3, %0, s38 offen offset:2048 sc1\n" \
"buffer_load_dwordx4 v[224:227], v3, %0, s34 offen offset:0 sc1\n" \
"buffer_load_dwordx4 v[228:231], v3, %0, s34 offen offset:2560 sc1\n" \
"buffer_load_dwordx4 v[232:235], v3, %0, s35 offen offset:1024 sc1\n" \
"buffer_load_dwordx4 v[236:239], v3, %0, s35 offen offset:3584 sc1\n" \
"buffer_load_dwordx4 v[240:243], v3, %0, s36 offen offset:2048 sc1\n" \
"buffer_load_dwordx4 v[244:247], v3, %0, s37 offen offset:512 sc1\n" \
"buffer_load_dwordx4 v[248:251], v3, %0, s37 offen offset:3072 sc1\n" \
"buffer_load_dwordx4 v[252:255], v3, %0, s38 offen offset:1536 sc1\n" \
"s_memrealtime s[28:29]\n" \
"s_waitcnt vmcnt(0) lgkmcnt(0)\n" \
"s_sub_u32 s28, s28, s20\n" \
"s_cmp_gt_u32 s28, 20000000\n" \
"s_cbranch_scc1 .Lc32_%=\n" \
"v_bitop3_b32 v12, v200, v204, 1 bitop3:168\n" \
"v_cmp_ne_u32 vcc, 0, v12\n" \
"s_cbranch_vccnz .Lc31_%=\n" \
"s_branch .Lc32_%=\n" \
".Lc33_%=:\n" \
"buffer_load_dwordx4 v[208:211], v3, %0, s36 offen offset:2560 sc1\n" \
"buffer_load_dwordx4 v[212:215], v3, %0, s37 offen offset:1024 sc1\n" \
"buffer_load_dwordx4 v[216:219], v3, %0, s37 offen offset:3584 sc1\n" \
"buffer_load_dwordx4 v[220:223], v3, %0, s38 offen offset:2048 sc1\n" \
"buffer_load_dwordx4 v[224:227], v3, %0, s34 offen offset:0 sc1\n" \
"buffer_load_dwordx4 v[228:231], v3, %0, s34 offen offset:2560 sc1\n" \
"buffer_load_dwordx4 v[232:235], v3, %0, s35 offen offset:1024 sc1\n" \
"buffer_load_dwordx4 v[236:239], v3, %0, s35 offen offset:3584 sc1\n" \
"buffer_load_dwordx4 v[240:243], v3, %0, s36 offen offset:2048 sc1\n" \
"buffer_load_dwordx4 v[244:247], v3, %0, s37 offen offset:512 sc1\n" \
"buffer_load_dwordx4 v[248:251], v3, %0, s37 offen offset:3072 sc1\n" \
"buffer_load_dwordx4 v[252:255], v3, %0, s38 offen offset:1536 sc1\n" \
"s_memrealtime s[28:29]\n" \
"s_waitcnt vmcnt(0) lgkmcnt(0)\n" \
"s_sub_u32 s28, s28, s20\n" \
"s_cmp_gt_u32 s28, 20000000\n" \
"s_cbranch_scc1 .Lc34_%=\n" \
"v_bitop3_b32 v12, v208, v212, 1 bitop3:168\n" \
"v_cmp_ne_u32 vcc, 0, v12\n" \
"s_cbranch_vccnz .Lc33_%=\n" \
"s_branch .Lc34_%=\n" \
".Lc35_%=:\n" \
"buffer_load_dwordx4 v[216:219], v3, %0, s37 offen offset:3584 sc1\n" \
"buffer_load_dwordx4 v[220:223], v3, %0, s38 offen offset:2048 sc1\n" \
"buffer_load_dwordx4 v[224:227], v3, %0, s34 offen offset:0 sc1\n" \
"buffer_load_dwordx4 v[228:231], v3, %0, s34 offen offset:2560 sc1\n" \
"buffer_load_dwordx4 v[232:235], v3, %0, s35 offen offset:1024 sc1\n" \
"buffer_load_dwordx4 v[236:239], v3, %0, s35 offen offset:3584 sc1\n" \
"buffer_load_dwordx4 v[240:243], v3, %0, s36 offen offset:2048 sc1\n" \
"buffer_load_dwordx4 v[244:247], v3, %0, s37 offen offset:512 sc1\n" \
"buffer_load_dwordx4 v[248:251], v3, %0, s37 offen offset:3072 sc1\n" \
"buffer_load_dwordx4 v[252:255], v3, %0, s38 offen offset:1536 sc1\n" \
"s_memrealtime s[28:29]\n" \
"s_waitcnt vmcnt(0) lgkmcnt(0)\n" \
"s_sub_u32 s28, s28, s20\n" \
"s_cmp_gt_u32 s28, 20000000\n" \
"s_cbranch_scc1 .Lc36_%=\n" \
"v_bitop3_b32 v12, v216, v220, 1 bitop3:168\n" \
"v_cmp_ne_u32 vcc, 0, v12\n" \
"s_cbranch_vccnz .Lc35_%=\n" \
"s_branch .Lc36_%=\n" \
".Lc37_%=:\n" \
"buffer_load_dwordx4 v[224:227], v3, %0, s34 offen offset:0 sc1\n" \
"buffer_load_dwordx4 v[228:231], v3, %0, s34 offen offset:2560 sc1\n" \
"buffer_load_dwordx4 v[232:235], v3, %0, s35 offen offset:1024 sc1\n" \
"buffer_load_dwordx4 v[236:239], v3, %0, s35 offen offset:3584 sc1\n" \
"buffer_load_dwordx4 v[240:243], v3, %0, s36 offen offset:2048 sc1\n" \
"buffer_load_dwordx4 v[244:247], v3, %0, s37 offen offset:512 sc1\n" \
"buffer_load_dwordx4 v[248:251], v3, %0, s37 offen offset:3072 sc1\n" \
"buffer_load_dwordx4 v[252:255], v3, %0, s38 offen offset:1536 sc1\n" \
"s_memrealtime s[28:29]\n" \
"s_waitcnt vmcnt(0) lgkmcnt(0)\n" \
"s_sub_u32 s28, s28, s20\n" \
"s_cmp_gt_u32 s28, 20000000\n" \
"s_cbranch_scc1 .Lc38_%=\n" \
"v_bitop3_b32 v12, v224, v228, 1 bitop3:168\n" \
"v_cmp_ne_u32 vcc, 0, v12\n" \
"s_cbranch_vccnz .Lc37_%=\n" \
"s_branch .Lc38_%=\n" \
".Lc39_%=:\n" \
"buffer_load_dwordx4 v[232:235], v3, %0, s35 offen offset:1024 sc1\n" \
"buffer_load_dwordx4 v[236:239], v3, %0, s35 offen offset:3584 sc1\n" \
"buffer_load_dwordx4 v[240:243], v3, %0, s36 offen offset:2048 sc1\n" \
"buffer_load_dwordx4 v[244:247], v3, %0, s37 offen offset:512 sc1\n" \
"buffer_load_dwordx4 v[248:251], v3, %0, s37 offen offset:3072 sc1\n" \
"buffer_load_dwordx4 v[252:255], v3, %0, s38 offen offset:1536 sc1\n" \
"s_memrealtime s[28:29]\n" \
"s_waitcnt vmcnt(0) lgkmcnt(0)\n" \
"s_sub_u32 s28, s28, s20\n" \
"s_cmp_gt_u32 s28, 20000000\n" \
"s_cbranch_scc1 .Lc40_%=\n" \
"v_bitop3_b32 v12, v232, v236, 1 bitop3:168\n" \
"v_cmp_ne_u32 vcc, 0, v12\n" \
"s_cbranch_vccnz .Lc39_%=\n" \
"s_branch .Lc40_%=\n" \
".Lc41_%=:\n" \
"buffer_load_dwordx4 v[240:243], v3, %0, s36 offen offset:2048 sc1\n" \
"buffer_load_dwordx4 v[244:247], v3, %0, s37 offen offset:512 sc1\n" \
"buffer_load_dwordx4 v[248:251], v3, %0, s37 offen offset:3072 sc1\n" \
"buffer_load_dwordx4 v[252:255], v3, %0, s38 offen offset:1536 sc1\n" \
"s_memrealtime s[28:29]\n" \
"s_waitcnt vmcnt(0) lgkmcnt(0)\n" \
"s_sub_u32 s28, s28, s20\n" \
"s_cmp_gt_u32 s28, 20000000\n" \
"s_cbranch_scc1 .Lc42_%=\n" \
"v_bitop3_b32 v12, v240, v244, 1 bitop3:168\n" \
"v_cmp_ne_u32 vcc, 0, v12\n" \
"s_cbranch_vccnz .Lc41_%=\n" \
"s_branch .Lc42_%=\n" \
".Lc43_%=:\n" \
"buffer_load_dwordx4 v[248:251], v3, %0, s37 offen offset:3072 sc1\n" \
"buffer_load_dwordx4 v[252:255], v3, %0, s38 offen offset:1536 sc1\n" \
"s_memrealtime s[28:29]\n" \
"s_waitcnt vmcnt(0) lgkmcnt(0)\n" \
"s_sub_u32 s28, s28, s20\n" \
"s_cmp_gt_u32 s28, 20000000\n" \
"s_cbranch_scc1 .Lc44_%=\n" \
"v_bitop3_b32 v12, v248, v252, 1 bitop3:168\n" \
"v_cmp_ne_u32 vcc, 0, v12\n" \
"s_cbranch_vccnz .Lc43_%=\n" \
"s_branch .Lc44_%=\n" \
".Lc45_%=:\n" \
"buffer_load_dwordx4 v[112:115], v0, %0, s44 offen offset:0 sc1\n" \
"buffer_load_dwordx4 v[116:119], v0, %0, s45 offen offset:0 sc1\n" \
"buffer_load_dwordx4 v[120:123], v0, %0, s44 offen offset:1024 sc1\n" \
"buffer_load_dwordx4 v[124:127], v0, %0, s45 offen offset:1024 sc1\n" \
"buffer_load_dwordx4 v[128:131], v0, %0, s44 offen offset:2048 sc1\n" \
"buffer_load_dwordx4 v[132:135], v0, %0, s45 offen offset:2048 sc1\n" \
"buffer_load_dwordx4 v[136:139], v0, %0, s44 offen offset:3072 sc1\n" \
"buffer_load_dwordx4 v[140:143], v0, %0, s45 offen offset:3072 sc1\n" \
"buffer_load_dwordx4 v[144:147], v0, %0, s48 offen offset:0 sc1\n" \
"buffer_load_dwordx4 v[148:151], v0, %0, s48 offen offset:2048 sc1\n" \
"s_memrealtime s[28:29]\n" \
"s_waitcnt vmcnt(0) lgkmcnt(0)\n" \
"s_sub_u32 s28, s28, s20\n" \
"s_cmp_gt_u32 s28, 20000000\n" \
"s_cbranch_scc1 .Lc46_%=\n" \
"v_bitop3_b32 v12, v112, v116, s24 bitop3:0x7e\n" \
"v_and_b32 v12, 1, v12\n" \
"v_cmp_ne_u32 vcc, 0, v12\n" \
"s_cbranch_vccnz .Lc45_%=\n" \
"s_branch .Lc46_%=\n" \
".Lc47_%=:\n" \
"buffer_load_dwordx4 v[120:123], v0, %0, s44 offen offset:1024 sc1\n" \
"buffer_load_dwordx4 v[124:127], v0, %0, s45 offen offset:1024 sc1\n" \
"buffer_load_dwordx4 v[128:131], v0, %0, s44 offen offset:2048 sc1\n" \
"buffer_load_dwordx4 v[132:135], v0, %0, s45 offen offset:2048 sc1\n" \
"buffer_load_dwordx4 v[136:139], v0, %0, s44 offen offset:3072 sc1\n" \
"buffer_load_dwordx4 v[140:143], v0, %0, s45 offen offset:3072 sc1\n" \
"buffer_load_dwordx4 v[144:147], v0, %0, s48 offen offset:0 sc1\n" \
"buffer_load_dwordx4 v[148:151], v0, %0, s48 offen offset:2048 sc1\n" \
"s_memrealtime s[28:29]\n" \
"s_waitcnt vmcnt(0) lgkmcnt(0)\n" \
"s_sub_u32 s28, s28, s20\n" \
"s_cmp_gt_u32 s28, 20000000\n" \
"s_cbranch_scc1 .Lc48_%=\n" \
"v_bitop3_b32 v12, v120, v124, s24 bitop3:0x7e\n" \
"v_and_b32 v12, 1, v12\n" \
"v_cmp_ne_u32 vcc, 0, v12\n" \
"s_cbranch_vccnz .Lc47_%=\n" \
"s_branch .Lc48_%=\n" \
".Lc49_%=:\n" \
"buffer_load_dwordx4 v[128:131], v0, %0, s44 offen offset:2048 sc1\n" \
"buffer_load_dwordx4 v[132:135], v0, %0, s45 offen offset:2048 sc1\n" \
"buffer_load_dwordx4 v[136:139], v0, %0, s44 offen offset:3072 sc1\n" \
"buffer_load_dwordx4 v[140:143], v0, %0, s45 offen offset:3072 sc1\n" \
"buffer_load_dwordx4 v[144:147], v0, %0, s48 offen offset:0 sc1\n" \
"buffer_load_dwordx4 v[148:151], v0, %0, s48 offen offset:2048 sc1\n" \
"s_memrealtime s[28:29]\n" \
"s_waitcnt vmcnt(0) lgkmcnt(0)\n" \
"s_sub_u32 s28, s28, s20\n" \
"s_cmp_gt_u32 s28, 20000000\n" \
"s_cbranch_scc1 .Lc50_%=\n" \
"v_bitop3_b32 v12, v128, v132, s24 bitop3:0x7e\n" \
"v_and_b32 v12, 1, v12\n" \
"v_cmp_ne_u32 vcc, 0, v12\n" \
"s_cbranch_vccnz .Lc49_%=\n" \
"s_branch .Lc50_%=\n" \
".Lc51_%=:\n" \
"buffer_load_dwordx4 v[136:139], v0, %0, s44 offen offset:3072 sc1\n" \
"buffer_load_dwordx4 v[140:143], v0, %0, s45 offen offset:3072 sc1\n" \
"buffer_load_dwordx4 v[144:147], v0, %0, s48 offen offset:0 sc1\n" \
"buffer_load_dwordx4 v[148:151], v0, %0, s48 offen offset:2048 sc1\n" \
"s_memrealtime s[28:29]\n" \
"s_waitcnt vmcnt(0) lgkmcnt(0)\n" \
"s_sub_u32 s28, s28, s20\n" \
"s_cmp_gt_u32 s28, 20000000\n" \
"s_cbranch_scc1 .Lc52_%=\n" \
"v_bitop3_b32 v12, v136, v140, s24 bitop3:0x7e\n" \
"v_and_b32 v12, 1, v12\n" \
"v_cmp_ne_u32 vcc, 0, v12\n" \
"s_cbranch_vccnz .Lc51_%=\n" \
"s_branch .Lc52_%=\n" \
".Lc53_%=:\n" \
"buffer_load_dwordx4 v[144:147], v0, %0, s48 offen offset:0 sc1\n" \
"buffer_load_dwordx4 v[148:151], v0, %0, s48 offen offset:2048 sc1\n" \
"s_memrealtime s[28:29]\n" \
"s_waitcnt vmcnt(0) lgkmcnt(0)\n" \
"s_sub_u32 s28, s28, s20\n" \
"s_cmp_gt_u32 s28, 20000000\n" \
"s_cbranch_scc1 .Lc54_%=\n" \
"v_bitop3_b32 v12, v144, v148, s24 bitop3:0x7e\n" \
"v_bitop3_b32 v12, v12, v146, s24 bitop3:0xf6\n" \
"v_bitop3_b32 v12, v12, v150, s24 bitop3:0xf6\n" \
"v_and_b32 v12, 1, v12\n" \
"v_cmp_ne_u32 vcc, 0, v12\n" \
"s_cbranch_vccnz .Lc53_%=\n" \
"s_branch .Lc54_%=\n" \
".Lc55_%=:\n" \
"buffer_load_dwordx4 v[16:19], v0, %0, s46 offen offset:0 sc1\n" \
"buffer_load_dwordx4 v[20:23], v0, %0, s47 offen offset:0 sc1\n" \
"buffer_load_dwordx4 v[24:27], v0, %0, s46 offen offset:1024 sc1\n" \
"buffer_load_dwordx4 v[28:31], v0, %0, s47 offen offset:1024 sc1\n" \
"buffer_load_dwordx4 v[76:79], v0, %0, s46 offen offset:2048 sc1\n" \
"buffer_load_dwordx4 v[80:83], v0, %0, s47 offen offset:2048 sc1\n" \
"buffer_load_dwordx4 v[84:87], v0, %0, s46 offen offset:3072 sc1\n" \
"buffer_load_dwordx4 v[88:91], v0, %0, s47 offen offset:3072 sc1\n" \
"buffer_load_dwordx4 v[92:95], v0, %0, s48 offen offset:1024 sc1\n" \
"buffer_load_dwordx4 v[252:255], v0, %0, s48 offen offset:3072 sc1\n" \
"s_memrealtime s[28:29]\n" \
"s_waitcnt vmcnt(0) lgkmcnt(0)\n" \
"s_sub_u32 s28, s28, s20\n" \
"s_cmp_gt_u32 s28, 20000000\n" \
"s_cbranch_scc1 .Lc56_%=\n" \
"v_bitop3_b32 v12, v16, v20, s24 bitop3:0x7e\n" \
"v_and_b32 v12, 1, v12\n" \
"v_cmp_ne_u32 vcc, 0, v12\n" \
"s_cbranch_vccnz .Lc55_%=\n" \
"s_branch .Lc56_%=\n" \
".Lc57_%=:\n" \
"buffer_load_dwordx4 v[24:27], v0, %0, s46 offen offset:1024 sc1\n" \
"buffer_load_dwordx4 v[28:31], v0, %0, s47 offen offset:1024 sc1\n" \
"buffer_load_dwordx4 v[76:79], v0, %0, s46 offen offset:2048 sc1\n" \
"buffer_load_dwordx4 v[80:83], v0, %0, s47 offen offset:2048 sc1\n" \
"buffer_load_dwordx4 v[84:87], v0, %0, s46 offen offset:3072 sc1\n" \
"buffer_load_dwordx4 v[88:91], v0, %0, s47 offen offset:3072 sc1\n" \
"buffer_load_dwordx4 v[92:95], v0, %0, s48 offen offset:1024 sc1\n" \
"buffer_load_dwordx4 v[252:255], v0, %0, s48 offen offset:3072 sc1\n" \
"s_memrealtime s[28:29]\n" \
"s_waitcnt vmcnt(0) lgkmcnt(0)\n" \
"s_sub_u32 s28, s28, s20\n" \
"s_cmp_gt_u32 s28, 20000000\n" \
"s_cbranch_scc1 .Lc58_%=\n" \
"v_bitop3_b32 v12, v24, v28, s24 bitop3:0x7e\n" \
"v_and_b32 v12, 1, v12\n" \
"v_cmp_ne_u32 vcc, 0, v12\n" \
"s_cbranch_vccnz .Lc57_%=\n" \
"s_branch .Lc58_%=\n" \
".Lc59_%=:\n" \
"buffer_load_dwordx4 v[76:79], v0, %0, s46 offen offset:2048 sc1\n" \
"buffer_load_dwordx4 v[80:83], v0, %0, s47 offen offset:2048 sc1\n" \
"buffer_load_dwordx4 v[84:87], v0, %0, s46 offen offset:3072 sc1\n" \
"buffer_load_dwordx4 v[88:91], v0, %0, s47 offen offset:3072 sc1\n" \
"buffer_load_dwordx4 v[92:95], v0, %0, s48 offen offset:1024 sc1\n" \
"buffer_load_dwordx4 v[252:255], v0, %0, s48 offen offset:3072 sc1\n" \
"s_memrealtime s[28:29]\n" \
"s_waitcnt vmcnt(0) lgkmcnt(0)\n" \
"s_sub_u32 s28, s28, s20\n" \
"s_cmp_gt_u32 s28, 20000000\n" \
"s_cbranch_scc1 .Lc60_%=\n" \
"v_bitop3_b32 v12, v76, v80, s24 bitop3:0x7e\n" \
"v_and_b32 v12, 1, v12\n" \
"v_cmp_ne_u32 vcc, 0, v12\n" \
"s_cbranch_vccnz .Lc59_%=\n" \
"s_branch .Lc60_%=\n" \
".Lc61_%=:\n" \
"buffer_load_dwordx4 v[84:87], v0, %0, s46 offen offset:3072 sc1\n" \
"buffer_load_dwordx4 v[88:91], v0, %0, s47 offen offset:3072 sc1\n" \
"buffer_load_dwordx4 v[92:95], v0, %0, s48 offen offset:1024 sc1\n" \
"buffer_load_dwordx4 v[252:255], v0, %0, s48 offen offset:3072 sc1\n" \
"s_memrealtime s[28:29]\n" \
"s_waitcnt vmcnt(0) lgkmcnt(0)\n" \
"s_sub_u32 s28, s28, s20\n" \
"s_cmp_gt_u32 s28, 20000000\n" \
"s_cbranch_scc1 .Lc62_%=\n" \
"v_bitop3_b32 v12, v84, v88, s24 bitop3:0x7e\n" \
"v_and_b32 v12, 1, v12\n" \
"v_cmp_ne_u32 vcc, 0, v12\n" \
"s_cbranch_vccnz .Lc61_%=\n" \
"s_branch .Lc62_%=\n" \
".Lc63_%=:\n" \
"buffer_load_dwordx4 v[92:95], v0, %0, s48 offen offset:1024 sc1\n" \
"buffer_load_dwordx4 v[252:255], v0, %0, s48 offen offset:3072 sc1\n" \
"s_memrealtime s[28:29]\n" \
"s_waitcnt vmcnt(0) lgkmcnt(0)\n" \
"s_sub_u32 s28, s28, s20\n" \
"s_cmp_gt_u32 s28, 20000000\n" \
"s_cbranch_scc1 .Lc64_%=\n" \
"v_bitop3_b32 v12, v92, v252, s24 bitop3:0x7e\n" \
"v_bitop3_b32 v12, v12, v94, s24 bitop3:0xf6\n" \
"v_bitop3_b32 v12, v12, v254, s24 bitop3:0xf6\n" \
"v_and_b32 v12, 1, v12\n" \
"v_cmp_ne_u32 vcc, 0, v12\n" \
"s_cbranch_vccnz .Lc63_%=\n" \
"s_branch .Lc64_%=\n" \
".Lc65_%=:\n" \
"buffer_load_dwordx4 v[96:99], v3, %0, s34 offen offset:2048 sc1\n" \
"buffer_load_dwordx4 v[100:103], v3, %0, s35 offen offset:512 sc1\n" \
"buffer_load_dwordx4 v[104:107], v3, %0, s35 offen offset:3072 sc1\n" \
"buffer_load_dwordx4 v[108:111], v3, %0, s36 offen offset:1536 sc1\n" \
"buffer_load_dwordx4 v[112:115], v3, %0, s37 offen offset:0 sc1\n" \
"buffer_load_dwordx4 v[116:119], v3, %0, s37 offen offset:2560 sc1\n" \
"buffer_load_dwordx4 v[120:123], v3, %0, s38 offen offset:1024 sc1\n" \
"buffer_load_dwordx4 v[124:127], v3, %0, s38 offen offset:3584 sc1\n" \
"buffer_load_dwordx4 v[128:131], v3, %0, s34 offen offset:1536 sc1\n" \
"buffer_load_dwordx4 v[132:135], v3, %0, s35 offen offset:0 sc1\n" \
"s_memrealtime s[28:29]\n" \
"s_waitcnt vmcnt(0) lgkmcnt(0)\n" \
"s_sub_u32 s28, s28, s20\n" \
"s_cmp_gt_u32 s28, 20000000\n" \
"s_cbranch_scc1 .Lc66_%=\n" \
"v_bitop3_b32 v12, v96, v100, 1 bitop3:42\n" \
"v_cmp_ne_u32 vcc, 0, v12\n" \
"s_cbranch_vccnz .Lc65_%=\n" \
"s_branch .Lc66_%=\n" \
".Lc67_%=:\n" \
"buffer_load_dwordx4 v[104:107], v3, %0, s35 offen offset:3072 sc1\n" \
"buffer_load_dwordx4 v[108:111], v3, %0, s36 offen offset:1536 sc1\n" \
"buffer_load_dwordx4 v[112:115], v3, %0, s37 offen offset:0 sc1\n" \
"buffer_load_dwordx4 v[116:119], v3, %0, s37 offen offset:2560 sc1\n" \
"buffer_load_dwordx4 v[120:123], v3, %0, s38 offen offset:1024 sc1\n" \
"buffer_load_dwordx4 v[124:127], v3, %0, s38 offen offset:3584 sc1\n" \
"buffer_load_dwordx4 v[128:131], v3, %0, s34 offen offset:1536 sc1\n" \
"buffer_load_dwordx4 v[132:135], v3, %0, s35 offen offset:0 sc1\n" \
"buffer_load_dwordx4 v[136:139], v3, %0, s35 offen offset:2560 sc1\n" \
"buffer_load_dwordx4 v[140:143], v3, %0, s36 offen offset:1024 sc1\n" \
"s_memrealtime s[28:29]\n" \
"s_waitcnt vmcnt(0) lgkmcnt(0)\n" \
"s_sub_u32 s28, s28, s20\n" \
"s_cmp_gt_u32 s28, 20000000\n" \
"s_cbranch_scc1 .Lc68_%=\n" \
"v_bitop3_b32 v12, v104, v108, 1 bitop3:42\n" \
"v_cmp_ne_u32 vcc, 0, v12\n" \
"s_cbranch_vccnz .Lc67_%=\n" \
"s_branch .Lc68_%=\n" \
".Lc69_%=:\n" \
"buffer_load_dwordx4 v[112:115], v3, %0, s37 offen offset:0 sc1\n" \
"buffer_load_dwordx4 v[116:119], v3, %0, s37 offen offset:2560 sc1\n" \
"buffer_load_dwordx4 v[120:123], v3, %0, s38 offen offset:1024 sc1\n" \
"buffer_load_dwordx4 v[124:127], v3, %0, s38 offen offset:3584 sc1\n" \
"buffer_load_dwordx4 v[128:131], v3, %0, s34 offen offset:1536 sc1\n" \
"buffer_load_dwordx4 v[132:135], v3, %0, s35 offen offset:0 sc1\n" \
"buffer_load_dwordx4 v[136:139], v3, %0, s35 offen offset:2560 sc1\n" \
"buffer_load_dwordx4 v[140:143], v3, %0, s36 offen offset:1024 sc1\n" \
"buffer_load_dwordx4 v[144:147], v3, %0, s36 offen offset:3584 sc1\n" \
"buffer_load_dwordx4 v[148:151], v3, %0, s37 offen offset:2048 sc1\n" \
"buffer_load_dwordx4 v[152:155], v3, %0, s38 offen offset:512 sc1\n" \
"s_memrealtime s[28:29]\n" \
"s_waitcnt vmcnt(0) lgkmcnt(0)\n" \
"s_sub_u32 s28, s28, s20\n" \
"s_cmp_gt_u32 s28, 20000000\n" \
"s_cbranch_scc1 .Lc70_%=\n" \
"v_bitop3_b32 v12, v112, v116, 1 bitop3:42\n" \
"v_cmp_ne_u32 vcc, 0, v12\n" \
"s_cbranch_vccnz .Lc69_%=\n" \
"s_branch .Lc70_%=\n" \
".Lc71_%=:\n" \
"buffer_load_dwordx4 v[120:123], v3, %0, s38 offen offset:1024 sc1\n" \
"buffer_load_dwordx4 v[124:127], v3, %0, s38 offen offset:3584 sc1\n" \
"buffer_load_dwordx4 v[128:131], v3, %0, s34 offen offset:1536 sc1\n" \
"buffer_load_dwordx4 v[132:135], v3, %0, s35 offen offset:0 sc1\n" \
"buffer_load_dwordx4 v[136:139], v3, %0, s35 offen offset:2560 sc1\n" \
"buffer_load_dwordx4 v[140:143], v3, %0, s36 offen offset:1024 sc1\n" \
"buffer_load_dwordx4 v[144:147], v3, %0, s36 offen offset:3584 sc1\n" \
"buffer_load_dwordx4 v[148:151], v3, %0, s37 offen offset:2048 sc1\n" \
"buffer_load_dwordx4 v[152:155], v3, %0, s38 offen offset:512 sc1\n" \
"buffer_load_dwordx4 v[156:159], v3, %0, s38 offen offset:3072 sc1\n" \
"buffer_load_dwordx4 v[160:163], v3, %0, s34 offen offset:1024 sc1\n" \
"buffer_load_dwordx4 v[164:167], v3, %0, s34 offen offset:3584 sc1\n" \
"s_memrealtime s[28:29]\n" \
"s_waitcnt vmcnt(0) lgkmcnt(0)\n" \
"s_sub_u32 s28, s28, s20\n" \
"s_cmp_gt_u32 s28, 20000000\n" \
"s_cbranch_scc1 .Lc72_%=\n" \
"v_bitop3_b32 v12, v120, v124, 1 bitop3:42\n" \
"v_cmp_ne_u32 vcc, 0, v12\n" \
"s_cbranch_vccnz .Lc71_%=\n" \
"s_branch .Lc72_%=\n" \
".Lc73_%=:\n" \
"buffer_load_dwordx4 v[128:131], v3, %0, s34 offen offset:1536 sc1\n" \
"buffer_load_dwordx4 v[132:135], v3, %0, s35 offen offset:0 sc1\n" \
"buffer_load_dwordx4 v[136:139], v3, %0, s35 offen offset:2560 sc1\n" \
"buffer_load_dwordx4 v[140:143], v3, %0, s36 offen offset:1024 sc1\n" \
"buffer_load_dwordx4 v[144:147], v3, %0, s36 offen offset:3584 sc1\n" \
"buffer_load_dwordx4 v[148:151], v3, %0, s37 offen offset:2048 sc1\n" \
"buffer_load_dwordx4 v[152:155], v3, %0, s38 offen offset:512 sc1\n" \
"buffer_load_dwordx4 v[156:159], v3, %0, s38 offen offset:3072 sc1\n" \
"buffer_load_dwordx4 v[160:163], v3, %0, s34 offen offset:1024 sc1\n" \
"buffer_load_dwordx4 v[164:167], v3, %0, s34 offen offset:3584 sc1\n" \
"buffer_load_dwordx4 v[168:171], v3, %0, s35 offen offset:2048 sc1\n" \
"buffer_load_dwordx4 v[172:175], v3, %0, s36 offen offset:512 sc1\n" \
"buffer_load_dwordx4 v[176:179], v3, %0, s36 offen offset:3072 sc1\n" \
"s_memrealtime s[28:29]\n" \
"s_waitcnt vmcnt(0) lgkmcnt(0)\n" \
"s_sub_u32 s28, s28, s20\n" \
"s_cmp_gt_u32 s28, 20000000\n" \
"s_cbranch_scc1 .Lc74_%=\n" \
"v_bitop3_b32 v12, v128, v132, 1 bitop3:42\n" \
"v_cmp_ne_u32 vcc, 0, v12\n" \
"s_cbranch_vccnz .Lc73_%=\n" \
"s_branch .Lc74_%=\n" \
".Lc75_%=:\n" \
"buffer_load_dwordx4 v[136:139], v3, %0, s35 offen offset:2560 sc1\n" \
"buffer_load_dwordx4 v[140:143], v3, %0, s36 offen offset:1024 sc1\n" \
"buffer_load_dwordx4 v[144:147], v3, %0, s36 offen offset:3584 sc1\n" \
"buffer_load_dwordx4 v[148:151], v3, %0, s37 offen offset:2048 sc1\n" \
"buffer_load_dwordx4 v[152:155], v3, %0, s38 offen offset:512 sc1\n" \
"buffer_load_dwordx4 v[156:159], v3, %0, s38 offen offset:3072 sc1\n" \
"buffer_load_dwordx4 v[160:163], v3, %0, s34 offen offset:1024 sc1\n" \
"buffer_load_dwordx4 v[164:167], v3, %0, s34 offen offset:3584 sc1\n" \
"buffer_load_dwordx4 v[168:171], v3, %0, s35 offen offset:2048 sc1\n" \
"buffer_load_dwordx4 v[172:175], v3, %0, s36 offen offset:512 sc1\n" \
"buffer_load_dwordx4 v[176:179], v3, %0, s36 offen offset:3072 sc1\n" \
"buffer_load_dwordx4 v[180:183], v3, %0, s37 offen offset:1536 sc1\n" \
"buffer_load_dwordx4 v[184:187], v3, %0, s38 offen offset:0 sc1\n" \
"buffer_load_dwordx4 v[188:191], v3, %0, s38 offen offset:2560 sc1\n" \
"s_memrealtime s[28:29]\n" \
"s_waitcnt vmcnt(0) lgkmcnt(0)\n" \
"s_sub_u32 s28, s28, s20\n" \
"s_cmp_gt_u32 s28, 20000000\n" \
"s_cbranch_scc1 .Lc76_%=\n" \
"v_bitop3_b32 v12, v136, v140, 1 bitop3:42\n" \
"v_cmp_ne_u32 vcc, 0, v12\n" \
"s_cbranch_vccnz .Lc75_%=\n" \
"s_branch .Lc76_%=\n" \
".Lc77_%=:\n" \
"buffer_load_dwordx4 v[144:147], v3, %0, s36 offen offset:3584 sc1\n" \
"buffer_load_dwordx4 v[148:151], v3, %0, s37 offen offset:2048 sc1\n" \
"buffer_load_dwordx4 v[152:155], v3, %0, s38 offen offset:512 sc1\n" \
"buffer_load_dwordx4 v[156:159], v3, %0, s38 offen offset:3072 sc1\n" \
"buffer_load_dwordx4 v[160:163], v3, %0, s34 offen offset:1024 sc1\n" \
"buffer_load_dwordx4 v[164:167], v3, %0, s34 offen offset:3584 sc1\n" \
"buffer_load_dwordx4 v[168:171], v3, %0, s35 offen offset:2048 sc1\n" \
"buffer_load_dwordx4 v[172:175], v3, %0, s36 offen offset:512 sc1\n" \
"buffer_load_dwordx4 v[176:179], v3, %0, s36 offen offset:3072 sc1\n" \
"buffer_load_dwordx4 v[180:183], v3, %0, s37 offen offset:1536 sc1\n" \
"buffer_load_dwordx4 v[184:187], v3, %0, s38 offen offset:0 sc1\n" \
"buffer_load_dwordx4 v[188:191], v3, %0, s38 offen offset:2560 sc1\n" \
"buffer_load_dwordx4 v[192:195], v3, %0, s34 offen offset:512 sc1\n" \
"buffer_load_dwordx4 v[196:199], v3, %0, s34 offen offset:3072 sc1\n" \
"buffer_load_dwordx4 v[200:203], v3, %0, s35 offen offset:1536 sc1\n" \
"s_memrealtime s[28:29]\n" \
"s_waitcnt vmcnt(0) lgkmcnt(0)\n" \
"s_sub_u32 s28, s28, s20\n" \
"s_cmp_gt_u32 s28, 20000000\n" \
"s_cbranch_scc1 .Lc78_%=\n" \
"v_bitop3_b32 v12, v144, v148, 1 bitop3:42\n" \
"v_cmp_ne_u32 vcc, 0, v12\n" \
"s_cbranch_vccnz .Lc77_%=\n" \
"s_branch .Lc78_%=\n" \
".Lc79_%=:\n" \
"buffer_load_dwordx4 v[152:155], v3, %0, s38 offen offset:512 sc1\n" \
"buffer_load_dwordx4 v[156:159], v3, %0, s38 offen offset:3072 sc1\n" \
"buffer_load_dwordx4 v[160:163], v3, %0, s34 offen offset:1024 sc1\n" \
"buffer_load_dwordx4 v[164:167], v3, %0, s34 offen offset:3584 sc1\n" \
"buffer_load_dwordx4 v[168:171], v3, %0, s35 offen offset:2048 sc1\n" \
"buffer_load_dwordx4 v[172:175], v3, %0, s36 offen offset:512 sc1\n" \
"buffer_load_dwordx4 v[176:179], v3, %0, s36 offen offset:3072 sc1\n" \
"buffer_load_dwordx4 v[180:183], v3, %0, s37 offen offset:1536 sc1\n" \
"buffer_load_dwordx4 v[184:187], v3, %0, s38 offen offset:0 sc1\n" \
"buffer_load_dwordx4 v[188:191], v3, %0, s38 offen offset:2560 sc1\n" \
"buffer_load_dwordx4 v[192:195], v3, %0, s34 offen offset:512 sc1\n" \
"buffer_load_dwordx4 v[196:199], v3, %0, s34 offen offset:3072 sc1\n" \
"buffer_load_dwordx4 v[200:203], v3, %0, s35 offen offset:1536 sc1\n" \
"buffer_load_dwordx4 v[204:207], v3, %0, s36 offen offset:0 sc1\n" \
"buffer_load_dwordx4 v[208:211], v3, %0, s36 offen offset:2560 sc1\n" \
"s_memrealtime s[28:29]\n" \
"s_waitcnt vmcnt(0) lgkmcnt(0)\n" \
"s_sub_u32 s28, s28, s20\n" \
"s_cmp_gt_u32 s28, 20000000\n" \
"s_cbranch_scc1 .Lc80_%=\n" \
"v_bitop3_b32 v12, v152, v156, 1 bitop3:42\n" \
"v_cmp_ne_u32 vcc, 0, v12\n" \
"s_cbranch_vccnz .Lc79_%=\n" \
"s_branch .Lc80_%=\n" \
".Lc81_%=:\n" \
"buffer_load_dwordx4 v[160:163], v3, %0, s34 offen offset:1024 sc1\n" \
"buffer_load_dwordx4 v[164:167], v3, %0, s34 offen offset:3584 sc1\n" \
"buffer_load_dwordx4 v[168:171], v3, %0, s35 offen offset:2048 sc1\n" \
"buffer_load_dwordx4 v[172:175], v3, %0, s36 offen offset:512 sc1\n" \
"buffer_load_dwordx4 v[176:179], v3, %0, s36 offen offset:3072 sc1\n" \
"buffer_load_dwordx4 v[180:183], v3, %0, s37 offen offset:1536 sc1\n" \
"buffer_load_dwordx4 v[184:187], v3, %0, s38 offen offset:0 sc1\n" \
"buffer_load_dwordx4 v[188:191], v3, %0, s38 offen offset:2560 sc1\n" \
"buffer_load_dwordx4 v[192:195], v3, %0, s34 offen offset:512 sc1\n" \
"buffer_load_dwordx4 v[196:199], v3, %0, s34 offen offset:3072 sc1\n" \
"buffer_load_dwordx4 v[200:203], v3, %0, s35 offen offset:1536 sc1\n" \
"buffer_load_dwordx4 v[204:207], v3, %0, s36 offen offset:0 sc1\n" \
"buffer_load_dwordx4 v[208:211], v3, %0, s36 offen offset:2560 sc1\n" \
"buffer_load_dwordx4 v[212:215], v3, %0, s37 offen offset:1024 sc1\n" \
"buffer_load_dwordx4 v[216:219], v3, %0, s37 offen offset:3584 sc1\n" \
"s_memrealtime s[28:29]\n" \
"s_waitcnt vmcnt(0) lgkmcnt(0)\n" \
"s_sub_u32 s28, s28, s20\n" \
"s_cmp_gt_u32 s28, 20000000\n" \
"s_cbranch_scc1 .Lc82_%=\n" \
"v_bitop3_b32 v12, v160, v164, 1 bitop3:42\n" \
"v_cmp_ne_u32 vcc, 0, v12\n" \
"s_cbranch_vccnz .Lc81_%=\n" \
"s_branch .Lc82_%=\n" \
".Lc83_%=:\n" \
"buffer_load_dwordx4 v[168:171], v3, %0, s35 offen offset:2048 sc1\n" \
"buffer_load_dwordx4 v[172:175], v3, %0, s36 offen offset:512 sc1\n" \
"buffer_load_dwordx4 v[176:179], v3, %0, s36 offen offset:3072 sc1\n" \
"buffer_load_dwordx4 v[180:183], v3, %0, s37 offen offset:1536 sc1\n" \
"buffer_load_dwordx4 v[184:187], v3, %0, s38 offen offset:0 sc1\n" \
"buffer_load_dwordx4 v[188:191], v3, %0, s38 offen offset:2560 sc1\n" \
"buffer_load_dwordx4 v[192:195], v3, %0, s34 offen offset:512 sc1\n" \
"buffer_load_dwordx4 v[196:199], v3, %0, s34 offen offset:3072 sc1\n" \
"buffer_load_dwordx4 v[200:203], v3, %0, s35 offen offset:1536 sc1\n" \
"buffer_load_dwordx4 v[204:207], v3, %0, s36 offen offset:0 sc1\n" \
"buffer_load_dwordx4 v[208:211], v3, %0, s36 offen offset:2560 sc1\n" \
"buffer_load_dwordx4 v[212:215], v3, %0, s37 offen offset:1024 sc1\n" \
"buffer_load_dwordx4 v[216:219], v3, %0, s37 offen offset:3584 sc1\n" \
"buffer_load_dwordx4 v[220:223], v3, %0, s38 offen offset:2048 sc1\n" \
"buffer_load_dwordx4 v[224:227], v3, %0, s34 offen offset:0 sc1\n" \
"s_memrealtime s[28:29]\n" \
"s_waitcnt vmcnt(0) lgkmcnt(0)\n" \
"s_sub_u32 s28, s28, s20\n" \
"s_cmp_gt_u32 s28, 20000000\n" \
"s_cbranch_scc1 .Lc84_%=\n" \
"v_bitop3_b32 v12, v168, v172, 1 bitop3:42\n" \
"v_cmp_ne_u32 vcc, 0, v12\n" \
"s_cbranch_vccnz .Lc83_%=\n" \
"s_branch .Lc84_%=\n" \
".Lc85_%=:\n" \
"buffer_load_dwordx4 v[176:179], v3, %0, s36 offen offset:3072 sc1\n" \
"buffer_load_dwordx4 v[180:183], v3, %0, s37 offen offset:1536 sc1\n" \
"buffer_load_dwordx4 v[184:187], v3, %0, s38 offen offset:0 sc1\n" \
"buffer_load_dwordx4 v[188:191], v3, %0, s38 offen offset:2560 sc1\n" \
"buffer_load_dwordx4 v[192:195], v3, %0, s34 offen offset:512 sc1\n" \
"buffer_load_dwordx4 v[196:199], v3, %0, s34 offen offset:3072 sc1\n" \
"buffer_load_dwordx4 v[200:203], v3, %0, s35 offen offset:1536 sc1\n" \
"buffer_load_dwordx4 v[204:207], v3, %0, s36 offen offset:0 sc1\n" \
"buffer_load_dwordx4 v[208:211], v3, %0, s36 offen offset:2560 sc1\n" \
"buffer_load_dwordx4 v[212:215], v3, %0, s37 offen offset:1024 sc1\n" \
"buffer_load_dwordx4 v[216:219], v3, %0, s37 offen offset:3584 sc1\n" \
"buffer_load_dwordx4 v[220:223], v3, %0, s38 offen offset:2048 sc1\n" \
"buffer_load_dwordx4 v[224:227], v3, %0, s34 offen offset:0 sc1\n" \
"buffer_load_dwordx4 v[228:231], v3, %0, s34 offen offset:2560 sc1\n" \
"buffer_load_dwordx4 v[232:235], v3, %0, s35 offen offset:1024 sc1\n" \
"s_memrealtime s[28:29]\n" \
"s_waitcnt vmcnt(0) lgkmcnt(0)\n" \
"s_sub_u32 s28, s28, s20\n" \
"s_cmp_gt_u32 s28, 20000000\n" \
"s_cbranch_scc1 .Lc86_%=\n" \
"v_bitop3_b32 v12, v176, v180, 1 bitop3:42\n" \
"v_cmp_ne_u32 vcc, 0, v12\n" \
"s_cbranch_vccnz .Lc85_%=\n" \
"s_branch .Lc86_%=\n" \
".Lc87_%=:\n" \
"buffer_load_dwordx4 v[184:187], v3, %0, s38 offen offset:0 sc1\n" \
"buffer_load_dwordx4 v[188:191], v3, %0, s38 offen offset:2560 sc1\n" \
"buffer_load_dwordx4 v[192:195], v3, %0, s34 offen offset:512 sc1\n" \
"buffer_load_dwordx4 v[196:199], v3, %0, s34 offen offset:3072 sc1\n" \
"buffer_load_dwordx4 v[200:203], v3, %0, s35 offen offset:1536 sc1\n" \
"buffer_load_dwordx4 v[204:207], v3, %0, s36 offen offset:0 sc1\n" \
"buffer_load_dwordx4 v[208:211], v3, %0, s36 offen offset:2560 sc1\n" \
"buffer_load_dwordx4 v[212:215], v3, %0, s37 offen offset:1024 sc1\n" \
"buffer_load_dwordx4 v[216:219], v3, %0, s37 offen offset:3584 sc1\n" \
"buffer_load_dwordx4 v[220:223], v3, %0, s38 offen offset:2048 sc1\n" \
"buffer_load_dwordx4 v[224:227], v3, %0, s34 offen offset:0 sc1\n" \
"buffer_load_dwordx4 v[228:231], v3, %0, s34 offen offset:2560 sc1\n" \
"buffer_load_dwordx4 v[232:235], v3, %0, s35 offen offset:1024 sc1\n" \
"buffer_load_dwordx4 v[236:239], v3, %0, s35 offen offset:3584 sc1\n" \
"buffer_load_dwordx4 v[240:243], v3, %0, s36 offen offset:2048 sc1\n" \
"s_memrealtime s[28:29]\n" \
"s_waitcnt vmcnt(0) lgkmcnt(0)\n" \
"s_sub_u32 s28, s28, s20\n" \
"s_cmp_gt_u32 s28, 20000000\n" \
"s_cbranch_scc1 .Lc88_%=\n" \
"v_bitop3_b32 v12, v184, v188, 1 bitop3:42\n" \
"v_cmp_ne_u32 vcc, 0, v12\n" \
"s_cbranch_vccnz .Lc87_%=\n" \
"s_branch .Lc88_%=\n" \
".Lc89_%=:\n" \
"buffer_load_dwordx2 v[16:17], v13, %0, s39 offen offset:0 sc1\n" \
"buffer_load_dwordx2 v[18:19], v13, %0, s39 offen offset:128 sc1\n" \
"buffer_load_dwordx2 v[20:21], v13, %0, s39 offen offset:256 sc1\n" \
"buffer_load_dwordx2 v[22:23], v13, %0, s39 offen offset:384 sc1\n" \
"s_memrealtime s[28:29]\n" \
"s_waitcnt vmcnt(0) lgkmcnt(0)\n" \
"s_sub_u32 s28, s28, s20\n" \
"s_cmp_gt_u32 s28, 20000000\n" \
"s_cbranch_scc1 .Lc90_%=\n" \
"v_bitop3_b32 v14, v16, v17, v18 bitop3:128\n" \
"v_bitop3_b32 v14, v14, v19, v20 bitop3:128\n" \
"v_bitop3_b32 v14, v14, v21, v22 bitop3:128\n" \
"v_bitop3_b32 v14, v14, v23, 1 bitop3:42\n" \
"v_cmp_ne_u32 s[28:29], 0, v14\n" \
"s_nop 0\n" \
"s_cmp_lg_u64 s[28:29], 0\n" \
"s_cbranch_scc1 .Lc89_%=\n" \
"s_branch .Lc90_%=\n" \
".Lc91_%=:\n" \
"buffer_load_dwordx4 v[192:195], v3, %0, s34 offen offset:512 sc1\n" \
"buffer_load_dwordx4 v[196:199], v3, %0, s34 offen offset:3072 sc1\n" \
"buffer_load_dwordx4 v[200:203], v3, %0, s35 offen offset:1536 sc1\n" \
"buffer_load_dwordx4 v[204:207], v3, %0, s36 offen offset:0 sc1\n" \
"buffer_load_dwordx4 v[208:211], v3, %0, s36 offen offset:2560 sc1\n" \
"buffer_load_dwordx4 v[212:215], v3, %0, s37 offen offset:1024 sc1\n" \
"buffer_load_dwordx4 v[216:219], v3, %0, s37 offen offset:3584 sc1\n" \
"buffer_load_dwordx4 v[220:223], v3, %0, s38 offen offset:2048 sc1\n" \
"buffer_load_dwordx4 v[224:227], v3, %0, s34 offen offset:0 sc1\n" \
"buffer_load_dwordx4 v[228:231], v3, %0, s34 offen offset:2560 sc1\n" \
"buffer_load_dwordx4 v[232:235], v3, %0, s35 offen offset:1024 sc1\n" \
"buffer_load_dwordx4 v[236:239], v3, %0, s35 offen offset:3584 sc1\n" \
"buffer_load_dwordx4 v[240:243], v3, %0, s36 offen offset:2048 sc1\n" \
"buffer_load_dwordx4 v[244:247], v3, %0, s37 offen offset:512 sc1\n" \
"buffer_load_dwordx4 v[248:251], v3, %0, s37 offen offset:3072 sc1\n" \
"s_memrealtime s[28:29]\n" \
"s_waitcnt vmcnt(0) lgkmcnt(0)\n" \
"s_sub_u32 s28, s28, s20\n" \
"s_cmp_gt_u32 s28, 20000000\n" \
"s_cbranch_scc1 .Lc92_%=\n" \
"v_bitop3_b32 v12, v192, v196, 1 bitop3:42\n" \
"v_cmp_ne_u32 vcc, 0, v12\n" \
"s_cbranch_vccnz .Lc91_%=\n" \
"s_branch .Lc92_%=\n" \
".Lc93_%=:\n" \
"buffer_load_dwordx4 v[200:203], v3, %0, s35 offen offset:1536 sc1\n" \
"buffer_load_dwordx4 v[204:207], v3, %0, s36 offen offset:0 sc1\n" \
"buffer_load_dwordx4 v[208:211], v3, %0, s36 offen offset:2560 sc1\n" \
"buffer_load_dwordx4 v[212:215], v3, %0, s37 offen offset:1024 sc1\n" \
"buffer_load_dwordx4 v[216:219], v3, %0, s37 offen offset:3584 sc1\n" \
"buffer_load_dwordx4 v[220:223], v3, %0, s38 offen offset:2048 sc1\n" \
"buffer_load_dwordx4 v[224:227], v3, %0, s34 offen offset:0 sc1\n" \
"buffer_load_dwordx4 v[228:231], v3, %0, s34 offen offset:2560 sc1\n" \
"buffer_load_dwordx4 v[232:235], v3, %0, s35 offen offset:1024 sc1\n" \
"buffer_load_dwordx4 v[236:239], v3, %0, s35 offen offset:3584 sc1\n" \
"buffer_load_dwordx4 v[240:243], v3, %0, s36 offen offset:2048 sc1\n" \
"buffer_load_dwordx4 v[244:247], v3, %0, s37 offen offset:512 sc1\n" \
"buffer_load_dwordx4 v[248:251], v3, %0, s37 offen offset:3072 sc1\n" \
"buffer_load_dwordx4 v[252:255], v3, %0, s38 offen offset:1536 sc1\n" \
"s_memrealtime s[28:29]\n" \
"s_waitcnt vmcnt(0) lgkmcnt(0)\n" \
"s_sub_u32 s28, s28, s20\n" \
"s_cmp_gt_u32 s28, 20000000\n" \
"s_cbranch_scc1 .Lc94_%=\n" \
"v_bitop3_b32 v12, v200, v204, 1 bitop3:42\n" \
"v_cmp_ne_u32 vcc, 0, v12\n" \
"s_cbranch_vccnz .Lc93_%=\n" \
"s_branch .Lc94_%=\n" \
".Lc95_%=:\n" \
"buffer_load_dwordx4 v[208:211], v3, %0, s36 offen offset:2560 sc1\n" \
"buffer_load_dwordx4 v[212:215], v3, %0, s37 offen offset:1024 sc1\n" \
"buffer_load_dwordx4 v[216:219], v3, %0, s37 offen offset:3584 sc1\n" \
"buffer_load_dwordx4 v[220:223], v3, %0, s38 offen offset:2048 sc1\n" \
"buffer_load_dwordx4 v[224:227], v3, %0, s34 offen offset:0 sc1\n" \
"buffer_load_dwordx4 v[228:231], v3, %0, s34 offen offset:2560 sc1\n" \
"buffer_load_dwordx4 v[232:235], v3, %0, s35 offen offset:1024 sc1\n" \
"buffer_load_dwordx4 v[236:239], v3, %0, s35 offen offset:3584 sc1\n" \
"buffer_load_dwordx4 v[240:243], v3, %0, s36 offen offset:2048 sc1\n" \
"buffer_load_dwordx4 v[244:247], v3, %0, s37 offen offset:512 sc1\n" \
"buffer_load_dwordx4 v[248:251], v3, %0, s37 offen offset:3072 sc1\n" \
"buffer_load_dwordx4 v[252:255], v3, %0, s38 offen offset:1536 sc1\n" \
"s_memrealtime s[28:29]\n" \
"s_waitcnt vmcnt(0) lgkmcnt(0)\n" \
"s_sub_u32 s28, s28, s20\n" \
"s_cmp_gt_u32 s28, 20000000\n" \
"s_cbranch_scc1 .Lc96_%=\n" \
"v_bitop3_b32 v12, v208, v212, 1 bitop3:42\n" \
"v_cmp_ne_u32 vcc, 0, v12\n" \
"s_cbranch_vccnz .Lc95_%=\n" \
"s_branch .Lc96_%=\n" \
".Lc97_%=:\n" \
"buffer_load_dwordx4 v[216:219], v3, %0, s37 offen offset:3584 sc1\n" \
"buffer_load_dwordx4 v[220:223], v3, %0, s38 offen offset:2048 sc1\n" \
"buffer_load_dwordx4 v[224:227], v3, %0, s34 offen offset:0 sc1\n" \
"buffer_load_dwordx4 v[228:231], v3, %0, s34 offen offset:2560 sc1\n" \
"buffer_load_dwordx4 v[232:235], v3, %0, s35 offen offset:1024 sc1\n" \
"buffer_load_dwordx4 v[236:239], v3, %0, s35 offen offset:3584 sc1\n" \
"buffer_load_dwordx4 v[240:243], v3, %0, s36 offen offset:2048 sc1\n" \
"buffer_load_dwordx4 v[244:247], v3, %0, s37 offen offset:512 sc1\n" \
"buffer_load_dwordx4 v[248:251], v3, %0, s37 offen offset:3072 sc1\n" \
"buffer_load_dwordx4 v[252:255], v3, %0, s38 offen offset:1536 sc1\n" \
"s_memrealtime s[28:29]\n" \
"s_waitcnt vmcnt(0) lgkmcnt(0)\n" \
"s_sub_u32 s28, s28, s20\n" \
"s_cmp_gt_u32 s28, 20000000\n" \
"s_cbranch_scc1 .Lc98_%=\n" \
"v_bitop3_b32 v12, v216, v220, 1 bitop3:42\n" \
"v_cmp_ne_u32 vcc, 0, v12\n" \
"s_cbranch_vccnz .Lc97_%=\n" \
"s_branch .Lc98_%=\n" \
".Lc99_%=:\n" \
"buffer_load_dwordx4 v[224:227], v3, %0, s34 offen offset:0 sc1\n" \
"buffer_load_dwordx4 v[228:231], v3, %0, s34 offen offset:2560 sc1\n" \
"buffer_load_dwordx4 v[232:235], v3, %0, s35 offen offset:1024 sc1\n" \
"buffer_load_dwordx4 v[236:239], v3, %0, s35 offen offset:3584 sc1\n" \
"buffer_load_dwordx4 v[240:243], v3, %0, s36 offen offset:2048 sc1\n" \
"buffer_load_dwordx4 v[244:247], v3, %0, s37 offen offset:512 sc1\n" \
"buffer_load_dwordx4 v[248:251], v3, %0, s37 offen offset:3072 sc1\n" \
"buffer_load_dwordx4 v[252:255], v3, %0, s38 offen offset:1536 sc1\n" \
"s_memrealtime s[28:29]\n" \
"s_waitcnt vmcnt(0) lgkmcnt(0)\n" \
"s_sub_u32 s28, s28, s20\n" \
"s_cmp_gt_u32 s28, 20000000\n" \
"s_cbranch_scc1 .Lc100_%=\n" \
"v_bitop3_b32 v12, v224, v228, 1 bitop3:42\n" \
"v_cmp_ne_u32 vcc, 0, v12\n" \
"s_cbranch_vccnz .Lc99_%=\n" \
"s_branch .Lc100_%=\n" \
".Lc101_%=:\n" \
"buffer_load_dwordx4 v[232:235], v3, %0, s35 offen offset:1024 sc1\n" \
"buffer_load_dwordx4 v[236:239], v3, %0, s35 offen offset:3584 sc1\n" \
"buffer_load_dwordx4 v[240:243], v3, %0, s36 offen offset:2048 sc1\n" \
"buffer_load_dwordx4 v[244:247], v3, %0, s37 offen offset:512 sc1\n" \
"buffer_load_dwordx4 v[248:251], v3, %0, s37 offen offset:3072 sc1\n" \
"buffer_load_dwordx4 v[252:255], v3, %0, s38 offen offset:1536 sc1\n" \
"s_memrealtime s[28:29]\n" \
"s_waitcnt vmcnt(0) lgkmcnt(0)\n" \
"s_sub_u32 s28, s28, s20\n" \
"s_cmp_gt_u32 s28, 20000000\n" \
"s_cbranch_scc1 .Lc102_%=\n" \
"v_bitop3_b32 v12, v232, v236, 1 bitop3:42\n" \
"v_cmp_ne_u32 vcc, 0, v12\n" \
"s_cbranch_vccnz .Lc101_%=\n" \
"s_branch .Lc102_%=\n" \
".Lc103_%=:\n" \
"buffer_load_dwordx4 v[240:243], v3, %0, s36 offen offset:2048 sc1\n" \
"buffer_load_dwordx4 v[244:247], v3, %0, s37 offen offset:512 sc1\n" \
"buffer_load_dwordx4 v[248:251], v3, %0, s37 offen offset:3072 sc1\n" \
"buffer_load_dwordx4 v[252:255], v3, %0, s38 offen offset:1536 sc1\n" \
"s_memrealtime s[28:29]\n" \
"s_waitcnt vmcnt(0) lgkmcnt(0)\n" \
"s_sub_u32 s28, s28, s20\n" \
"s_cmp_gt_u32 s28, 20000000\n" \
"s_cbranch_scc1 .Lc104_%=\n" \
"v_bitop3_b32 v12, v240, v244, 1 bitop3:42\n" \
"v_cmp_ne_u32 vcc, 0, v12\n" \
"s_cbranch_vccnz .Lc103_%=\n" \
"s_branch .Lc104_%=\n" \
".Lc105_%=:\n" \
"buffer_load_dwordx4 v[248:251], v3, %0, s37 offen offset:3072 sc1\n" \
"buffer_load_dwordx4 v[252:255], v3, %0, s38 offen offset:1536 sc1\n" \
"s_memrealtime s[28:29]\n" \
"s_waitcnt vmcnt(0) lgkmcnt(0)\n" \
"s_sub_u32 s28, s28, s20\n" \
"s_cmp_gt_u32 s28, 20000000\n" \
"s_cbranch_scc1 .Lc106_%=\n" \
"v_bitop3_b32 v12, v248, v252, 1 bitop3:42\n" \
"v_cmp_ne_u32 vcc, 0, v12\n" \
"s_cbranch_vccnz .Lc105_%=\n" \
"s_branch .Lc106_%=\n" \
".Lc107_%=:\n" \
"s_sub_u32 s25, s53, 8192\n" \
"s_sub_u32 s26, s54, 8192\n" \
"buffer_load_dwordx4 v[96:99], v0, %0, s25 offen offset:0 sc1\n" \
"buffer_load_dwordx4 v[100:103], v0, %0, s26 offen offset:0 sc1\n" \
"buffer_load_dwordx4 v[104:107], v0, %0, s25 offen offset:1024 sc1\n" \
"buffer_load_dwordx4 v[108:111], v0, %0, s26 offen offset:1024 sc1\n" \
"buffer_load_dwordx4 v[112:115], v0, %0, s25 offen offset:2048 sc1\n" \
"buffer_load_dwordx4 v[116:119], v0, %0, s26 offen offset:2048 sc1\n" \
"buffer_load_dwordx4 v[120:123], v0, %0, s25 offen offset:3072 sc1\n" \
"buffer_load_dwordx4 v[124:127], v0, %0, s26 offen offset:3072 sc1\n" \
"s_sub_u32 s25, s53, 4096\n" \
"s_sub_u32 s26, s54, 4096\n" \
"buffer_load_dwordx4 v[128:131], v0, %0, s25 offen offset:0 sc1\n" \
"buffer_load_dwordx4 v[132:135], v0, %0, s26 offen offset:0 sc1\n" \
"buffer_load_dwordx4 v[136:139], v0, %0, s25 offen offset:1024 sc1\n" \
"buffer_load_dwordx4 v[140:143], v0, %0, s26 offen offset:1024 sc1\n" \
"buffer_load_dwordx4 v[144:147], v0, %0, s25 offen offset:2048 sc1\n" \
"buffer_load_dwordx4 v[148:151], v0, %0, s26 offen offset:2048 sc1\n" \
"buffer_load_dwordx4 v[152:155], v0, %0, s25 offen offset:3072 sc1\n" \
"buffer_load_dwordx4 v[156:159], v0, %0, s26 offen offset:3072 sc1\n" \
"s_memrealtime s[28:29]\n" \
"s_waitcnt vmcnt(0) lgkmcnt(0)\n" \
"s_sub_u32 s28, s28, s20\n" \
"s_cmp_gt_u32 s28, 20000000\n" \
"s_cbranch_scc1 .Lc108_%=\n" \
"v_bitop3_b32 v12, v96, v100, s24 bitop3:0x7e\n" \
"v_bitop3_b32 v12, v12, v104, s24 bitop3:0xf6\n" \
"v_bitop3_b32 v12, v12, v108, s24 bitop3:0xf6\n" \
"v_bitop3_b32 v12, v12, v112, s24 bitop3:0xf6\n" \
"v_bitop3_b32 v12, v12, v116, s24 bitop3:0xf6\n" \
"v_bitop3_b32 v12, v12, v120, s24 bitop3:0xf6\n" \
"v_bitop3_b32 v12, v12, v124, s24 bitop3:0xf6\n" \
"v_and_b32 v12, 1, v12\n" \
"v_cmp_ne_u32 vcc, 0, v12\n" \
"s_cbranch_vccnz .Lc107_%=\n" \
"s_branch .Lc108_%=\n" \
".Lc109_%=:\n" \
"s_sub_u32 s25, s53, 8192\n" \
"s_sub_u32 s26, s54, 8192\n" \
"buffer_load_dwordx4 v[128:131], v0, %0, s25 offen offset:0 sc1\n" \
"buffer_load_dwordx4 v[132:135], v0, %0, s26 offen offset:0 sc1\n" \
"buffer_load_dwordx4 v[136:139], v0, %0, s25 offen offset:1024 sc1\n" \
"buffer_load_dwordx4 v[140:143], v0, %0, s26 offen offset:1024 sc1\n" \
"buffer_load_dwordx4 v[144:147], v0, %0, s25 offen offset:2048 sc1\n" \
"buffer_load_dwordx4 v[148:151], v0, %0, s26 offen offset:2048 sc1\n" \
"buffer_load_dwordx4 v[152:155], v0, %0, s25 offen offset:3072 sc1\n" \
"buffer_load_dwordx4 v[156:159], v0, %0, s26 offen offset:3072 sc1\n" \
"s_sub_u32 s25, s53, 4096\n" \
"s_sub_u32 s26, s54, 4096\n" \
"buffer_load_dwordx4 v[160:163], v0, %0, s25 offen offset:0 sc1\n" \
"buffer_load_dwordx4 v[164:167], v0, %0, s26 offen offset:0 sc1\n" \
"buffer_load_dwordx4 v[168:171], v0, %0, s25 offen offset:1024 sc1\n" \
"buffer_load_dwordx4 v[172:175], v0, %0, s26 offen offset:1024 sc1\n" \
"buffer_load_dwordx4 v[176:179], v0, %0, s25 offen offset:2048 sc1\n" \
"buffer_load_dwordx4 v[180:183], v0, %0, s26 offen offset:2048 sc1\n" \
"buffer_load_dwordx4 v[184:187], v0, %0, s25 offen offset:3072 sc1\n" \
"buffer_load_dwordx4 v[188:191], v0, %0, s55 offen offset:0 sc1\n" \
"s_memrealtime s[28:29]\n" \
"s_waitcnt vmcnt(0) lgkmcnt(0)\n" \
"s_sub_u32 s28, s28, s20\n" \
"s_cmp_gt_u32 s28, 20000000\n" \
"s_cbranch_scc1 .Lc110_%=\n" \
"v_bitop3_b32 v12, v128, v132, s24 bitop3:0x7e\n" \
"v_bitop3_b32 v12, v12, v136, s24 bitop3:0xf6\n" \
"v_bitop3_b32 v12, v12, v140, s24 bitop3:0xf6\n" \
"v_bitop3_b32 v12, v12, v144, s24 bitop3:0xf6\n" \
"v_bitop3_b32 v12, v12, v148, s24 bitop3:0xf6\n" \
"v_bitop3_b32 v12, v12, v152, s24 bitop3:0xf6\n" \
"v_bitop3_b32 v12, v12, v156, s24 bitop3:0xf6\n" \
"v_and_b32 v12, 1, v12\n" \
"v_cmp_ne_u32 vcc, 0, v12\n" \
"s_cbranch_vccnz .Lc109_%=\n" \
"s_branch .Lc110_%=\n" \
".Lc111_%=:\n" \
"s_sub_u32 s25, s53, 4096\n" \
"s_sub_u32 s26, s54, 4096\n" \
"buffer_load_dwordx4 v[160:163], v0, %0, s25 offen offset:0 sc1\n" \
"buffer_load_dwordx4 v[164:167], v0, %0, s26 offen offset:0 sc1\n" \
"buffer_load_dwordx4 v[168:171], v0, %0, s25 offen offset:1024 sc1\n" \
"buffer_load_dwordx4 v[172:175], v0, %0, s26 offen offset:1024 sc1\n" \
"buffer_load_dwordx4 v[176:179], v0, %0, s25 offen offset:2048 sc1\n" \
"buffer_load_dwordx4 v[180:183], v0, %0, s26 offen offset:2048 sc1\n" \
"buffer_load_dwordx4 v[184:187], v0, %0, s25 offen offset:3072 sc1\n" \
"buffer_load_dwordx4 v[188:191], v0, %0, s55 offen offset:0 sc1\n" \
"s_sub_u32 s25, s53, 0\n" \
"s_sub_u32 s26, s54, 0\n" \
"buffer_load_dwordx4 v[192:195], v0, %0, s25 offen offset:0 sc1\n" \
"buffer_load_dwordx4 v[196:199], v0, %0, s55 offen offset:1024 sc1\n" \
"s_memrealtime s[28:29]\n" \
"s_waitcnt vmcnt(0) lgkmcnt(0)\n" \
"s_sub_u32 s28, s28, s20\n" \
"s_cmp_gt_u32 s28, 20000000\n" \
"s_cbranch_scc1 .Lc112_%=\n" \
"v_bitop3_b32 v12, v160, v164, s24 bitop3:0x7e\n" \
"v_bitop3_b32 v12, v12, v168, s24 bitop3:0xf6\n" \
"v_bitop3_b32 v12, v12, v172, s24 bitop3:0xf6\n" \
"v_and_b32 v12, 1, v12\n" \
"v_cmp_ne_u32 vcc, 0, v12\n" \
"s_cbranch_vccnz .Lc111_%=\n" \
"s_branch .Lc112_%=\n" \
".Lc113_%=:\n" \
"s_sub_u32 s25, s53, 4096\n" \
"s_sub_u32 s26, s54, 4096\n" \
"buffer_load_dwordx4 v[176:179], v0, %0, s25 offen offset:2048 sc1\n" \
"buffer_load_dwordx4 v[180:183], v0, %0, s26 offen offset:2048 sc1\n" \
"buffer_load_dwordx4 v[184:187], v0, %0, s25 offen offset:3072 sc1\n" \
"buffer_load_dwordx4 v[188:191], v0, %0, s55 offen offset:0 sc1\n" \
"s_sub_u32 s25, s53, 0\n" \
"s_sub_u32 s26, s54, 0\n" \
"buffer_load_dwordx4 v[192:195], v0, %0, s25 offen offset:0 sc1\n" \
"buffer_load_dwordx4 v[196:199], v0, %0, s55 offen offset:1024 sc1\n" \
"buffer_load_dwordx4 v[200:203], v0, %0, s25 offen offset:1024 sc1\n" \
"buffer_load_dwordx4 v[204:207], v0, %0, s55 offen offset:2048 sc1\n" \
"buffer_load_dwordx4 v[208:211], v0, %0, s25 offen offset:2048 sc1\n" \
"buffer_load_dwordx4 v[212:215], v0, %0, s55 offen offset:3072 sc1\n" \
"s_memrealtime s[28:29]\n" \
"s_waitcnt vmcnt(0) lgkmcnt(0)\n" \
"s_sub_u32 s28, s28, s20\n" \
"s_cmp_gt_u32 s28, 20000000\n" \
"s_cbranch_scc1 .Lc114_%=\n" \
"v_bitop3_b32 v12, v176, v180, s24 bitop3:0x7e\n" \
"v_and_b32 v12, 1, v12\n" \
"v_cmp_ne_u32 vcc, 0, v12\n" \
"s_cbranch_vccnz .Lc113_%=\n" \
"s_branch .Lc114_%=\n" \
".Lc115_%=:\n" \
"s_sub_u32 s25, s53, 4096\n" \
"s_sub_u32 s26, s54, 4096\n" \
"buffer_load_dwordx4 v[184:187], v0, %0, s25 offen offset:3072 sc1\n" \
"buffer_load_dwordx4 v[188:191], v0, %0, s55 offen offset:0 sc1\n" \
"s_sub_u32 s25, s53, 0\n" \
"s_sub_u32 s26, s54, 0\n" \
"buffer_load_dwordx4 v[192:195], v0, %0, s25 offen offset:0 sc1\n" \
"buffer_load_dwordx4 v[196:199], v0, %0, s55 offen offset:1024 sc1\n" \
"buffer_load_dwordx4 v[200:203], v0, %0, s25 offen offset:1024 sc1\n" \
"buffer_load_dwordx4 v[204:207], v0, %0, s55 offen offset:2048 sc1\n" \
"buffer_load_dwordx4 v[208:211], v0, %0, s25 offen offset:2048 sc1\n" \
"buffer_load_dwordx4 v[212:215], v0, %0, s55 offen offset:3072 sc1\n" \
"s_memrealtime s[28:29]\n" \
"s_waitcnt vmcnt(0) lgkmcnt(0)\n" \
"s_sub_u32 s28, s28, s20\n" \
"s_cmp_gt_u32 s28, 20000000\n" \
"s_cbranch_scc1 .Lc116_%=\n" \
"v_bitop3_b32 v12, v184, v188, s24 bitop3:0x7e\n" \
"v_and_b32 v12, 1, v12\n" \
"v_cmp_ne_u32 vcc, 0, v12\n" \
"s_cbranch_vccnz .Lc115_%=\n" \
"s_branch .Lc116_%=\n" \
".Lc117_%=:\n" \
"s_sub_u32 s25, s53, 0\n" \
"s_sub_u32 s26, s54, 0\n" \
"buffer_load_dwordx4 v[192:195], v0, %0, s25 offen offset:0 sc1\n" \
"buffer_load_dwordx4 v[196:199], v0, %0, s55 offen offset:1024 sc1\n" \
"buffer_load_dwordx4 v[200:203], v0, %0, s25 offen offset:1024 sc1\n" \
"buffer_load_dwordx4 v[204:207], v0, %0, s55 offen offset:2048 sc1\n" \
"buffer_load_dwordx4 v[208:211], v0, %0, s25 offen offset:2048 sc1\n" \
"buffer_load_dwordx4 v[212:215], v0, %0, s55 offen offset:3072 sc1\n" \
"s_memrealtime s[28:29]\n" \
"s_waitcnt vmcnt(0) lgkmcnt(0)\n" \
"s_sub_u32 s28, s28, s20\n" \
"s_cmp_gt_u32 s28, 20000000\n" \
"s_cbranch_scc1 .Lc118_%=\n" \
"v_bitop3_b32 v12, v192, v196, s24 bitop3:0x7e\n" \
"v_bitop3_b32 v12, v12, v200, s24 bitop3:0xf6\n" \
"v_bitop3_b32 v12, v12, v204, s24 bitop3:0xf6\n" \
"v_bitop3_b32 v12, v12, v208, s24 bitop3:0xf6\n" \
"v_bitop3_b32 v12, v12, v212, s24 bitop3:0xf6\n" \
"v_and_b32 v12, 1, v12\n" \
"v_cmp_ne_u32 vcc, 0, v12\n" \
"s_cbranch_vccnz .Lc117_%=\n" \
"s_branch .Lc118_%=\n" \
".Ll1_%=:\n" \
"buffer_load_dword v29, v30, %0, s26 offen sc1\n" \
"s_memrealtime s[28:29]\n" \
"s_waitcnt vmcnt(0) lgkmcnt(0)\n" \
"s_sub_u32 s28, s28, s20\n" \
"s_cmp_gt_u32 s28, 20000000\n" \
"s_cbranch_scc1 .Ll2_%=\n" \
"v_cmp_ne_u32 vcc, %5, v29\n" \
"s_cbranch_vccnz .Ll1_%=\n" \
"s_branch .Ll2_%=\n" \
".Ll3_%=:\n" \
"buffer_load_dword v23, v22, %0, s62 offen sc1\n" \
"s_memrealtime s[28:29]\n" \
"s_waitcnt vmcnt(0) lgkmcnt(0)\n" \
"s_sub_u32 s28, s28, s20\n" \
"s_cmp_gt_u32 s28, 20000000\n" \
"s_cbranch_scc1 .Ll4_%=\n" \
"v_cmp_ne_u32 vcc, %5, v23\n" \
"s_cbranch_vccnz .Ll3_%=\n" \
"s_branch .Ll4_%=\n" \
".Ll5_%=:\n" \
"buffer_load_dword v31, v30, %0, s61 offen sc1\n" \
"s_memrealtime s[28:29]\n" \
"s_waitcnt vmcnt(0) lgkmcnt(0)\n" \
"s_sub_u32 s28, s28, s20\n" \
"s_cmp_gt_u32 s28, 20000000\n" \
"s_cbranch_scc1 .Ll6_%=\n" \
"v_cmp_ne_u32 vcc, %5, v31\n" \
"s_cbranch_vccnz .Ll5_%=\n" \
"s_branch .Ll6_%=\n" \
".Ll501_%=:\n" \
"buffer_load_dwordx4 v[96:99], v44, %0, s44 offen offset:0 sc1\n" \
"buffer_load_dwordx4 v[100:103], v44, %0, s44 offen offset:1024 sc1\n" \
"buffer_load_dwordx4 v[104:107], v44, %0, s44 offen offset:2048 sc1\n" \
"buffer_load_dwordx4 v[108:111], v45, %0, s45 offen offset:0 sc1\n" \
"buffer_load_dwordx4 v[112:115], v45, %0, s45 offen offset:1024 sc1\n" \
"buffer_load_dwordx4 v[116:119], v45, %0, s45 offen offset:2048 sc1\n" \
"s_memrealtime s[28:29]\n" \
"s_waitcnt vmcnt(0) lgkmcnt(0)\n" \
"s_sub_u32 s28, s28, s20\n" \
"s_cmp_gt_u32 s28, 20000000\n" \
"s_cbranch_scc1 .Ll502_%=\n" \
"v_bitop3_b32 v29, v96, v100, s24 bitop3:0x7e\n" \
"v_bitop3_b32 v29, v29, v104, s24 bitop3:0xf6\n" \
"v_bitop3_b32 v29, v29, v108, s24 bitop3:0xf6\n" \
"v_bitop3_b32 v29, v29, v112, s24 bitop3:0xf6\n" \
"v_bitop3_b32 v29, v29, v116, s24 bitop3:0xf6\n" \
"v_and_b32 v29, 1, v29\n" \
"v_cmp_ne_u32 vcc, 0, v29\n" \
"s_cbranch_vccnz .Ll501_%=\n" \
"s_branch .Ll502_%=\n" \
".Ll503_%=:\n" \
"buffer_load_dwordx4 v[96:99], v13, %0, s46 offen offset:0 sc1\n" \
"buffer_load_dwordx4 v[100:103], v13, %0, s46 offen offset:1024 sc1\n" \
"buffer_load_dwordx4 v[104:107], v14, %0, s47 offen offset:0 sc1\n" \
"buffer_load_dwordx4 v[108:111], v14, %0, s47 offen offset:1024 sc1\n" \
"s_memrealtime s[28:29]\n" \
"s_waitcnt vmcnt(0) lgkmcnt(0)\n" \
"s_sub_u32 s28, s28, s20\n" \
"s_cmp_gt_u32 s28, 20000000\n" \
"s_cbranch_scc1 .Ll504_%=\n" \
"v_bitop3_b32 v28, v96, v100, s24 bitop3:0x7e\n" \
"v_and_b32 v28, s48, v28\n" \
"v_bitop3_b32 v29, v104, v108, s24 bitop3:0x7e\n" \
"v_and_b32 v29, s49, v29\n" \
"v_bitop3_b32 v29, v28, v29, 1 bitop3:168\n" \
"v_cmp_ne_u32 vcc, 0, v29\n" \
"s_cbranch_vccnz .Ll503_%=\n" \
"s_branch .Ll504_%=\n" \
".Ll508_%=:\n" \
"s_add_u32 s25, s51, -8192\n" \
"buffer_load_dwordx4 v[216:219], v0, %0, s25 offen offset:0 sc1\n" \
"s_memrealtime s[28:29]\n" \
"s_waitcnt vmcnt(0) lgkmcnt(0)\n" \
"s_sub_u32 s28, s28, s20\n" \
"s_cmp_gt_u32 s28, 20000000\n" \
"s_cbranch_scc1 .Ll509_%=\n" \
"v_bitop3_b32 v12, v216, s24, 1 bitop3:0x28\n" \
"v_cmp_ne_u32 vcc, 0, v12\n" \
"s_cbranch_vccnz .Ll508_%=\n" \
"s_branch .Ll509_%=\n" \
".Ll510_%=:\n" \
"s_add_u32 s25, s51, -8192\n" \
"buffer_load_dwordx4 v[220:223], v0, %0, s25 offen offset:2048 sc1\n" \
"s_memrealtime s[28:29]\n" \
"s_waitcnt vmcnt(0) lgkmcnt(0)\n" \
"s_sub_u32 s28, s28, s20\n" \
"s_cmp_gt_u32 s28, 20000000\n" \
"s_cbranch_scc1 .Ll511_%=\n" \
"v_bitop3_b32 v12, v220, s24, 1 bitop3:0x28\n" \
"v_cmp_ne_u32 vcc, 0, v12\n" \
"s_cbranch_vccnz .Ll510_%=\n" \
"s_branch .Ll511_%=\n" \
".Ll512_%=:\n" \
"s_add_u32 s25, s52, -4096\n" \
"buffer_load_dwordx4 v[224:227], v0, %0, s25 offen offset:0 sc1\n" \
"s_memrealtime s[28:29]\n" \
"s_waitcnt vmcnt(0) lgkmcnt(0)\n" \
"s_sub_u32 s28, s28, s20\n" \
"s_cmp_gt_u32 s28, 20000000\n" \
"s_cbranch_scc1 .Ll513_%=\n" \
"v_bitop3_b32 v12, v224, s24, 1 bitop3:0x28\n" \
"v_cmp_ne_u32 vcc, 0, v12\n" \
"s_cbranch_vccnz .Ll512_%=\n" \
"s_branch .Ll513_%=\n" \
".Ll514_%=:\n" \
"s_add_u32 s25, s52, -8192\n" \
"buffer_load_dwordx4 v[228:231], v0, %0, s25 offen offset:2048 sc1\n" \
"s_memrealtime s[28:29]\n" \
"s_waitcnt vmcnt(0) lgkmcnt(0)\n" \
"s_sub_u32 s28, s28, s20\n" \
"s_cmp_gt_u32 s28, 20000000\n" \
"s_cbranch_scc1 .Ll515_%=\n" \
"v_bitop3_b32 v12, v228, s24, 1 bitop3:0x28\n" \
"v_cmp_ne_u32 vcc, 0, v12\n" \
"s_cbranch_vccnz .Ll514_%=\n" \
"s_branch .Ll515_%=\n" \
".Ll516_%=:\n" \
"s_add_u32 s25, s53, -4096\n" \
"buffer_load_dwordx4 v[232:235], v0, %0, s25 offen offset:0 sc1\n" \
"s_memrealtime s[28:29]\n" \
"s_waitcnt vmcnt(0) lgkmcnt(0)\n" \
"s_sub_u32 s28, s28, s20\n" \
"s_cmp_gt_u32 s28, 20000000\n" \
"s_cbranch_scc1 .Ll517_%=\n" \
"v_bitop3_b32 v12, v232, s24, 1 bitop3:0x28\n" \
"v_cmp_ne_u32 vcc, 0, v12\n" \
"s_cbranch_vccnz .Ll516_%=\n" \
"s_branch .Ll517_%=\n" \
".Ll518_%=:\n" \
"s_add_u32 s25, s53, -8192\n" \
"buffer_load_dwordx4 v[236:239], v0, %0, s25 offen offset:2048 sc1\n" \
"s_memrealtime s[28:29]\n" \
"s_waitcnt vmcnt(0) lgkmcnt(0)\n" \
"s_sub_u32 s28, s28, s20\n" \
"s_cmp_gt_u32 s28, 20000000\n" \
"s_cbranch_scc1 .Ll519_%=\n" \
"v_bitop3_b32 v12, v236, s24, 1 bitop3:0x28\n" \
"v_cmp_ne_u32 vcc, 0, v12\n" \
"s_cbranch_vccnz .Ll518_%=\n" \
"s_branch .Ll519_%=\n" \
".Ll520_%=:\n" \
"s_add_u32 s25, s54, -4096\n" \
"buffer_load_dwordx4 v[240:243], v0, %0, s25 offen offset:0 sc1\n" \
"s_memrealtime s[28:29]\n" \
"s_waitcnt vmcnt(0) lgkmcnt(0)\n" \
"s_sub_u32 s28, s28, s20\n" \
"s_cmp_gt_u32 s28, 20000000\n" \
"s_cbranch_scc1 .Ll521_%=\n" \
"v_bitop3_b32 v12, v240, s24, 1 bitop3:0x28\n" \
"v_cmp_ne_u32 vcc, 0, v12\n" \
"s_cbranch_vccnz .Ll520_%=\n" \
"s_branch .Ll521_%=\n" \
".Ll522_%=:\n" \
"s_add_u32 s25, s54, -8192\n" \
"buffer_load_dwordx4 v[244:247], v0, %0, s25 offen offset:2048 sc1\n" \
"s_memrealtime s[28:29]\n" \
"s_waitcnt vmcnt(0) lgkmcnt(0)\n" \
"s_sub_u32 s28, s28, s20\n" \
"s_cmp_gt_u32 s28, 20000000\n" \
"s_cbranch_scc1 .Ll523_%=\n" \
"v_bitop3_b32 v12, v244, s24, 1 bitop3:0x28\n" \
"v_cmp_ne_u32 vcc, 0, v12\n" \
"s_cbranch_vccnz .Ll522_%=\n" \
"s_branch .Ll523_%=\n" \
".Ll524_%=:\n" \
"s_add_u32 s25, s51, -4096\n" \
"buffer_load_dwordx4 v[248:251], v0, %0, s25 offen offset:0 sc1\n" \
"s_memrealtime s[28:29]\n" \
"s_waitcnt vmcnt(0) lgkmcnt(0)\n" \
"s_sub_u32 s28, s28, s20\n" \
"s_cmp_gt_u32 s28, 20000000\n" \
"s_cbranch_scc1 .Ll525_%=\n" \
"v_bitop3_b32 v12, v248, s24, 1 bitop3:0x28\n" \
"v_cmp_ne_u32 vcc, 0, v12\n" \
"s_cbranch_vccnz .Ll524_%=\n" \
"s_branch .Ll525_%=\n" \
".Ll526_%=:\n" \
"s_add_u32 s25, s51, 4096\n" \
"buffer_load_dwordx4 v[252:255], v0, %0, s25 offen offset:2048 sc1\n" \
"s_memrealtime s[28:29]\n" \
"s_waitcnt vmcnt(0) lgkmcnt(0)\n" \
"s_sub_u32 s28, s28, s20\n" \
"s_cmp_gt_u32 s28, 20000000\n" \
"s_cbranch_scc1 .Ll527_%=\n" \
"v_bitop3_b32 v12, v252, s24, 1 bitop3:0x28\n" \
"v_cmp_ne_u32 vcc, 0, v12\n" \
"s_cbranch_vccnz .Ll526_%=\n" \
"s_branch .Ll527_%=\n" \
".Ll528_%=:\n" \
"s_add_u32 s25, s52, -4096\n" \
"buffer_load_dwordx4 v[216:219], v0, %0, s25 offen offset:0 sc1\n" \
"s_memrealtime s[28:29]\n" \
"s_waitcnt vmcnt(0) lgkmcnt(0)\n" \
"s_sub_u32 s28, s28, s20\n" \
"s_cmp_gt_u32 s28, 20000000\n" \
"s_cbranch_scc1 .Ll529_%=\n" \
"v_bitop3_b32 v12, v216, s24, 1 bitop3:0x28\n" \
"v_cmp_ne_u32 vcc, 0, v12\n" \
"s_cbranch_vccnz .Ll528_%=\n" \
"s_branch .Ll529_%=\n" \
".Ll530_%=:\n" \
"s_add_u32 s25, s52, 4096\n" \
"buffer_load_dwordx4 v[220:223], v0, %0, s25 offen offset:2048 sc1\n" \
"s_memrealtime s[28:29]\n" \
"s_waitcnt vmcnt(0) lgkmcnt(0)\n" \
"s_sub_u32 s28, s28, s20\n" \
"s_cmp_gt_u32 s28, 20000000\n" \
"s_cbranch_scc1 .Ll531_%=\n" \
"v_bitop3_b32 v12, v220, s24, 1 bitop3:0x28\n" \
"v_cmp_ne_u32 vcc, 0, v12\n" \
"s_cbranch_vccnz .Ll530_%=\n" \
"s_branch .Ll531_%=\n" \
".Ll532_%=:\n" \
"s_add_u32 s25, s53, -4096\n" \
"buffer_load_dwordx4 v[224:227], v0, %0, s25 offen offset:0 sc1\n" \
"s_memrealtime s[28:29]\n" \
"s_waitcnt vmcnt(0) lgkmcnt(0)\n" \
"s_sub_u32 s28, s28, s20\n" \
"s_cmp_gt_u32 s28, 20000000\n" \
"s_cbranch_scc1 .Ll533_%=\n" \
"v_bitop3_b32 v12, v224, s24, 1 bitop3:0x28\n" \
"v_cmp_ne_u32 vcc, 0, v12\n" \
"s_cbranch_vccnz .Ll532_%=\n" \
"s_branch .Ll533_%=\n" \
".Ll534_%=:\n" \
"s_add_u32 s25, s53, 4096\n" \
"buffer_load_dwordx4 v[228:231], v0, %0, s25 offen offset:2048 sc1\n" \
"s_memrealtime s[28:29]\n" \
"s_waitcnt vmcnt(0) lgkmcnt(0)\n" \
"s_sub_u32 s28, s28, s20\n" \
"s_cmp_gt_u32 s28, 20000000\n" \
"s_cbranch_scc1 .Ll535_%=\n" \
"v_bitop3_b32 v12, v228, s24, 1 bitop3:0x28\n" \
"v_cmp_ne_u32 vcc, 0, v12\n" \
"s_cbranch_vccnz .Ll534_%=\n" \
"s_branch .Ll535_%=\n" \
".Ll536_%=:\n" \
"s_add_u32 s25, s54, -4096\n" \
"buffer_load_dwordx4 v[232:235], v0, %0, s25 offen offset:0 sc1\n" \
"s_memrealtime s[28:29]\n" \
"s_waitcnt vmcnt(0) lgkmcnt(0)\n" \
"s_sub_u32 s28, s28, s20\n" \
"s_cmp_gt_u32 s28, 20000000\n" \
"s_cbranch_scc1 .Ll537_%=\n" \
"v_bitop3_b32 v12, v232, s24, 1 bitop3:0x28\n" \
"v_cmp_ne_u32 vcc, 0, v12\n" \
"s_cbranch_vccnz .Ll536_%=\n" \
"s_branch .Ll537_%=\n" \
".Ll538_%=:\n" \
"s_add_u32 s25, s54, 4096\n" \
"buffer_load_dwordx4 v[236:239], v0, %0, s25 offen offset:2048 sc1\n" \
"s_memrealtime s[28:29]\n" \
"s_waitcnt vmcnt(0) lgkmcnt(0)\n" \
"s_sub_u32 s28, s28, s20\n" \
"s_cmp_gt_u32 s28, 20000000\n" \
"s_cbranch_scc1 .Ll539_%=\n" \
"v_bitop3_b32 v12, v236, s24, 1 bitop3:0x28\n" \
"v_cmp_ne_u32 vcc, 0, v12\n" \
"s_cbranch_vccnz .Ll538_%=\n" \
"s_branch .Ll539_%=\n" \
".Ll540_%=:\n" \
"s_add_u32 s25, s51, 8192\n" \
"buffer_load_dwordx4 v[240:243], v0, %0, s25 offen offset:0 sc1\n" \
"s_memrealtime s[28:29]\n" \
"s_waitcnt vmcnt(0) lgkmcnt(0)\n" \
"s_sub_u32 s28, s28, s20\n" \
"s_cmp_gt_u32 s28, 20000000\n" \
"s_cbranch_scc1 .Ll541_%=\n" \
"v_bitop3_b32 v12, v240, s24, 1 bitop3:0x28\n" \
"v_cmp_ne_u32 vcc, 0, v12\n" \
"s_cbranch_vccnz .Ll540_%=\n" \
"s_branch .Ll541_%=\n" \
".Ll542_%=:\n" \
"s_add_u32 s25, s51, 8192\n" \
"buffer_load_dwordx4 v[244:247], v0, %0, s25 offen offset:2048 sc1\n" \
"s_memrealtime s[28:29]\n" \
"s_waitcnt vmcnt(0) lgkmcnt(0)\n" \
"s_sub_u32 s28, s28, s20\n" \
"s_cmp_gt_u32 s28, 20000000\n" \
"s_cbranch_scc1 .Ll543_%=\n" \
"v_bitop3_b32 v12, v244, s24, 1 bitop3:0x28\n" \
"v_cmp_ne_u32 vcc, 0, v12\n" \
"s_cbranch_vccnz .Ll542_%=\n" \
"s_branch .Ll543_%=\n" \
".Ll544_%=:\n" \
"s_add_u32 s25, s52, 8192\n" \
"buffer_load_dwordx4 v[248:251], v0, %0, s25 offen offset:0 sc1\n" \
"s_memrealtime s[28:29]\n" \
"s_waitcnt vmcnt(0) lgkmcnt(0)\n" \
"s_sub_u32 s28, s28, s20\n" \
"s_cmp_gt_u32 s28, 20000000\n" \
"s_cbranch_scc1 .Ll545_%=\n" \
"v_bitop3_b32 v12, v248, s24, 1 bitop3:0x28\n" \
"v_cmp_ne_u32 vcc, 0, v12\n" \
"s_cbranch_vccnz .Ll544_%=\n" \
"s_branch .Ll545_%=\n" \
".Ll546_%=:\n" \
"s_add_u32 s25, s52, 8192\n" \
"buffer_load_dwordx4 v[252:255], v0, %0, s25 offen offset:2048 sc1\n" \
"s_memrealtime s[28:29]\n" \
"s_waitcnt vmcnt(0) lgkmcnt(0)\n" \
"s_sub_u32 s28, s28, s20\n" \
"s_cmp_gt_u32 s28, 20000000\n" \
"s_cbranch_scc1 .Ll547_%=\n" \
"v_bitop3_b32 v12, v252, s24, 1 bitop3:0x28\n" \
"v_cmp_ne_u32 vcc, 0, v12\n" \
"s_cbranch_vccnz .Ll546_%=\n" \
"s_branch .Ll547_%=\n" \
".Ll548_%=:\n" \
"s_add_u32 s25, s53, 8192\n" \
"buffer_load_dwordx4 v[216:219], v0, %0, s25 offen offset:0 sc1\n" \
"s_memrealtime s[28:29]\n" \
"s_waitcnt vmcnt(0) lgkmcnt(0)\n" \
"s_sub_u32 s28, s28, s20\n" \
"s_cmp_gt_u32 s28, 20000000\n" \
"s_cbranch_scc1 .Ll549_%=\n" \
"v_bitop3_b32 v12, v216, s24, 1 bitop3:0x28\n" \
"v_cmp_ne_u32 vcc, 0, v12\n" \
"s_cbranch_vccnz .Ll548_%=\n" \
"s_branch .Ll549_%=\n" \
".Ll550_%=:\n" \
"s_add_u32 s25, s53, 8192\n" \
"buffer_load_dwordx4 v[220:223], v0, %0, s25 offen offset:2048 sc1\n" \
"s_memrealtime s[28:29]\n" \
"s_waitcnt vmcnt(0) lgkmcnt(0)\n" \
"s_sub_u32 s28, s28, s20\n" \
"s_cmp_gt_u32 s28, 20000000\n" \
"s_cbranch_scc1 .Ll551_%=\n" \
"v_bitop3_b32 v12, v220, s24, 1 bitop3:0x28\n" \
"v_cmp_ne_u32 vcc, 0, v12\n" \
"s_cbranch_vccnz .Ll550_%=\n" \
"s_branch .Ll551_%=\n" \
".Ll552_%=:\n" \
"s_add_u32 s25, s54, 8192\n" \
"buffer_load_dwordx4 v[224:227], v0, %0, s25 offen offset:0 sc1\n" \
"s_memrealtime s[28:29]\n" \
"s_waitcnt vmcnt(0) lgkmcnt(0)\n" \
"s_sub_u32 s28, s28, s20\n" \
"s_cmp_gt_u32 s28, 20000000\n" \
"s_cbranch_scc1 .Ll553_%=\n" \
"v_bitop3_b32 v12, v224, s24, 1 bitop3:0x28\n" \
"v_cmp_ne_u32 vcc, 0, v12\n" \
"s_cbranch_vccnz .Ll552_%=\n" \
"s_branch .Ll553_%=\n" \
".Ll554_%=:\n" \
"s_add_u32 s25, s54, 8192\n" \
"buffer_load_dwordx4 v[228:231], v0, %0, s25 offen offset:2048 sc1\n" \
"s_memrealtime s[28:29]\n" \
"s_waitcnt vmcnt(0) lgkmcnt(0)\n" \
"s_sub_u32 s28, s28, s20\n" \
"s_cmp_gt_u32 s28, 20000000\n" \
"s_cbranch_scc1 .Ll555_%=\n" \
"v_bitop3_b32 v12, v228, s24, 1 bitop3:0x28\n" \
"v_cmp_ne_u32 vcc, 0, v12\n" \
"s_cbranch_vccnz .Ll554_%=\n" \
"s_branch .Ll555_%=\n" \
".Ll558_%=:\n" \
"buffer_load_dword v29, v30, %0, s26 offen sc1\n" \
"s_memrealtime s[28:29]\n" \
"s_waitcnt vmcnt(0) lgkmcnt(0)\n" \
"s_sub_u32 s28, s28, s20\n" \
"s_cmp_gt_u32 s28, 20000000\n" \
"s_cbranch_scc1 .Ll559_%=\n" \
"v_cmp_ne_u32 vcc, %5, v29\n" \
"s_cbranch_vccnz .Ll558_%=\n" \
"s_branch .Ll559_%=\n" \
".Lend_%=:\n" \
"s_or_b32 s25, %2, %3\n" \
"s_or_b32 s25, s25, %4\n" \
"s_cbranch_scc1 .Lfin_%=\n" \
"s_mov_b32 s25, 27770880\n" \
"v_mov_b32 v1, %5\n" \
"s_mov_b64 exec, 1\n" \
"buffer_store_dword v1, off, %0, s25 sc1\n" \
"s_mov_b64 exec, -1\n" \
".Lfin_%=:\n"
#define MAIN_CLOB "v0", "v1", "v2", "v3", "v4", "v5", "v6", "v7", "v8", "v9", "v10", "v11", "v12", "v13", "v14", "v15", "v20", "v21", "v22", "v23", "v24", "v25", "v26", "v27", "v28", "v29", "v30", "v31", "v56", "v57", "v58", "v59", "v60", "v61", "v62", "v63", "v96", "v97", "v98", "v99", "v100", "v101", "v102", "v103", "v104", "v105", "v106", "v107", "v108", "v109", "v110", "v111", "v112", "v113", "v114", "v115", "v116", "v117", "v118", "v119", "v120", "v121", "v122", "v123", "v124", "v125", "v126", "v127", "v128", "v129", "v130", "v131", "v132", "v133", "v134", "v135", "v136", "v137", "v138", "v139", "v140", "v141", "v142", "v143", "v144", "v145", "v146", "v147", "v148", "v149", "v150", "v151", "v152", "v153", "v154", "v155", "v156", "v157", "v158", "v159", "v160", "v161", "v162", "v163", "v164", "v165", "v166", "v167", "v168", "v169", "v170", "v171", "v172", "v173", "v174", "v175", "v176", "v177", "v178", "v179", "v180", "v181", "v182", "v183", "v184", "v185", "v186", "v187", "v188", "v189", "v190", "v191", "v192", "v193", "v194", "v195", "v196", "v197", "v198", "v199", "v200", "v201", "v202", "v203", "v204", "v205", "v206", "v207", "v208", "v209", "v210", "v211", "v212", "v213", "v214", "v215", "v216", "v217", "v218", "v219", "v220", "v221", "v222", "v223", "v224", "v225", "v226", "v227", "v228", "v229", "v230", "v231", "v232", "v233", "v234", "v235", "v236", "v237", "v238", "v239", "v240", "v241", "v242", "v243", "v244", "v245", "v246", "v247", "v248", "v249", "v250", "v251", "v252", "v253", "v254", "v255", "s20", "s21", "s22", "s23", "s24", "s25", "s26", "s27", "s28", "s29", "s30", "s31", "s34", "s35", "s36", "s37", "s38", "s39", "s40", "s41", "s42", "s43", "s44", "s45", "s46", "s47", "s48", "s49", "s50", "s51", "s52", "s53", "s54", "s55", "s56", "s57", "s58", "s59", "s60", "s61", "s62", "s63", "s64", "s65", "s66", "s67", "s68", "s69", "s70", "s71", "s72", "s73", "s74", "s75", "s76", "s77", "s78", "s79", "s80", "s81", "s82", "s83", "s84", "s85", "s86", "s87", "s88", "s89", "s90", "s91", "s92", "s93", "s94", "s95", "s96", "s97", "s98", "s99", "vcc", "scc", "memory"

DI float bf_lo(u32 x) { return __uint_as_float(x << 16); }
DI float bf_hi(u32 x) { return __uint_as_float(x & 0xffff0000u); }
DI v4u lds_r128(u32 a) { return *(const LAS v4u*)(uintptr_t)a; }
#define SB() __builtin_amdgcn_sched_barrier(0)
#define PIN(x) asm volatile("" : "+v"(x))
#define BAR() do { SB(); asm volatile("s_barrier" ::: "memory"); SB(); } while (0)
#define WAIT_VM(n) asm volatile("s_waitcnt vmcnt(%0)" :: "n"(n) : "memory")
#define LBAR(n, lg) asm volatile("s_waitcnt vmcnt(%0) lgkmcnt(0)\n\ts_barrier" :: "n"((n) < 63 ? (n) : 63) : "memory")
DI void keep(v4u v) { asm volatile("s_nop 1" :: "v"(v)); }
template <int A = 16> DI void st128(rsrc_t r, v4u v, u32 vo, u32 so) { __builtin_amdgcn_raw_buffer_store_b128(v, r, vo, so, A); keep(v); }
template <int A = 16> DI void st64(rsrc_t r, v2u v, u32 vo, u32 so) { __builtin_amdgcn_raw_buffer_store_b64(v, r, vo, so, A); }
template <int A = 16> DI void st32(rsrc_t r, u32 v, u32 vo, u32 so) { __builtin_amdgcn_raw_buffer_store_b32(v, r, vo, so, A); }
DI float sum32(float v) { auto r = __builtin_amdgcn_permlane32_swap(__float_as_uint(v), __float_as_uint(v), false, false); return __uint_as_float(r[0]) + __uint_as_float(r[1]); }
DI float sum16(float v) { auto r = __builtin_amdgcn_permlane16_swap(__float_as_uint(v), __float_as_uint(v), false, false); return __uint_as_float(r[0]) + __uint_as_float(r[1]); }
DI u32 sopq(u32 v) { v = __builtin_amdgcn_readfirstlane(v); asm volatile("" : "+s"(v)); return v; }
constexpr u32 OOB = 0x80000000u;

#define STAMP(i) do {} while (0)
struct Cx {
  u32 t3;
  rsrc_t rs;
  rsrc_t ro;
  int g, j, c, rh, w;
  u32 lane;
  u32 tag;
  v4u p4[5], p5[5]; u32 cs, a4[6], a5[7], ra[5];
  rsrc_t rw;
};
template <int N> DI void post(Cx& cx, u32 so) {
  u32 t;
  asm volatile("v_mov_b32 %0, %1\n\ts_waitcnt vmcnt(%4)\n\ts_mov_b64 exec, 1\n\tbuffer_store_dword %0, off, %2, %3 sc1\n\ts_mov_b64 exec, -1"
               : "=&v"(t) : "s"(cx.tag), "s"(cx.rs), "s"(so), "n"(N) : "memory");
}

typedef short s4v __attribute__((ext_vector_type(4)));
DI v2u lds_tr(u32 a) { return __builtin_bit_cast(v2u, __builtin_amdgcn_ds_read_tr16_b64_v4i16((LAS s4v*)(uintptr_t)a)); }
DI v4u lds_tr2(u32 a, u32 d) { const v2u x = lds_tr(a), y = lds_tr(a + d); return (v4u){x.x, x.y, y.x, y.y}; }
DI void ld_raw1(rsrc_t r, u32 vo, u32 src, u32 ld) {
  asm volatile("s_mov_b32 m0, %1\n\ts_nop 0\n\tbuffer_load_dwordx4 %0, %2, %3 offen " NT1 " lds" :: "v"(vo), "s"(ld), "s"(r), "s"(src) : "memory");
}
DI void ld_raw(rsrc_t r, u32 vo, u32 src, u32 ld) {
  asm volatile("s_mov_b32 m0, %1\n\ts_nop 0\n\tbuffer_load_dwordx4 %0, %2, %3 offen " RAWNT " lds" :: "v"(vo), "s"(ld), "s"(r), "s"(src) : "memory");
}
DI float sum8(v4u p) { return ((bf_lo(p.x) + bf_hi(p.x)) + (bf_lo(p.y) + bf_hi(p.y))) + ((bf_lo(p.z) + bf_hi(p.z)) + (bf_lo(p.w) + bf_hi(p.w))); }
DI u32 mk_tag(u32 e) { return e * 0x9E3779B1u + 0x7F4A7C15u; }
DI rsrc_t mk_rs(const void* p, u32 n) { return __builtin_amdgcn_make_buffer_rsrc((void*)p, 0, n, 0x00020000); }
DI u32 tile0(u32 j) { const u32 a = 7 * (j & 7); return 54 * (j >> 3) + (a < 48 ? a : 48); }
constexpr u32 L_RX = L_MISC, L_R1 = (RAW ? 2 : 14) * UNIT, L_R4 = (LW == 5 ? 4 : RAW ? 6 : 10) * UNIT, L_R5 = (LW == 5 ? 6 : RAW ? 13 : 12) * UNIT;
DI u32 w35_ra(Cx& cx) {
  const u32 lane = cx.lane, w = cx.w & 3, p4 = lane & 3;
  return L_R5 + (AL ? (cx.c & 1) * 32 : 0) + (w >> 1) * 1024 + ((lane >> 2) & 3) * 256 + ((w & 1) ? 128 + p4 * 8 : (lane >> 4) * 32 + (p4 >> 1) * 8 + (p4 & 1) * 16);
}
DI void w35_rd(Cx& cx, v4u (&p)[5]) {
  const u32 a = w35_ra(cx);
#pragma unroll
  for (int k = 0; k < 5; ++k) p[k] = lds_tr2(a + k * 4096, 2048);
}
DI void w35_ad(Cx& cx, u32 (&a)[7]) {
  const u32 lane = cx.lane, w = cx.w & 3, g = cx.g, jj = g & 7;
  const u32 no = (w & 1) && lane >= 16 ? OOB : 0u;
  const u32 d = (w >> 1) * 256 + ((w & 1) ? 4096 + lane * 16 : ((lane >> 5) * 2 + ((lane >> 3) & 1)) * 1024 + ((lane >> 4) & 1) * 128 + (lane & 7) * 16);
  const u32 s5 = O_W5 + (cx.c * 27 + 16 * cx.rh + (jj >> 1)) * UNIT + (g >> 3) * 5120 + (jj & 1) * 512, s3 = O_W3 + cx.c * 10 * UNIT + (g >> 3) * 5120;
  const u32 ne = no | (w >= 2 || !cx.rh ? OOB : 0u), se = s3 + (jj >> 1) * 256 + (jj & 1) * 8;
  a[0] = (d + s3 + ((cx.rh ? 0 : 5) + (jj >> 1)) * UNIT + (jj & 1) * 512) | no;
  a[1] = (d + se + 9 * UNIT) | ne; a[2] = (d + se + 4 * UNIT) | ne;
  a[3] = (d + s5 + 12 * UNIT) | no | (cx.rh ? OOB : 0u);
  a[4] = (d + s5) | no; a[5] = (d + s5 + 4 * UNIT) | no;
  a[6] = (d + s5 + 8 * UNIT) | no | (cx.rh && jj >= 6 ? OOB : 0u);
}
DI void w35_st(Cx& cx, v4u (&p)[5]) {
  u32 a[7]; w35_ad(cx, a);
  st128(cx.rs, p[4], a[0], 0);
  if (cx.rh) { st64(cx.rs, (v2u){p[3].x, p[3].y}, a[1], 0); st64(cx.rs, (v2u){p[3].z, p[3].w}, a[2], 0); }
  else st128(cx.rs, p[3], a[3], 0);
  st128(cx.rs, p[0], a[4], 0); st128(cx.rs, p[1], a[5], 0); st128(cx.rs, p[2], a[6], 0);
}
DI void w4_ld(Cx& cx, const void* wg, const void* wu) {
    const u32 lane = cx.lane, w = cx.w & 3;
    const rsrc_t r = mk_rs(w >> 1 ? wu : wg, 640 * 3456);
    const u32 vo = AL || (lane & 15) < 14 ? (lane >> 4) * 3456 + (lane & 15) * 16 : OOB;
    const u32 so = (40 * cx.g + 20 * (w & 1)) * 3456 + (tile0(cx.j) & (AL ? ~1u : ~0u)) * 32;
#pragma unroll
    for (int k = 0; k < 5; ++k) ld_raw(r, vo, so + k * 4 * 3456, L_R4 + w * 5120 + k * 1024);
  }
DI void start_c(Cx& cx, u32 ep, const void* wo, const void* wd, const void* wg, const void* wu) {
  const u32 lane = cx.lane, gr = lane >> 3, tok = lane & 7, w = cx.w;
  SB();
  STAMP(22);
  auto late = [&]() {
    const rsrc_t rd = mk_rs(wd, 1728 * 1280), ro = mk_rs(wo, 640 * 1280), r3 = mk_rs(cx.rh ? wo : wd, cx.rh ? 640 * 1280 : 1728 * 1280);
    const u32 vo = (lane & 15) < (AL ? 12 : 10) ? (lane >> 4) * 1280 + (lane & 15) * 16 : OOB, c0 = cx.c * 160 - (AL ? (cx.c & 1) * 32 : 0);
    const u32 sd = (16 * (tile0(cx.g) + 4 * cx.rh) + 4 * w) * 1280 + c0, so = 40 * cx.g * 1280 + c0, ld = L_R5 + w * 1024;
    ld_raw(ro, vo, so + (20 * cx.rh + 4 * w) * 1280, ld + 4 * 4096);
    ld_raw(r3, vo, cx.rh ? so + (16 + (w >> 1) * 20) * 1280 : sd + 3 * 20480, ld + 3 * 4096);
    ld_raw(rd, vo, sd, ld); ld_raw(rd, vo, sd + 20480, ld + 4096);
    ld_raw(rd, vo, sd + (cx.rh && (cx.g & 7) >= 6 ? 0u : 2 * 20480u), ld + 2 * 4096);
  };
  if (LW == 0) late();
  SB();
  if (RAW) {
    asm volatile(XR_ASM :: "v"((lane & 31) * 1280 + (lane >> 5) * 16), "s"(cx.rw), "s"((cx.g * 128 + w * 32) * 1280) : XR_CLOB);
    SB();
  }
  STAMP(23);
  const u32 tb = 8 * cx.g + (cx.j >> 1);
  u32 dvo = gr * 256 + tok * 16, dv2 = dvo + (w < 2 ? 0u : OOB), ra = L_RX + w * 1024 + tok * 128 + ((gr ^ tok) & 7) * 16, svo = gr < 2 ? gr * 512 + tok * 8 : OOB;
  const u32 dso = sopq(O_X + tb * 20480 + w * 2048 + (cx.j & 1) * 128), sso = sopq(O_ST + tb * 1024 + w * 128 + (cx.j & 1) * 64);
  const u32 fo = sopq(F_1 + (cx.g * 16 + cx.j) * 128);
  asm volatile("" : "+v"(dvo), "+v"(dv2), "+v"(ra), "+v"(svo));
  STAMP(24);
  BAR();
  u32 es; asm volatile("s_waitcnt vmcnt(%2)\n\tv_readfirstlane_b32 %0, %1" : "=s"(es) : "v"(ep), "n"((LW ? 0 : 5) + LXR + PFL) : "memory");
  if (LW == 2) { late(); SB(); }
  STAMP(25);
  v4u v[3];
  v[0] = lds_r128(ra); v[1] = lds_r128(ra + 4096); v[2] = lds_r128(ra + 8192 - (w >> 1) * 2048);
  float s1 = 0, s2 = 0;
#pragma unroll
  for (int k = 0; k < 3; ++k) {
    v[k].x &= ~1u;
    st128(cx.rs, v[k], k == 2 ? dv2 : dvo, dso + k * 8192);
    SB();
  }
  cx.tag = mk_tag(es);
  if (w >= 2) v[2] = (v4u){0u, 0u, 0u, 0u};
#pragma unroll
  for (int k = 0; k < 3; ++k)
#pragma unroll
    for (int e = 0; e < 4; ++e) { const float a = bf_lo(v[k][e]), b = bf_hi(v[k][e]); s1 += a + b; s2 += a * a + b * b; }
  s1 += __uint_as_float(__builtin_amdgcn_mov_dpp(__float_as_uint(s1), 0x128, 15, 15, true));
  s2 += __uint_as_float(__builtin_amdgcn_mov_dpp(__float_as_uint(s2), 0x128, 15, 15, true));
  s1 = sum32(sum16(s1)); s2 = sum32(sum16(s2));
  st64(cx.rs, gr ? (v2u){0u, 0u} : (v2u){__float_as_uint(s1) & ~1u, __float_as_uint(s2) & ~1u}, svo, sso);
  SB();
  if (A2 & 1) {
    u32 a; asm volatile("v_mov_b32 %0, -1" : "=v"(a)); const v4u ones = {a, a, a, a};
    const u32 so = O_OM + cx.g * 131072 + cx.j * 8192 + w * 2048;
    st128(cx.rs, ones, lane * 16, so); st128(cx.rs, ones, lane * 16 + 1024, so);
    st128(cx.rs, ones, lane < 32 ? lane * 16 : OOB, O_OE + cx.g * 32768 + cx.j * 2048 + w * 512);
    st128(cx.rs, ones, lane < 48 ? lane * 16 : OOB, O_H + 48 * 512 + (cx.g * 16 + cx.j) * 27648 + w * 768);
    SB();
  }
  if (A2 & 2) {
    u32 a; asm volatile("v_mov_b32 %0, -1" : "=v"(a)); const v4u ones = {a, a, a, a};
    const u32 so = O_H + (cx.g * 16 + cx.j) * 27648 + cx.w * 3072;
#pragma unroll
    for (int p = 0; p < 3; ++p) st128(cx.rs, ones, lane * 16 + p * 1024, so);
    SB();
  }
  if (LW == 4 || (E5 & 4)) BAR();
  if (LW == 1 || LW == 4 || (E5 & 1)) { late(); SB(); }
  if (E5 & 2) { w4_ld(cx, wg, wu); SB(); }
  STAMP(26);
  WAIT_VM((A2 & 1) * 4 + (A2 & 2) / 2 * 3 + (LW == 1 || LW == 4 || (E5 & 1) ? 5 : 0) + (E5 & 2 ? 5 : 0));
  STAMP(27);
  BAR();
  if (w == 0) post<63>(cx, fo);
  STAMP(28);
  if (LW == 3) { late(); SB(); }
  if (LW == 5) { if (!(E5 & 1)) late(); if (!(E5 & 2)) w4_ld(cx, wg, wu); SB(); }
  if (LW == 1 || LW == 4) { if (w == 0) WAIT_VM(1); else WAIT_VM(0); BAR(); }
  if (LW == 3) { WAIT_VM(0); BAR(); }
  if (!RAW && LW != 5) { v4u p[5]; w35_rd(cx, p); w35_st(cx, p); SB(); }
#pragma unroll
  for (int k = 0; k < 5; ++k) asm volatile("" : "=v"(cx.p4[k]), "=v"(cx.p5[k]), "=v"(cx.a4[k]), "=v"(cx.a5[k]), "=v"(cx.ra[k]));
  asm volatile("" : "=v"(cx.cs), "=v"(cx.a4[5]), "=v"(cx.a5[5]), "=v"(cx.a5[6]));
  STAMP(29);
}
DI void start_l(Cx& cx, u32 ep, const void* wg, const void* wu) {
  const u32 lane = cx.lane, n = lane & 31, h = lane >> 5, w = cx.w & 3;
  SB();
  STAMP(22);
  auto late = [&]() { w4_ld(cx, wg, wu); };
  if (LW == 0) late();
  SB();
#pragma unroll
  for (int m = 0; m < 5; ++m) { const u32 i = 64 * m + lane, r = (i * 13108) >> 16; cx.ra[m] = !RAW ? 0 : r * 3840 + (i - 5 * r) * 16 + (w & 1) * 64 * 3840 + cx.j * 80; }
  if (RAW) {
    constexpr u32 pr[5][4] = RAW_PRE;
#pragma unroll
    for (int k = 0; k < 5; ++k) {
      const u32 so = pr[k][0] + (w >> 1) * (pr[k][2] - pr[k][0]), ld = pr[k][1] + (w >> 1) * (pr[k][3] - pr[k][1]) + (w & 1) * 5120;
#pragma unroll
      for (int m = 0; m < 5; ++m)
        asm volatile("s_mov_b32 m0, %1\n\ts_nop 0\n\tbuffer_load_dwordx4 %0, %2, %3 offen lds" :: "v"(cx.ra[m]), "s"(ld + m * 1024), "s"(cx.rw), "s"(so) : "memory");
    }
    SB();
  }
  STAMP(23);
  const u32 q4 = (lane >> 2) & 3, p4 = lane & 3, hd = (lane >> 4) & 1, e0 = 5 * (cx.g & 1), G = cx.g >> 1;
  u32 ra = L_R1 + h * 2048 + q4 * 256 + (w == 2 ? (hd ? 0 : (p4 >> 1) * 80 + 80) + (p4 & 1) * 40 + 32 : (w == 3 ? 0 : w * 80 + 80) + hd * 40 + p4 * 8);
  u32 dv[3];
#pragma unroll
  for (int k = 0; k < 3; ++k) { const u32 e = e0 + 2 * k + h;
    dv[k] = (w != 2 || n < 24) && !(k == 2 && h) ? ((32 - 8 * (e >> 1) + G) * 2 + (e & 1)) * (w == 2 ? 384 : 512) + n * 16 : OOB; }
  u32 cvo = h ? OOB : n * 4, r2 = ra + 8192 - h * 2048;
  const u32 so1 = sopq(O_W1 + (cx.j * 15 + (w == 3 ? 11 : 4 * w)) * UNIT), cso = sopq(O_C1 + (cx.j * 16 + cx.g) * 512 + w * 128);
  u32 t4 = L_R4 + hd * 10240 + q4 * 256 + (2 * w + h + (AL ? tile0(cx.j) & 1 : 0)) * 32 + p4 * 8;
  asm volatile("" : "+v"(ra), "+v"(dv[0]), "+v"(dv[1]), "+v"(dv[2]), "+v"(cvo), "+v"(r2), "+v"(t4));
  SB();
  STAMP(24);
  LBAR((LW ? 0 : 5) + (RAW ? 25 : 0) + PFL, 0);
  if (LW == 2) { late(); SB(); }
  u32 es; asm volatile("v_readfirstlane_b32 %0, %1" : "=s"(es) : "v"(ep) : "memory");
  STAMP(25);
  {
    const v4u p0 = lds_tr2(ra, 1024), p1 = lds_tr2(ra + 4096, 1024), p2 = lds_tr2(r2, 1024);
    st128(cx.rs, p0, dv[0], so1); st128(cx.rs, p1, dv[1], so1); st128(cx.rs, p2, dv[2], so1);
    SB();
    cx.tag = mk_tag(es);
    st32(cx.rs, __float_as_uint(sum32(sum8(p0) + sum8(p1) + (h ? 0.f : sum8(p2)))), cvo, cso);
  }
  SB();
  if (A2 & 1) {
    u32 a; asm volatile("v_mov_b32 %0, -1" : "=v"(a)); const v4u ones = {a, a, a, a};
    const u32 so = O_KV + ((4 * cx.g + w) * 16 + cx.j) * 5120;
#pragma unroll
    for (int p = 0; p < 5; ++p) st128(cx.rs, ones, lane * 16 + (p & 3) * 1024, so + (p >> 2) * 4096);
    SB();
  }
  if (A2 & 2) {
    u32 a; asm volatile("v_mov_b32 %0, -1" : "=v"(a)); const v4u ones = {a, a, a, a};
    const u32 so = O_H + (cx.g * 16 + cx.j) * 27648 + cx.w * 3072;
#pragma unroll
    for (int p = 0; p < 3; ++p) st128(cx.rs, ones, lane * 16 + p * 1024, so);
    SB();
  }
  if (LW == 4 || (E5 & 4)) BAR();
  if (LW == 1 || LW == 4) { late(); SB(); }
  STAMP(26);
  LBAR((A2 & 1) * 5 + (A2 & 2) / 2 * 3 + (LW == 1 || LW == 4 ? 5 : 0), 0);
  STAMP(28);
  if (LW == 3) { late(); SB(); }
  if (LW == 1 || LW == 3 || LW == 4) LBAR(0, 0);
  {
    const u32 no = w == 3 && h ? OOB : 0u, vo = h * 4 * UNIT + n * 16 + O_W4 + (cx.j * 7 + 2 * w) * 4 * UNIT + (32 + G) * 1024;
    cx.a4[5] = lane * 4 + O_C4 + (cx.j * 16 + cx.g) * 1024 + w * 256;
#pragma unroll
    for (int k = 0; k < 5; ++k) { const u32 e = e0 + k; cx.a4[k] = (vo - (e >> 1) * 8192 + (e & 1) * 512) | no; }
    w35_ad(cx, cx.a5);
    if (LW == 5) {
      cx.ra[0] = t4; cx.ra[1] = w35_ra(cx); cx.cs = 0;
#pragma unroll
      for (int k = 0; k < 5; ++k) asm volatile("" : "=v"(cx.p4[k]), "=v"(cx.p5[k]));
    } else {
      float cs = 0;
#pragma unroll
      for (int k = 0; k < 5; ++k) cx.p4[k] = lds_tr2(t4 + k * 2048, 1024);
#pragma unroll
      for (int k = 0; k < 5; ++k) if (!RAW) st128(cx.rs, cx.p4[k], cx.a4[k], 0);
      SB();
#pragma unroll
      for (int k = 0; k < 5; ++k) cs += sum8(cx.p4[k]);
      cx.cs = __float_as_uint(cs);
      if (!RAW) st32(cx.rs, cx.cs, cx.a4[5], 0);
      if (RAW) w35_rd(cx, cx.p5); else for (int k = 0; k < 5; ++k) asm volatile("" : "=v"(cx.p5[k]));
    }
  }
  SB();
  STAMP(29);
}

__global__ void __launch_bounds__(512, 2) k_main(const void* x, const void* wqkv, const void* wo, const void* wg, const void* wu, const void* wd, char* ws, char* out) {
  Cx cx;
  cx.rs = mk_rs(ws, WS_USED);
  cx.ro = mk_rs(out, 2048 * 640 * 2);
  asm volatile("" : "+s"(cx.rs));
  const int b = blockIdx.x, xcd = b & 7, bi = b >> 3;
  cx.g = (xcd >> 1) * 4 + (bi >> 3); cx.j = (xcd & 1) * 8 + (bi & 7);
  cx.c = (cx.j >> 3) * 4 + (cx.j & 3); cx.rh = (cx.j >> 2) & 1;
  cx.w = __builtin_amdgcn_readfirstlane(threadIdx.x >> 6);
  cx.lane = threadIdx.x & 63; cx.t3 = 0;
  STAMP(21);
  auto pull = [&]() {
    unsigned long long pc; u32 len;
    asm volatile("s_getpc_b64 %0\n.Lk_first:\n\ts_mov_b32 %1, .Lk_last-.Lk_first" : "=s"(pc), "=s"(len));
    asm volatile("s_mov_b32 m0, %2\n\ts_nop 0\n\tbuffer_load_dword %0, %1, 0 offen lds"
                 :: "v"((bi * 16 + cx.w * 2 + (cx.lane & 1)) * 128), "s"(mk_rs((const void*)pc, len)), "s"(PFL ? 9 * UNIT + cx.w * 256 : (cx.w < 4 ? L_RX : L_R1) + (cx.w & 3) * 1024) : "memory");
  };
  if (!PFL) pull();
  cx.rw = mk_rs(cx.w < 4 ? x : wqkv, cx.w < 4 ? 2048 * 1280 : 640 * 3840);
  u32 ep;
  {
    const u32 lane = cx.lane, gr = lane >> 3, p = lane & 15, w = cx.w & 3; const bool cw = cx.w < 4;
    u32 va = gr * 1280 + ((lane ^ gr) & 7) * 16, vb = p < 15 ? (lane >> 4) * 3840 + p * 16 + ((p * 13) >> 6) * 1200 : OOB;
    asm volatile("" : "+v"(va), "+v"(vb));
    const u32 vo = cw ? va : vb, u = cw ? 128u : 4u * 3840u, ld = (cw ? L_RX : L_R1);
    const u32 so = cx.g * (cw ? 128u * 1280u : 40u * 3840u) + cx.j * (cw ? 8u * 1280u : 80u) + w * u;
    ld_raw1(cx.rw, vo, so, ld + w * 1024); ld_raw1(cx.rw, vo, so + 4 * u, ld + 4096 + w * 1024);
    ld_raw1(cx.rw, vo, so + (8 - (w >> 1) * 2) * u, ld + 8192 + (w & 1) * 1024);
    asm volatile("buffer_load_dword %0, off, %1, %2 sc1" : "=v"(ep) : "s"(cx.rs), "s"(F_EP) : "memory");
    SB();
  }
  if (PFL) pull();
  SB();
  if (cx.w < 4) start_c(cx, ep, wo, wd, wg, wu); else start_l(cx, ep, wg, wu);
  cx.tag = sopq(cx.tag);
  asm volatile(MAIN_ASM :: "s"(cx.rs), "s"(cx.ro), "s"(cx.g), "s"(cx.j), "s"(cx.w), "s"(cx.tag)
    , "s"(cx.rw), "{v[32:35]}"(cx.p4[0]), "{v[36:39]}"(cx.p4[1]), "{v[40:43]}"(cx.p4[2]), "{v[44:47]}"(cx.p4[3]), "{v[48:51]}"(cx.p4[4])
    , "{v[64:67]}"(cx.p5[0]), "{v[68:71]}"(cx.p5[1]), "{v[72:75]}"(cx.p5[2]), "{v[76:79]}"(cx.p5[3]), "{v[80:83]}"(cx.p5[4])
    , "{v[52:55]}"((v4u){cx.a4[0], cx.a4[1], cx.a4[2], cx.a4[3]}), "{v[84:87]}"((v4u){cx.a4[4], cx.a4[5], cx.cs, cx.a5[0]})
    , "{v[88:91]}"((v4u){cx.a5[1], cx.a5[2], cx.a5[3], cx.a5[4]}), "{v[92:95]}"((v4u){cx.a5[5], cx.a5[6], cx.ra[4], cx.ra[4]})
    , "{v[16:19]}"((v4u){cx.ra[0], cx.ra[1], cx.ra[2], cx.ra[3]})
    : MAIN_CLOB);
}

extern "C" void kernel_launch(void* const* d_in, const int* in_sizes, int n_in, void* d_out, int out_size, void* d_ws, size_t ws_size, hipStream_t stream) {
  (void)in_sizes; (void)n_in; (void)out_size; (void)ws_size;
  (void)hipFuncSetAttribute((const void*)k_main, hipFuncAttributeMaxDynamicSharedMemorySize, LDS_BYTES);
  k_main<<<256, 512, LDS_BYTES, stream>>>(d_in[0], d_in[1], d_in[2], d_in[4], d_in[5], d_in[6], (char*)d_ws, (char*)d_out);
}
```
